```python
import torch

def _softmax(x, axis=-1):
    return torch.softmax(x, dim=axis)

def _gelu(x):
    return torch.nn.functional.gelu(x, approximate='tanh')

def _silu(x):
    return torch.nn.functional.silu(x)

def _sigmoid(x):
    return torch.sigmoid(x)

def _layernorm(x, axis=-1, eps=1e-05):
    m = x.mean(dim=axis, keepdim=True)
    v = x.var(dim=axis, keepdim=True, unbiased=False)
    return (x - m) / torch.sqrt(v + eps)

def _scatter_add(tgt, idx, src, axis=0):
    return tgt.clone().index_add_(axis, idx, src)

def _segment_sum(src, seg, n):
    out = torch.zeros((n,) + src.shape[1:], dtype=src.dtype)
    return out.index_add_(0, seg, src)

def _pad(x, pw):
    flat = tuple((v for pair in reversed(pw) for v in pair))
    return torch.nn.functional.pad(x, flat)
kernel_name = 'gated_mix__memory_aug_recurrent_mem_tr__dense_activation_squared_relu__prompt'

def setup_inputs():

    _w = lambda a: (lambda t: t.to(torch.bfloat16) if t.is_floating_point() else t)(torch.as_tensor(a))

    def _A_setup():
        B, S, Dm = (1, 2048, 512)
        H, Dh = (32, 128)
        M, seg = (8, 32)
        seg = min(seg, S)
        S = S // seg * seg
        return {'x': _w((lambda *s: torch.randn(*s, dtype=torch.bfloat16) * 0.05)(B, S, Dm)), 'mem0': _w((lambda *s: torch.randn(*s, dtype=torch.bfloat16) * 0.8)(B, M, Dm)), 'Wq': _w((lambda *s: torch.randn(*s, dtype=torch.bfloat16) * 1.6)(Dm, H, Dh) * Dm ** (-0.5)), 'Wk': _w((lambda *s: torch.randn(*s, dtype=torch.bfloat16) * 1.6)(Dm, H, Dh) * Dm ** (-0.5)), 'Wv': _w((lambda *s: torch.randn(*s, dtype=torch.bfloat16) * 0.8)(Dm, H, Dh) * Dm ** (-0.5)), 'Wo': _w((lambda *s: torch.randn(*s, dtype=torch.bfloat16) * 0.8)(H, Dh, Dm) * (H * Dh) ** (-0.5)), 'seg_len': seg}
    _A_in = _A_setup()

    def _B_setup():
        B, S, D, F = (1, 2048, 512, 1792)
        return {'x': _w((lambda *s: torch.randn(*s, dtype=torch.bfloat16) * 0.05)(B, S, D)), 'w1': _w((lambda *s: torch.randn(*s, dtype=torch.bfloat16) * 0.05)(D, F)), 'b1': _w((lambda *s: torch.randn(*s, dtype=torch.bfloat16) * 0.1)(F)), 'w2': _w((lambda *s: torch.randn(*s, dtype=torch.bfloat16) * 0.05)(F, D)), 'b2': _w((lambda *s: torch.randn(*s, dtype=torch.bfloat16) * 0.05)(D))}
    _B_in = _B_setup()
    return {'A_x': _A_in['x'], 'A_mem0': _A_in['mem0'], 'A_Wq': _A_in['Wq'], 'A_Wk': _A_in['Wk'], 'A_Wv': _A_in['Wv'], 'A_Wo': _A_in['Wo'], 'A_seg_len': _A_in['seg_len'], 'B_x': _B_in['x'], 'B_w1': _B_in['w1'], 'B_b1': _B_in['b1'], 'B_w2': _B_in['w2'], 'B_b2': _B_in['b2'], 'gate_w': _w((lambda *s: torch.randn(*s, dtype=torch.bfloat16) * 0.05)(512) * 16.0)}

def reference(A_x, A_mem0, A_Wq, A_Wk, A_Wv, A_Wo, A_seg_len, B_x, B_w1, B_b1, B_w2, B_b2, gate_w):

    def _A_ref(x, mem0, Wq, Wk, Wv, Wo, seg_len):
        B, S, Dm = x.shape
        H, Dh = (Wq.shape[1], Wq.shape[2])
        M = mem0.shape[1]
        scale = Dh ** (-0.5)
        n_seg = S // seg_len
        L = M + seg_len
        idx = torch.arange(L)
        mask = (idx[:, None] < M) | (idx[:, None] >= idx[None, :])
        mem = mem0
        outs = []
        for s in range(n_seg):
            xs = x[:, s * seg_len:(s + 1) * seg_len, :].clone()
            h = torch.concatenate([mem, xs], axis=1)
            q = torch.einsum('btd,dhe->bhte', h, Wq)
            k = torch.einsum('btd,dhe->bhte', h, Wk)
            v = torch.einsum('btd,dhe->bhte', h, Wv)
            scores = torch.einsum('bhqe,bhke->bhqk', q, k) * scale
            scores = torch.where(mask[None, None], scores, -1000000000.0)
            p = _softmax(scores, axis=-1)
            o = torch.einsum('bhqk,bhke->bhqe', p, v)
            o = torch.einsum('bhte,hed->btd', o, Wo)
            mem = o[:, :M, :].clone()
            outs.append(o[:, M:, :])
        return torch.concatenate(outs, axis=1)

    def _B_ref(x, w1, b1, w2, b2):
        h = torch.einsum('bsd,df->bsf', x, w1) + b1
        zero = torch.zeros_like(h)
        r = torch.where(h > zero, h, zero)
        h = r * r
        out = torch.einsum('bsf,fd->bsd', h, w2) + b2
        return out
    x = A_x
    g = _sigmoid((x * gate_w).sum(-1, keepdims=True))
    a = _A_ref(x, A_mem0, A_Wq, A_Wk, A_Wv, A_Wo, A_seg_len)
    b = _B_ref(x, B_w1, B_b1, B_w2, B_b2)
    return g * a + (1 - g) * b
```

```cpp
#include <hip/hip_runtime.h>
#include <hip/hip_bf16.h>

typedef unsigned u32;
typedef unsigned long long u64;
typedef __bf16 bf16;
typedef bf16 bf16x2 __attribute__((ext_vector_type(2)));
typedef bf16 bf16x8 __attribute__((ext_vector_type(8)));
typedef bf16 bf16x16 __attribute__((ext_vector_type(16)));
typedef short s16x4 __attribute__((ext_vector_type(4)));
typedef short s16x8 __attribute__((ext_vector_type(8)));
typedef short s16x16 __attribute__((ext_vector_type(16)));
typedef float f32x2 __attribute__((ext_vector_type(2)));
typedef float f32x4 __attribute__((ext_vector_type(4)));
typedef u32 u32x2 __attribute__((ext_vector_type(2)));
typedef u32 u32x4 __attribute__((ext_vector_type(4)));
typedef __amdgpu_buffer_rsrc_t rsrc_t;
#define LAS __attribute__((address_space(3)))
typedef LAS char* lptr;
#define DI __device__ __forceinline__

constexpr u32 MB = 1u << 20;
constexpr u32 OFF_PART = 0;
constexpr u32 OFF_MEM = 2 * MB;
constexpr u32 OFF_CTRL = 3 * MB;
constexpr u32 C_SEL = OFF_CTRL;
constexpr u32 C_KV = OFF_CTRL + 1024;
constexpr u32 C_KM = C_KV + 8192;
constexpr u32 C_OC = C_KM + 16384;
constexpr u32 C_HC = C_OC + 8192;
constexpr u32 C_END = C_HC + 8192;
constexpr u32 OFF_PROBE = OFF_CTRL + 512 * 1024;
constexpr u32 OFF_QX = 4 * MB, OFF_KX = 20 * MB, OFF_VX = 36 * MB;
constexpr u32 OFF_KM = 52 * MB, OFF_VM = 60 * MB, OFF_OF = 68 * MB, OFF_HF = 84 * MB, OFF_G = 92 * MB;
constexpr u32 WS_BYTES = 93 * MB;
constexpr u32 INIT_LO = OFF_MEM + 63 * 16384;
constexpr int LDS_BYTES = 144 * 1024;
constexpr int SPIN_MAX = 1 << 15;
constexpr int AUX = 16;

DI u32x4 ld16(rsrc_t r, u32 v, u32 s) { return __builtin_amdgcn_raw_buffer_load_b128(r, v, s, AUX); }
DI u32x2 ld8(rsrc_t r, u32 v, u32 s) { return __builtin_amdgcn_raw_buffer_load_b64(r, v, s, AUX); }
DI u32 ld4(rsrc_t r, u32 v, u32 s) { return __builtin_amdgcn_raw_buffer_load_b32(r, v, s, AUX); }
DI void st16(rsrc_t r, u32x4 x, u32 v, u32 s) {
  __builtin_amdgcn_raw_buffer_store_b128(x, r, v, s, AUX);
  asm volatile("s_nop 1" ::"v"(x) : "memory");
}
DI void st8(rsrc_t r, u32x2 x, u32 v, u32 s) { __builtin_amdgcn_raw_buffer_store_b64(x, r, v, s, AUX); }
DI void st4(rsrc_t r, u32 x, u32 v, u32 s) { __builtin_amdgcn_raw_buffer_store_b32(x, r, v, s, AUX); }
#define MEMBAR() asm volatile("" ::: "memory")
#define WAITVM(n) asm volatile("s_waitcnt vmcnt(" #n ")" ::: "memory")
#define WAITVM0() do { MEMBAR(); __builtin_amdgcn_s_waitcnt(0x0F70); MEMBAR(); } while (0)
#define BAR() asm volatile("s_waitcnt lgkmcnt(0)\n\ts_barrier" ::: "memory")
#define SCHED() __builtin_amdgcn_sched_barrier(0)
#define PINA(x) asm volatile("" : "+a"(x))
#define PINV(x) asm volatile("" : "+v"(x))
constexpr u32 OOB = 0x80000000u;

DI void dma16(const char* sbase, u32 voff, u32 ldsdst) {
  u32 keep;
  asm volatile("s_mov_b32 %0, m0\n\ts_mov_b32 m0, %3\n\ts_nop 0\n\tglobal_load_lds_dwordx4 %1, %2\n\ts_mov_b32 m0, %0"
               : "=&s"(keep) : "v"(voff), "s"(sbase), "s"(ldsdst) : "memory");
}
DI s16x4 trr(lptr p) { return __builtin_amdgcn_ds_read_tr16_b64_v4i16((LAS s16x4*)p); }
DI bf16x8 cat2(s16x4 a, s16x4 b) { return __builtin_bit_cast(bf16x8, __builtin_shufflevector(a, b, 0, 1, 2, 3, 4, 5, 6, 7)); }
DI bf16x16 cat4(s16x4 a, s16x4 b, s16x4 c, s16x4 d) {
  s16x8 x = __builtin_shufflevector(a, b, 0, 1, 2, 3, 4, 5, 6, 7), y = __builtin_shufflevector(c, d, 0, 1, 2, 3, 4, 5, 6, 7);
  return __builtin_bit_cast(bf16x16, __builtin_shufflevector(x, y, 0, 1, 2, 3, 4, 5, 6, 7, 8, 9, 10, 11, 12, 13, 14, 15));
}
DI u32 pk(float lo, float hi) { f32x2 v = {lo, hi}; return __builtin_bit_cast(u32, __builtin_convertvector(v, bf16x2)); }
DI float rbf(float x) { return (float)(bf16)x; }
DI bf16x8 as8(u32x4 x) { return __builtin_bit_cast(bf16x8, x); }
DI f32x4 mfma32(bf16x8 a, bf16x8 b, f32x4 c) { return __builtin_amdgcn_mfma_f32_16x16x32_bf16(a, b, c, 0, 0, 0); }
DI f32x4 mfma16(u32x2 a, u32x2 b, f32x4 c) {
  return __builtin_amdgcn_mfma_f32_16x16x16bf16_1k(__builtin_bit_cast(s16x4, a), __builtin_bit_cast(s16x4, b), c, 0, 0, 0);
}
DI f32x4 smf(u32x4 a, bf16x16 b, f32x4 c, int idx) { return __builtin_amdgcn_smfmac_f32_16x16x64_bf16(as8(a), b, c, idx, 0, 0); }
DI float comb(float x, float y) {
  auto r = __builtin_amdgcn_permlane32_swap(__builtin_bit_cast(u32, x), __builtin_bit_cast(u32, y), false, false);
  return __builtin_bit_cast(float, (u32)r[0]) + __builtin_bit_cast(float, (u32)r[1]);
}
DI float maxall(float m) {
  auto r = __builtin_amdgcn_permlane32_swap(__builtin_bit_cast(u32, m), __builtin_bit_cast(u32, m), false, false);
  m = fmaxf(__builtin_bit_cast(float, (u32)r[0]), __builtin_bit_cast(float, (u32)r[1]));
  auto q = __builtin_amdgcn_permlane16_swap(__builtin_bit_cast(u32, m), __builtin_bit_cast(u32, m), false, false);
  return fmaxf(__builtin_bit_cast(float, (u32)q[0]), __builtin_bit_cast(float, (u32)q[1]));
}
DI bool anyl(bool p) { return __builtin_amdgcn_ballot_w64(p) != 0; }
template <bool EQ>
DI void waitword(rsrc_t r, u32 s, u32 val, bool& dead, int sleep) {
  int n = 0;
#pragma unroll 1
  while (!dead) {
    MEMBAR();
    u32 x = __builtin_amdgcn_readfirstlane(ld4(r, 0, s));
    if (EQ ? (x == val) : (x != val)) break;
    if (++n > SPIN_MAX) dead = true;
    for (int i = 0; i < sleep; i++) __builtin_amdgcn_s_sleep(4);
  }
  MEMBAR();
}
constexpr float NEG = -3.0e38f;
constexpr float SCL = 0.08838834764831845f * 1.4426950408889634f;

DI void chain_role(lptr smem, rsrc_t ws, int h, int a, int w, int l, const char* Wq, const char* Wk, const char* Wv,
                   const char* Wo, const bf16* mem0) {
  const int gb = l >> 4, q = (l >> 2) & 3, p = l & 3, c = l & 15;
  const u32 lds0 = (u32)(size_t)smem;
  bf16x16 Bq[2][8], Bk[2][8], Bv[8], Bo[8];
  bool dead = false;
  {
    const u32 bx = (u32)((l & 15) ^ ((l >> 5) << 1));
    const u32 rowoff = (u32)(64 * w + (l >> 4)) * 8192u + (u32)h * 256u;
    auto issueQK = [&](const char* W, int half, int buf) {
#pragma unroll
      for (int it = 0; it < 16; it++) {
        const u32 cit = (u32)((((it >> 3) & 1) << 3) | ((it & 1) << 2));
        dma16(W, rowoff + (u32)(half * 256 + it * 4) * 8192u + ((bx ^ cit) << 4), lds0 + buf * 65536 + (w * 16 + it) * 1024);
      }
    };
    auto issueV = [&](int buf) {
      const u32 bv = (u32)((l & 7) ^ ((l >> 5) << 1));
      const u32 ro = (u32)(128 * w + ((l >> 3) & 1) * 32 + (l >> 4)) * 8192u + (u32)h * 256u + (u32)a * 128u;
#pragma unroll
      for (int it = 0; it < 16; it++)
        dma16(Wv, ro + (u32)((it >> 3) * 64 + (it & 7) * 4) * 8192u + ((bv ^ (u32)((it & 1) << 2)) << 4),
              lds0 + buf * 65536 + (w * 16 + it) * 1024);
    };
    auto issueO = [&](int buf) {
      const u32 ro = (u32)(h * 128 + a * 64 + w * 16) * 1024u;
#pragma unroll
      for (int it = 0; it < 16; it++) {
        const u32 f = (u32)((((it >> 3) & 1) << 2) | (((it >> 2) & 1) << 1) | (it & 1));
        dma16(Wo, ro + (u32)it * 1024u + (((u32)l ^ (f << 1)) << 4), lds0 + buf * 65536 + (w * 16 + it) * 1024);
      }
    };
    const int rowl = 32 * (q & 1) + 8 * (gb >> 1) + 4 * (q >> 1) + 2 * (gb & 1);
    const int fq = ((q & 1) << 2) | ((q >> 1) << 1) | (gb & 1);
    auto readQK = [&](bf16x16(&B)[2][8], int half, int buf) {
#pragma unroll
      for (int y = 0; y < 2; y++) {
        lptr b = smem + buf * 65536 + rowl * 256 + (((4 * w + 2 * y + (p >> 1)) ^ (fq << 1)) << 4) + (p & 1) * 8;
#pragma unroll
        for (int t = 0; t < 4; t++)
        {
          B[y][half * 4 + t] = cat4(trr(b + t * 16384), trr(b + t * 16384 + 256), trr(b + t * 16384 + 4096), trr(b + t * 16384 + 4352));
          PINA(B[y][half * 4 + t]);
        }
      }
    };
    issueQK(Wq, 0, 0);
    issueQK(Wq, 1, 1);
    WAITVM(16); BAR(); readQK(Bq, 0, 0); BAR(); issueQK(Wk, 0, 0);
    WAITVM(16); BAR(); readQK(Bq, 1, 1); BAR(); issueQK(Wk, 1, 1);
    WAITVM(16); BAR(); readQK(Bk, 0, 0); BAR(); issueV(0);
    WAITVM(16); BAR(); readQK(Bk, 1, 1); BAR(); issueO(1);
    WAITVM(16); BAR();
    {
      const int f2 = ((q >> 1) << 1) | (gb & 1);
      lptr b = smem + (8 * (gb >> 1) + 4 * (q >> 1) + 2 * (gb & 1)) * 256 + (q & 1) * 128 + (((2 * w + (p >> 1)) ^ (f2 << 1)) << 4) + (p & 1) * 8;
#pragma unroll
      for (int t = 0; t < 8; t++)
      {
        Bv[t] = cat4(trr(b + t * 8192), trr(b + t * 8192 + 256), trr(b + t * 8192 + 4096), trr(b + t * 8192 + 4352));
        PINV(Bv[t]);
      }
    }
    WAITVM0(); BAR();
    {
      const int fo = ((q >> 1) << 2) | ((gb & 1) << 1) | (q & 1);
      const int er = 16 * (gb >> 1) + 8 * (q >> 1) + 4 * (gb & 1) + (q & 1);
#pragma unroll
      for (int T = 0; T < 8; T++) {
        lptr b = smem + 65536 + er * 1024 + (((16 * w + 2 * T + (p >> 1)) ^ (fo << 1)) << 4) + (p & 1) * 8;
        Bo[T] = cat4(trr(b), trr(b + 2048), trr(b + 32768), trr(b + 32768 + 2048));
        PINV(Bo[T]);
      }
    }
    BAR();
  }
  waitword<false>(ws, C_SEL, 0xFFFFFFFFu, dead, 1);
  const u32 selw = __builtin_amdgcn_readfirstlane(ld4(ws, 0, C_SEL));
  const u32 sel0 = (selw & 1u) * 8192u, sel1 = ((selw >> 1) & 1u) * 8192u;
  const int ci = 2 * h + a;
  const int rho = 2 * ci + (w & 1);
  const u32 rblk = (u32)(rho >> 1) * 16384u + (u32)(rho & 1) * 4096u + sel0;
  const u32 rgat = rblk + (u32)(4 * gb + (c & 3)) * 64u + (u32)(c >> 2) * 16u;
  const int rt = rho >> 4, ry = (rho >> 3) & 1, rq4 = (rho >> 2) & 1, ru = rho & 3;
  const u32 rpiece = (u32)rt * 1024u + (u32)(16 * (2 * ry + (ru >> 1)) + 8 * (ru & 1) + 4 * rq4) * 16u;
  const u32 rpub = rpiece + (u32)(c & 3) * 16u + (u32)gb * 4u;
  const u32 pst = (u32)(w * 16 + (l >> 3)) * 16384u + (u32)((l >> 2) & 1) * 4096u + sel0 + (u32)ci * 64u + (u32)(l & 3) * 16u;
  const u32x4 ones = {~0u, ~0u, ~0u, ~0u};
  const u32 rrst = rblk + (u32)(w >> 1) * 2048u + (u32)l * 16u;
  const u32 rpub4 = c < 4 ? rpub : OOB;
  const u32 lane0 = l == 0 ? 0u : OOB;
  if (w < 2) {
#pragma unroll
    for (int k = 0; k < 8; k++) st16(ws, ones, rblk + (u32)l * 16u + (k & 3) * 1024u, OFF_PART + (k >> 2) * MB);
#pragma unroll
    for (int k = 0; k < 4; k++) {
      const int s = 1 + k * 16 + (l >> 2);
      if (s < 64) st16(ws, ones, (u32)(63 - s) * 16384u + rpiece + (u32)(l & 3) * 16u, OFF_MEM + sel1);
    }
    const int d0 = 64 * rt + 16 * ry + 4 * ru + gb, r0 = 4 * rq4 + (c & 3);
    const unsigned short* m0 = (const unsigned short*)mem0;
    const u32 mv = (u32)m0[r0 * 512 + d0] | ((u32)m0[r0 * 512 + d0 + 32] << 16);
    WAITVM0();
    if (c < 4) st4(ws, mv, rpub, OFF_MEM + sel1 + 63u * 16384u);
  }
  const u32 kvlane = (u32)l * 16u;
  lptr Lq = smem, Lk = smem + 2048, Lo = smem + 4096, Lx = smem + 8192, Lv = smem + 16384;
  u32 flg = 0xFFFFFFFFu;
  auto fetch_kv = [&](int sn) {
    if (__builtin_amdgcn_readfirstlane(flg) == 0xFFFFFFFFu) waitword<false>(ws, C_KV + (u32)(h * 64 + sn) * 4u, 0xFFFFFFFFu, dead, 1);
    const u32 so = (u32)(sn * 32 + h) * 8192u;
    u32x4 t[6];
#pragma unroll
    for (int i = 0; i < 4; i++) t[i] = ld16(ws, kvlane + i * 1024u, OFF_KX + so + (u32)(w - 2) * 4096u);
#pragma unroll
    for (int i = 0; i < 2; i++) t[4 + i] = ld16(ws, kvlane + i * 1024u, OFF_VX + so + (u32)(4 * a + 2 * (w - 2)) * 1024u);
    flg = ld4(ws, 0, C_KV + (u32)(h * 64 + sn + 1) * 4u);
#pragma unroll
    for (int i = 0; i < 4; i++) *(LAS u32x4*)(Lx + (w - 2) * 4096 + i * 1024 + l * 16) = t[i];
#pragma unroll
    for (int i = 0; i < 2; i++) *(LAS u32x4*)(Lv + (w - 2) * 2048 + i * 1024 + l * 16) = t[4 + i];
  };
  if (w >= 2) fetch_kv(0);
  WAITVM0();
  BAR();
  const u32 kmoff = (l >> 5) == a ? kvlane + (u32)w * 1024u : OOB;
  const u32 flgoff = w == 2 ? lane0 : OOB;
  const int idx = (l & 8) ? 0xEEEE : 0x4444;
  const int wq = (32 * w + 8 * (c >> 2) + 4 * (gb >> 1) + (c & 3)) * 16 + (gb & 1) * 8;
  const int rq = (8 * gb + q) * 16 + (p & 1) * 8;
  u32x4 selB;
  {
    const int j = c & 7;
    const u32 one = (j & 1) ? 0x3F800000u : 0x00003F80u;
    selB[0] = (j >> 1) == 0 ? one : 0u; selB[1] = (j >> 1) == 1 ? one : 0u;
    selB[2] = (j >> 1) == 2 ? one : 0u; selB[3] = (j >> 1) == 3 ? one : 0u;
  }
  u32x4 one8 = {0x3F803F80u, 0x3F803F80u, 0x3F803F80u, 0x3F803F80u};
  u32x2 one4 = {0x3F803F80u, 0x3F803F80u};
  f32x4 cM = gb >= 2 ? (f32x4){NEG, NEG, NEG, NEG} : (f32x4){0.f, 0.f, 0.f, 0.f};
  PINV(one8); PINV(one4); PINV(selB); PINV(cM);
  const f32x4 z4 = {0.f, 0.f, 0.f, 0.f};
#define QKSTEP(t)                                                                                                   \
  asm volatile("v_smfmac_f32_16x16x64_bf16 %0, %4, %5, %9\n\tv_smfmac_f32_16x16x64_bf16 %1, %4, %6, %9\n\t"         \
               "v_smfmac_f32_16x16x64_bf16 %2, %4, %7, %9\n\tv_smfmac_f32_16x16x64_bf16 %3, %4, %8, %9"             \
               : "+v"(q0), "+v"(q1), "+v"(k0), "+v"(k1)                                                             \
               : "v"(A[t]), "a"(Bq[0][t]), "a"(Bq[1][t]), "a"(Bk[0][t]), "a"(Bk[1][t]), "v"(idx))
#define VSTEP(t) asm volatile("v_smfmac_f32_16x16x64_bf16 %0, %1, %2, %3" : "+v"(v0) : "v"(A[t]), "v"(Bv[t]), "v"(idx))

#define NSTEP 64
  for (int s = 0; s < NSTEP; s++) {
    const u32 mslot = OFF_MEM + sel1 + (u32)(63 - s) * 16384u;
    u32x4 A[8];
#pragma unroll
    for (int t = 0; t < 8; t++) { A[t] = ld16(ws, kvlane + (t & 3) * 1024u, mslot + (t >> 2) * 4096u); SCHED(); }
    st16(ws, ones, rrst, OFF_PART + ((s + 1) & 1) * MB);
    st16(ws, ones, rrst + 1024u, OFF_PART + ((s + 1) & 1) * MB);
    st4(ws, 1u, s > 0 ? flgoff : OOB, C_KM - 8u + (u32)(h * 128 + a) * 4u + (u32)s * 8u);
    f32x4 q0 = z4, q1 = z4, k0 = z4, k1 = z4, v0 = z4;
    asm volatile("s_nop 1" : "+v"(q0), "+v"(q1), "+v"(k0), "+v"(k1), "+v"(v0));
    SCHED();
    bool bad;
#define QKALL()                                                                                                      \
  {                                                                                                                  \
    QKSTEP(0); QKSTEP(1); QKSTEP(2); QKSTEP(3); QKSTEP(4); QKSTEP(5); QKSTEP(6); QKSTEP(7);                           \
    asm volatile("s_nop 5" : "+v"(q0), "+v"(q1));                                                                    \
    const float c0 = comb(q0[0], q1[0]), c1 = comb(q0[1], q1[1]), c2 = comb(q0[2], q1[2]), c3 = comb(q0[3], q1[3]);   \
    u32x2 a2 = {pk(c0, c1), pk(c2, c3)};                                                                             \
    asm volatile("" : "+v"(k0), "+v"(k1), "+v"(a2));                                                                 \
    *(LAS u32x2*)(Lq + wq) = a2;                                                                                     \
    u32x2 b2 = {pk(comb(k0[0], k1[0]), comb(k0[1], k1[1])), pk(comb(k0[2], k1[2]), comb(k0[3], k1[3]))};             \
    *(LAS u32x2*)(Lk + wq) = b2;                                                                                     \
    SCHED();                                                                                                         \
    bad = anyl(__builtin_isunordered(c0, c1)) || anyl(__builtin_isunordered(c2, c3));                                \
  }
    QKALL();
    if (__builtin_expect(bad && !dead, 0)) {
      int n = 0;
#pragma unroll 1
      do {
        MEMBAR();
#pragma unroll
        for (int t = 0; t < 8; t++) A[t] = ld16(ws, kvlane + (t & 3) * 1024u, mslot + (t >> 2) * 4096u);
        q0 = z4; q1 = z4; k0 = z4; k1 = z4;
        WAITVM0();
        asm volatile("s_nop 1" : "+v"(q0), "+v"(q1), "+v"(k0), "+v"(k1));
        QKALL();
        if (++n > SPIN_MAX) dead = true;
      } while (bad && !dead);
    }
    BAR();
    bf16x8 Fq[4], Fk[4];
    u32x4 X0[4], X1[4];
#pragma unroll
    for (int ks = 0; ks < 4; ks++) {
      Fk[ks] = cat2(trr(Lk + rq + ks * 512), trr(Lk + rq + ks * 512 + 64));
      Fq[ks] = cat2(trr(Lq + rq + ks * 512), trr(Lq + rq + ks * 512 + 64));
    }
    SCHED();
    VSTEP(0); VSTEP(1); VSTEP(2); VSTEP(3);
#pragma unroll
    for (int ks = 0; ks < 4; ks++) X0[ks] = *(LAS u32x4*)(Lx + ks * 1024 + l * 16);
    SCHED();
    VSTEP(4); VSTEP(5); VSTEP(6); VSTEP(7);
#pragma unroll
    for (int ks = 0; ks < 4; ks++) X1[ks] = *(LAS u32x4*)(Lx + 4096 + ks * 1024 + l * 16);
    const u32x4 vx = *(LAS u32x4*)(Lv + w * 1024 + l * 16);
    const bf16x8 kf = cat2(trr(Lk + rq + w * 512), trr(Lk + rq + w * 512 + 64));
    SCHED();
    f32x4 sM, s0, s1;
#define STILE(D, F, C0)                                                                                              \
  asm volatile("v_mfma_f32_16x16x32_bf16 %0, %1, %5, " C0 "\n\tv_mfma_f32_16x16x32_bf16 %0, %2, %6, %0\n\t"               \
               "v_mfma_f32_16x16x32_bf16 %0, %3, %7, %0\n\tv_mfma_f32_16x16x32_bf16 %0, %4, %8, %0"                  \
               : "=&v"(D) : "v"(F[0]), "v"(F[1]), "v"(F[2]), "v"(F[3]), "v"(Fq[0]), "v"(Fq[1]), "v"(Fq[2]), "v"(Fq[3]), "v"(cM))
    STILE(sM, Fk, "%9");
    asm volatile("s_nop 3" : "+v"(v0));
    const u32x2 vm = {pk(comb(v0[0], v0[0]), comb(v0[1], v0[1])), pk(comb(v0[2], v0[2]), comb(v0[3], v0[3]))};
    STILE(s0, X0, "0");
    {
      const u32 so = (u32)(s * 32 + h) * 4096u;
      st8(ws, vm, (u32)l * 8u + (u32)(4 * a + w) * 512u, OFF_VM + so);
      st16(ws, __builtin_bit_cast(u32x4, kf), kmoff, OFF_KM + so);
    }
    if (s == 63) break;
    float m, mt;
    asm volatile(
        "v_mfma_f32_16x16x32_bf16 %0, %3, %7, 0\n\tv_max3_f32 %1, %11, %12, %13\n\t"
        "v_mfma_f32_16x16x32_bf16 %0, %4, %8, %0\n\tv_max_f32 %1, %1, %14\n\t"
        "v_mfma_f32_16x16x32_bf16 %0, %5, %9, %0\n\tv_mfma_f32_16x16x32_bf16 %0, %6, %10, %0\n\ts_nop 1\n\t"
        "v_max3_f32 %1, %1, %15, %16\n\tv_max3_f32 %1, %1, %17, %18\n\ts_nop 3"
        : "=&v"(s1), "=&v"(m), "=&v"(mt)
        : "v"(X1[0]), "v"(X1[1]), "v"(X1[2]), "v"(X1[3]), "v"(Fq[0]), "v"(Fq[1]), "v"(Fq[2]), "v"(Fq[3]), "v"(sM[0]), "v"(sM[1]),
          "v"(sM[2]), "v"(sM[3]), "v"(s0[0]), "v"(s0[1]), "v"(s0[2]), "v"(s0[3]));
    asm volatile(
        "v_max3_f32 %0, %0, %2, %3\n\tv_max3_f32 %0, %0, %4, %5\n\tv_mov_b32 %1, %0\n\ts_nop 1\n\t"
        "v_permlane32_swap_b32 %0, %1\n\tv_max_f32 %0, %0, %1\n\tv_mov_b32 %1, %0\n\ts_nop 1\n\t"
        "v_permlane16_swap_b32 %0, %1\n\tv_max_f32 %0, %0, %1"
        : "+v"(m), "+v"(mt) : "v"(s1[0]), "v"(s1[1]), "v"(s1[2]), "v"(s1[3]));
    m *= -SCL;
    float pM[4], p0[4], p1[4];
#pragma unroll
    for (int i = 0; i < 4; i++) pM[i] = __builtin_amdgcn_exp2f(__builtin_fmaf(sM[i], SCL, m));
    const u32x2 PM = {pk(pM[0], pM[1]), pk(pM[2], pM[3])};
    f32x4 sum, o;
    asm volatile("s_nop 1\n\tv_mfma_f32_16x16x16_bf16 %0, %3, %4, 0\n\tv_mfma_f32_16x16x16_bf16 %1, %5, %4, 0"
                 : "=&v"(sum), "=&v"(o), "+v"(m) : "v"(one4), "v"(PM), "v"(vm));
#pragma unroll
    for (int i = 0; i < 4; i++) {
      p0[i] = __builtin_amdgcn_exp2f(__builtin_fmaf(s0[i], SCL, m));
      p1[i] = __builtin_amdgcn_exp2f(__builtin_fmaf(s1[i], SCL, m));
    }
    const u32x4 PX = {pk(p0[0], p0[1]), pk(p0[2], p0[3]), pk(p1[0], p1[1]), pk(p1[2], p1[3])};
    asm volatile("s_nop 1\n\tv_mfma_f32_16x16x32_bf16 %0, %2, %3, %0\n\tv_mfma_f32_16x16x32_bf16 %1, %4, %3, %1\n\ts_nop 6"
                 : "+v"(sum), "+v"(o) : "v"(one8), "v"(PX), "v"(vx));
    float rs = __builtin_amdgcn_rcpf(sum[0]);
    asm volatile("s_nop 0" : "+v"(o), "+v"(rs));
    {
      u32x2 ob = {pk(o[0] * rs, o[1] * rs), pk(o[2] * rs, o[3] * rs)};
      *(LAS u32x2*)(Lo + c * 144 + w * 32 + gb * 8) = ob;
    }
    WAITVM(2);
    BAR();
    const u32x4 Ao = *(LAS u32x4*)(Lo + (c & 7) * 144 + gb * 32 + (c >> 3) * 16);
    SCHED();
    f32x4 D[8] = {z4, z4, z4, z4, z4, z4, z4, z4};
    asm volatile("s_nop 1" : "+v"(D[0]), "+v"(D[1]), "+v"(D[2]), "+v"(D[3]), "+v"(D[4]), "+v"(D[5]), "+v"(D[6]), "+v"(D[7]));
#define OSTEP(T)                                                                                                     \
  asm volatile("v_smfmac_f32_16x16x64_bf16 %0, %4, %5, %9\n\tv_smfmac_f32_16x16x64_bf16 %1, %4, %6, %9\n\t"          \
               "v_smfmac_f32_16x16x64_bf16 %2, %4, %7, %9\n\tv_smfmac_f32_16x16x64_bf16 %3, %4, %8, %9"              \
               : "+v"(D[T]), "+v"(D[T + 1]), "+v"(D[T + 2]), "+v"(D[T + 3])                                          \
               : "v"(Ao), "v"(Bo[T]), "v"(Bo[T + 1]), "v"(Bo[T + 2]), "v"(Bo[T + 3]), "v"(idx))
    OSTEP(0);
    OSTEP(4);
    asm volatile("s_nop 3" : "+v"(D[0]), "+v"(D[1]), "+v"(D[2]), "+v"(D[3]));
#pragma unroll
    for (int mm = 0; mm < 2; mm++) {
      u32x4 pv;
#pragma unroll
      for (int x = 0; x < 2; x++) {
        const f32x4 da = D[4 * mm + 2 * x], db = D[4 * mm + 2 * x + 1];
        pv[2 * x] = pk(comb(da[0], db[0]), comb(da[1], db[1]));
        pv[2 * x + 1] = pk(comb(da[2], db[2]), comb(da[3], db[3]));
      }
      st16(ws, pv, pst, OFF_PART + (s & 1) * MB + mm * 8 * 16384u);
      if (mm == 0) asm volatile("" : "+v"(D[4]), "+v"(D[5]), "+v"(D[6]), "+v"(D[7])::"memory");
    }
    WAITVM0();
    if (w < 2) {
      int m2 = 0;
      float tot;
      while (true) {
        u32x4 g[4];
#pragma unroll
        for (int k = 0; k < 4; k++) { g[k] = ld16(ws, rgat + k * 1024u, OFF_PART + (s & 1) * MB); SCHED(); }
        f32x4 acc;
        asm volatile("v_mfma_f32_16x16x32_bf16 %0, %1, %2, 0" : "=&v"(acc) : "v"(g[0]), "v"(selB));
        asm volatile("v_mfma_f32_16x16x32_bf16 %0, %1, %2, %0" : "+v"(acc) : "v"(g[1]), "v"(selB));
        asm volatile("v_mfma_f32_16x16x32_bf16 %0, %1, %2, %0" : "+v"(acc) : "v"(g[2]), "v"(selB));
        asm volatile("v_mfma_f32_16x16x32_bf16 %0, %1, %2, %0\n\ts_nop 7" : "+v"(acc) : "v"(g[3]), "v"(selB));
        tot = (acc[0] + acc[1]) + (acc[2] + acc[3]);
        if (__builtin_expect(!anyl(tot != tot) || dead, 1)) break;
        if (++m2 > SPIN_MAX) dead = true;
        MEMBAR();
      }
      const float hi = __builtin_bit_cast(float, __builtin_amdgcn_update_dpp(0, __builtin_bit_cast(int, tot), 0x104, 0xf, 0xf, true));
      st4(ws, pk(tot, hi), rpub4, mslot - 16384u);
      WAITVM0();
    } else if (s < 62) {
      __builtin_amdgcn_s_sleep(1);
      fetch_kv(s + 1);
    }
    BAR();
  }
  WAITVM0();
  BAR();
  if (w == 2 && l == 0) st4(ws, 1u, 0, C_KM + (u32)((h * 64 + 63) * 2 + a) * 4u);
}

DI void stageW(const char* W, u32 off0, u32 rs, u32 ldsbuf, int w, int l) {
  const u32 bx = (u32)((l & 15) ^ ((l >> 4) << 1));
  const u32 ro = off0 + (u32)(64 * w + (l >> 4)) * rs;
#pragma unroll
  for (int it = 0; it < 16; it++)
    dma16(W, ro + (u32)(it * 4) * rs + ((bx ^ (u32)(((it >> 1) & 1) << 3)) << 4), ldsbuf + (w * 16 + it) * 1024);
}
template <bool AG>
DI void readW(bf16x8 (&B)[2][16], int half, lptr buf, int w, int l) {
  const int gb = l >> 4, q = (l >> 2) & 3, p = l & 3;
  const int f = ((gb & 1) << 2) | q;
#pragma unroll
  for (int y = 0; y < 2; y++) {
    lptr b = buf + (8 * gb + q) * 256 + (((4 * w + 2 * y + (p >> 1)) ^ (f << 1)) << 4) + (p & 1) * 8;
#pragma unroll
    for (int k = 0; k < 8; k++) {
      B[y][half * 8 + k] = cat2(trr(b + k * 8192), trr(b + k * 8192 + 1024));
    }
  }
}
DI void stageX(const char* x, int s, u32 ldsbuf, int w, int l) {
  const u32 ro = (u32)(32 * s + 16 * (w >> 1) + (l & 15)) * 1024u + (u32)((w & 1) * 8) * 64u + (u32)(l >> 4) * 16u;
#pragma unroll
  for (int it = 0; it < 8; it++) dma16(x, ro + it * 64u, ldsbuf + (w * 8 + it) * 1024);
}

DI void qkv_role(lptr smem, rsrc_t ws, int h, int j, int w, int l, const char* x, const char* Wq, const char* Wk, const char* Wv) {
  const u32 lds0 = (u32)(size_t)smem;
  bf16x8 Bq[2][16], Bk[2][16], Bv[2][16];
  const u32 ho = (u32)h * 256u, hh = 256u * 8192u;
  stageW(Wk, ho, 8192u, lds0, w, l);
  stageW(Wk, ho + hh, 8192u, lds0 + 65536, w, l);
  WAITVM(16); BAR(); readW<true>(Bk, 0, smem, w, l); BAR(); stageW(Wv, ho, 8192u, lds0, w, l);
  WAITVM(16); BAR(); readW<true>(Bk, 1, smem + 65536, w, l); BAR(); stageW(Wv, ho + hh, 8192u, lds0 + 65536, w, l);
  WAITVM(16); BAR(); readW<false>(Bv, 0, smem, w, l); BAR(); stageW(Wq, ho, 8192u, lds0, w, l);
  WAITVM(16); BAR(); readW<false>(Bv, 1, smem + 65536, w, l); BAR(); stageW(Wq, ho + hh, 8192u, lds0 + 65536, w, l);
  WAITVM(16); BAR(); readW<true>(Bq, 0, smem, w, l); BAR(); stageX(x, j, lds0, w, l);
  WAITVM(8); BAR(); readW<true>(Bq, 1, smem + 65536, w, l);
  WAITVM(0); BAR();
  const f32x4 z4 = {0.f, 0.f, 0.f, 0.f};
  int buf = 0;
  for (int s = j; s < 64; s += 6) {
    if (s + 6 < 64) stageX(x, s + 6, lds0 + (buf ^ 1) * 32768, w, l);
    lptr xb = smem + buf * 32768 + l * 16;
    f32x4 aq[2][2], ak[2][2], av[2][2];
#pragma unroll
    for (int i = 0; i < 4; i++) { aq[i >> 1][i & 1] = z4; ak[i >> 1][i & 1] = z4; av[i >> 1][i & 1] = z4; }
    bf16x8 n0 = *(LAS bf16x8*)(xb), n1 = *(LAS bf16x8*)(xb + 16384);
#pragma unroll
    for (int ks = 0; ks < 16; ks++) {
      bf16x8 x0 = n0, x1 = n1;
      if (ks < 15) { n0 = *(LAS bf16x8*)(xb + (ks + 1) * 1024); n1 = *(LAS bf16x8*)(xb + 16384 + (ks + 1) * 1024); }
      asm volatile("" : "+v"(x0), "+v"(x1));
#pragma unroll
      for (int y = 0; y < 2; y++) {
        ak[0][y] = mfma32(Bk[y][ks], x0, ak[0][y]); ak[1][y] = mfma32(Bk[y][ks], x1, ak[1][y]);
        av[0][y] = mfma32(x0, Bv[y][ks], av[0][y]); av[1][y] = mfma32(x1, Bv[y][ks], av[1][y]);
        aq[0][y] = mfma32(Bq[y][ks], x0, aq[0][y]); aq[1][y] = mfma32(Bq[y][ks], x1, aq[1][y]);
      }
      asm volatile("" : "+a"(ak[0][0]), "+a"(ak[0][1]), "+a"(ak[1][0]), "+a"(ak[1][1]), "+a"(av[0][0]), "+a"(av[0][1]), "+a"(av[1][0]),
                   "+a"(av[1][1]), "+a"(aq[0][0]), "+a"(aq[0][1]), "+a"(aq[1][0]), "+a"(aq[1][1]));
    }
    const u32 so = (u32)(s * 32 + h) * 8192u;
#pragma unroll
    for (int t = 0; t < 2; t++) {
      u32x4 kk = {pk(ak[t][0][0], ak[t][0][1]), pk(ak[t][0][2], ak[t][0][3]), pk(ak[t][1][0], ak[t][1][1]), pk(ak[t][1][2], ak[t][1][3])};
      u32x4 vv = {pk(av[0][t][0], av[0][t][1]), pk(av[0][t][2], av[0][t][3]), pk(av[1][t][0], av[1][t][1]), pk(av[1][t][2], av[1][t][3])};
      u32x4 qq = {pk(aq[t][0][0], aq[t][0][1]), pk(aq[t][0][2], aq[t][0][3]), pk(aq[t][1][0], aq[t][1][1]), pk(aq[t][1][2], aq[t][1][3])};
      st16(ws, kk, (u32)l * 16u + (u32)w * 1024u, OFF_KX + so + t * 4096u);
      st16(ws, vv, (u32)l * 16u + (u32)(2 * w + t) * 1024u, OFF_VX + so);
      st16(ws, qq, (u32)l * 16u + (u32)w * 1024u, OFF_QX + so + t * 4096u);
    }
    WAITVM(0); BAR();
    if (w == 0 && l == 0) st4(ws, 1u, 0, C_KV + (u32)(h * 64 + s) * 4u);
    buf ^= 1;
  }
}

DI void ffn1_role(lptr smem, rsrc_t ws, u32* wsp, int cg, int jj, int w, int l, const char* x, const char* w1, const bf16* b1, const bf16* gw) {
  const u32 lds0 = (u32)(size_t)smem;
  bf16x8 B[2][16], Gf[16];
  stageW(w1, (u32)cg * 256u, 3584u, lds0, w, l);
  stageW(w1, (u32)cg * 256u + 256u * 3584u, 3584u, lds0 + 65536, w, l);
  float bias[8];
#pragma unroll
  for (int i = 0; i < 8; i++) bias[i] = (float)b1[cg * 128 + 32 * w + 16 * (i >> 2) + 4 * (l >> 4) + (i & 3)];
#pragma unroll
  for (int ks = 0; ks < 16; ks++) Gf[ks] = *(const bf16x8*)(gw + 32 * ks + 8 * (l >> 4));
  WAITVM(0); BAR(); readW<true>(B, 0, smem, w, l); readW<true>(B, 1, smem + 65536, w, l); BAR();
  stageX(x, jj, lds0, w, l);
  WAITVM(0); BAR();
  const f32x4 z4 = {0.f, 0.f, 0.f, 0.f};
  const bool dog = (cg == 0 && w == 0);
  int buf = 0;
  for (int s = jj; s < 64; s += 13) {
    if (s + 13 < 64) stageX(x, s + 13, lds0 + (buf ^ 1) * 32768, w, l);
    lptr xb = smem + buf * 32768 + l * 16;
    f32x4 a[2][2] = {{z4, z4}, {z4, z4}};
#pragma unroll
    for (int ks = 0; ks < 16; ks++) {
      const bf16x8 x0 = *(LAS bf16x8*)(xb + ks * 1024), x1 = *(LAS bf16x8*)(xb + 16384 + ks * 1024);
#pragma unroll
      for (int y = 0; y < 2; y++) { a[0][y] = mfma32(B[y][ks], x0, a[0][y]); a[1][y] = mfma32(B[y][ks], x1, a[1][y]); }
    }
#pragma unroll
    for (int t = 0; t < 2; t++) {
      float r[8];
#pragma unroll
      for (int i = 0; i < 8; i++) {
        const float hv = rbf(rbf(a[t][i >> 2][i & 3]) + bias[i]);
        const float z = hv > 0.f ? hv : 0.f;
        r[i] = z * z;
      }
      u32x4 o = {pk(r[0], r[1]), pk(r[2], r[3]), pk(r[4], r[5]), pk(r[6], r[7])};
      st16(ws, o, (u32)l * 16u + (u32)(cg * 4 + w) * 1024u, OFF_HF + (u32)(s * 2 + t) * 57344u);
    }
    if (dog) {
      f32x4 g0 = z4, g1 = z4;
#pragma unroll
      for (int ks = 0; ks < 16; ks++) {
        g0 = mfma32(Gf[ks], *(LAS bf16x8*)(xb + ks * 1024), g0);
        g1 = mfma32(Gf[ks], *(LAS bf16x8*)(xb + 16384 + ks * 1024), g1);
      }
      const float zz = rbf(l < 16 ? g0[0] : g1[0]);
      const float gg = rbf(1.f / (1.f + __expf(-zz)));
      if (l < 32) st4(ws, __builtin_bit_cast(u32, gg), (u32)l * 4u, OFF_G + (u32)s * 128u);
    }
    WAITVM(0); BAR();
    if (w == 0 && l == 0) __hip_atomic_fetch_add(wsp + (C_HC + (u32)s * 128u) / 4, 1u, __ATOMIC_RELAXED, __HIP_MEMORY_SCOPE_AGENT);
    buf ^= 1;
  }
}

DI void attn_role(rsrc_t ws, u32* wsp, int u, int l) {
  const int h = u & 31, gb = l >> 4, c = l & 15;
  bool dead = false;
  const f32x4 z4 = {0.f, 0.f, 0.f, 0.f};
  const u32x4 one8 = {0x3F803F80u, 0x3F803F80u, 0x3F803F80u, 0x3F803F80u};
  const u32x2 one4 = {0x3F803F80u, 0x3F803F80u};
  for (int s = u >> 5; s < 64; s += 8) {
    const u32 so = (u32)(s * 32 + h) * 8192u, sm = (u32)(s * 32 + h) * 4096u, lo = (u32)l * 16u;
    waitword<false>(ws, C_KV + (u32)(h * 64 + s) * 4u, 0xFFFFFFFFu, dead, 2);
    u32x4 Q[2][4], K[2][4], V[8], KM[4];
    u32x2 VM[8];
#pragma unroll
    for (int i = 0; i < 8; i++) {
      Q[i >> 2][i & 3] = ld16(ws, lo + (i & 3) * 1024u, OFF_QX + so + (i >> 2) * 4096u);
      K[i >> 2][i & 3] = ld16(ws, lo + (i & 3) * 1024u, OFF_KX + so + (i >> 2) * 4096u);
      V[i] = ld16(ws, lo + (i & 3) * 1024u, OFF_VX + so + (i >> 2) * 4096u);
    }
    f32x4 s00 = z4, s01 = z4, s11 = z4;
#pragma unroll
    for (int ks = 0; ks < 4; ks++) {
      s00 = mfma32(as8(K[0][ks]), as8(Q[0][ks]), s00);
      s01 = mfma32(as8(K[0][ks]), as8(Q[1][ks]), s01);
      s11 = mfma32(as8(K[1][ks]), as8(Q[1][ks]), s11);
    }
#pragma unroll
    for (int i = 0; i < 4; i++)
      if (4 * gb + i > c) { s00[i] = NEG; s11[i] = NEG; }
    waitword<false>(ws, C_KM + (u32)((h * 64 + s) * 2) * 4u, 0xFFFFFFFFu, dead, 2);
    waitword<false>(ws, C_KM + (u32)((h * 64 + s) * 2 + 1) * 4u, 0xFFFFFFFFu, dead, 2);
#pragma unroll
    for (int i = 0; i < 4; i++) KM[i] = ld16(ws, lo + i * 1024u, OFF_KM + sm);
#pragma unroll
    for (int i = 0; i < 8; i++) VM[i] = ld8(ws, (u32)l * 8u + i * 512u, OFF_VM + sm);
    f32x4 m0 = z4, m1 = z4;
#pragma unroll
    for (int ks = 0; ks < 4; ks++) { m0 = mfma32(as8(KM[ks]), as8(Q[0][ks]), m0); m1 = mfma32(as8(KM[ks]), as8(Q[1][ks]), m1); }
    if (gb >= 2) { m0 = (f32x4){NEG, NEG, NEG, NEG}; m1 = m0; }
#pragma unroll
    for (int t = 0; t < 2; t++) {
      const f32x4 sM = t ? m1 : m0, sa = t ? s01 : s00;
      float m = fmaxf(fmaxf(fmaxf(sM[0], sM[1]), fmaxf(sM[2], sM[3])), fmaxf(fmaxf(sa[0], sa[1]), fmaxf(sa[2], sa[3])));
      if (t) m = fmaxf(m, fmaxf(fmaxf(s11[0], s11[1]), fmaxf(s11[2], s11[3])));
      m = maxall(m) * SCL;
      float pM[4], p0[4], p1[4];
#pragma unroll
      for (int i = 0; i < 4; i++) {
        pM[i] = __builtin_amdgcn_exp2f(__builtin_fmaf(sM[i], SCL, -m));
        p0[i] = __builtin_amdgcn_exp2f(__builtin_fmaf(sa[i], SCL, -m));
        p1[i] = t ? __builtin_amdgcn_exp2f(__builtin_fmaf(s11[i], SCL, -m)) : 0.f;
      }
      const u32x2 PM = {pk(pM[0], pM[1]), pk(pM[2], pM[3])};
      const u32x4 PX = {pk(p0[0], p0[1]), pk(p0[2], p0[3]), pk(p1[0], p1[1]), pk(p1[2], p1[3])};
      f32x4 sum = mfma16(one4, PM, z4);
      sum = mfma32(as8(one8), as8(PX), sum);
      const float rs = __builtin_amdgcn_rcpf(sum[0]);
#pragma unroll
      for (int ks = 0; ks < 4; ks++) {
        f32x4 oa = mfma32(as8(V[2 * ks]), as8(PX), mfma16(VM[2 * ks], PM, z4));
        f32x4 ob = mfma32(as8(V[2 * ks + 1]), as8(PX), mfma16(VM[2 * ks + 1], PM, z4));
        u32x4 o = {pk(oa[0] * rs, oa[1] * rs), pk(oa[2] * rs, oa[3] * rs), pk(ob[0] * rs, ob[1] * rs), pk(ob[2] * rs, ob[3] * rs)};
        st16(ws, o, lo + ks * 1024u, OFF_OF + (u32)(s * 2 + t) * 131072u + (u32)h * 4096u);
      }
    }
    WAITVM(0);
    if (l == 0) __hip_atomic_fetch_add(wsp + (C_OC + (u32)s * 128u) / 4, 1u, __ATOMIC_RELAXED, __HIP_MEMORY_SCOPE_AGENT);
  }
}

DI void out_role(lptr smem, rsrc_t ws, int grp, int xt, int sl, int w, int l, const char* Wo, const char* w2, const bf16* b2, bf16* out) {
  const u32 lds0 = (u32)(size_t)smem + (u32)w * 32768u;
  lptr wb = smem + w * 32768;
  const int gb = l >> 4, q = (l >> 2) & 3, p = l & 3, c = l & 15;
  bf16x8 A[2][32], A2[2][14];
  bool dead = false;
  {
    const u32 ro = (u32)(l >> 2) * 1024u + (u32)sl * 64u + (u32)(l & 3) * 16u;
    auto issue = [&](const char* W, int row0, int n, int buf) {
#pragma unroll
      for (int it = 0; it < 16; it++)
        if (it < n) dma16(W, ro + (u32)(row0 + it * 16) * 1024u, lds0 + buf * 16384 + it * 1024);
    };
    lptr b = wb + (4 * gb + q) * 64 + p * 16;
    issue(w2, w * 448, 16, 0);
    issue(w2, w * 448 + 256, 12, 1);
    WAITVM(12);
#pragma unroll
    for (int k = 0; k < 8; k++)
#pragma unroll
      for (int y = 0; y < 2; y++) { A2[y][k] = cat2(trr(b + k * 2048 + y * 8), trr(b + k * 2048 + 1024 + y * 8)); }
    asm volatile("s_waitcnt lgkmcnt(0)" ::: "memory");
    issue(Wo, w * 1024, 16, 0);
    WAITVM(16);
#pragma unroll
    for (int k = 0; k < 6; k++)
#pragma unroll
      for (int y = 0; y < 2; y++) { A2[y][8 + k] = cat2(trr(b + 16384 + k * 2048 + y * 8), trr(b + 16384 + k * 2048 + 1024 + y * 8)); }
#pragma unroll
    for (int r = 0; r < 4; r++) {
      asm volatile("s_waitcnt lgkmcnt(0)" ::: "memory");
      if (r < 3) { issue(Wo, w * 1024 + (r + 1) * 256, 16, (r + 1) & 1); WAITVM(16); } else { WAITVM(0); }
#pragma unroll
      for (int k = 0; k < 8; k++)
#pragma unroll
        for (int y = 0; y < 2; y++)
        { A[y][r * 8 + k] = cat2(trr(b + (r & 1) * 16384 + k * 2048 + y * 8), trr(b + (r & 1) * 16384 + k * 2048 + 1024 + y * 8)); }
    }
  }
  float bias[8];
#pragma unroll
  for (int i = 0; i < 8; i++) bias[i] = (float)b2[sl * 32 + 8 * gb + i];
  BAR();
  const f32x4 z4 = {0.f, 0.f, 0.f, 0.f};
  const u32 lo = (u32)l * 16u;
  for (int s = grp; s < 64; s += 4) {
    f32x4 a0 = z4, a1 = z4, h0 = z4, h1 = z4;
    waitword<true>(ws, C_HC + (u32)s * 128u, 13u, dead, 2);
    {
      const u32 so = OFF_HF + (u32)(s * 2 + xt) * 57344u + (u32)w * 14336u;
      u32x4 F[14];
#pragma unroll
      for (int k = 0; k < 14; k++) F[k] = ld16(ws, lo + (k & 3) * 1024u, so + (k >> 2) * 4096u);
#pragma unroll
      for (int k = 0; k < 14; k++) { h0 = mfma32(A2[0][k], as8(F[k]), h0); h1 = mfma32(A2[1][k], as8(F[k]), h1); }
    }
    const float g = __builtin_bit_cast(float, ld4(ws, (u32)c * 4u, OFF_G + (u32)(s * 2 + xt) * 64u));
    waitword<true>(ws, C_OC + (u32)s * 128u, 31u, dead, 1);
    {
      const u32 so = OFF_OF + (u32)(s * 2 + xt) * 131072u + (u32)w * 32768u;
      u32x4 F[16];
#pragma unroll
      for (int k = 0; k < 16; k++) F[k] = ld16(ws, lo + (k & 3) * 1024u, so + (k >> 2) * 4096u);
#pragma unroll
      for (int k = 0; k < 32; k++) {
        a0 = mfma32(A[0][k], as8(F[k & 15]), a0); a1 = mfma32(A[1][k], as8(F[k & 15]), a1);
        if (k < 16) F[k] = ld16(ws, lo + (k & 3) * 1024u, so + 16384u + (k >> 2) * 4096u);
      }
    }
    lptr rb = smem + w * 4096 + lo;
    *(LAS f32x4*)(rb) = a0; *(LAS f32x4*)(rb + 1024) = a1; *(LAS f32x4*)(rb + 2048) = h0; *(LAS f32x4*)(rb + 3072) = h1;
    BAR();
    if (w == 0) {
#pragma unroll
      for (int k = 1; k < 4; k++) {
        a0 += *(LAS f32x4*)(rb + k * 4096); a1 += *(LAS f32x4*)(rb + k * 4096 + 1024);
        h0 += *(LAS f32x4*)(rb + k * 4096 + 2048); h1 += *(LAS f32x4*)(rb + k * 4096 + 3072);
      }
      const float g1 = rbf(1.f - g);
      float r[8];
#pragma unroll
      for (int i = 0; i < 8; i++) {
        const float av = rbf(i < 4 ? a0[i & 3] : a1[i & 3]);
        const float bv = rbf(rbf(i < 4 ? h0[i & 3] : h1[i & 3]) + bias[i]);
        r[i] = rbf(g * av) + rbf(g1 * bv);
      }
      u32x4 o = {pk(r[0], r[1]), pk(r[2], r[3]), pk(r[4], r[5]), pk(r[6], r[7])};
      *(u32x4*)(out + (size_t)(32 * s + 16 * xt + c) * 512 + sl * 32 + 8 * gb) = o;
    }
    BAR();
  }
}

DI void probe_role(lptr smem, rsrc_t ws, int w, int l) {
  const u32 base = ((w >> 1) ? OFF_PROBE : OFF_PART) + (u32)(w & 1) * 8192u;
  u32 best = 0xFFFFFFFFu;
  for (int i = 0; i < 6; i++) {
    u64 t0 = __builtin_amdgcn_s_memtime();
    asm volatile("s_waitcnt lgkmcnt(0)" ::: "memory");
    if (l == 0) st4(ws, 0xFFFFFFFFu, 0, base + (u32)i * 16384u);
    WAITVM(0);
    u64 t1 = __builtin_amdgcn_s_memtime();
    best = min(best, (u32)(t1 - t0));
  }
  if (l == 0) *(LAS u32*)(smem + w * 4) = best;
  BAR();
  if (w == 0 && l == 0) {
    const u32 a = *(LAS u32*)(smem), b = *(LAS u32*)(smem + 4), c = *(LAS u32*)(smem + 8), d = *(LAS u32*)(smem + 12);
    st4(ws, (b < a ? 1u : 0u) | (d < c ? 2u : 0u), 0, C_SEL);
  }
  BAR();
}

__global__ __launch_bounds__(256, 1) void fused_kernel(const char* x, const bf16* mem0, const char* Wq, const char* Wk, const char* Wv,
                                                      const char* Wo, const char* w1, const bf16* b1, const char* w2, const bf16* b2,
                                                      const bf16* gw, bf16* out, char* wsb, int mask) {
  extern __shared__ __attribute__((aligned(16))) char smem_[];
  lptr smem = (lptr)smem_;
  const int b = blockIdx.x, xq = b & 7, r = b >> 3;
  const int w = __builtin_amdgcn_readfirstlane(threadIdx.x >> 6), l = threadIdx.x & 63;
  const rsrc_t ws = __builtin_amdgcn_make_buffer_rsrc(wsb, 0, (int)WS_BYTES, 0x00020000);
  u32* wsp = (u32*)wsb;
  if (xq < 4 && r < 16) {
    if (mask & 2) chain_role(smem, ws, 8 * xq + (r >> 1), r & 1, w, l, Wq, Wk, Wv, Wo, mem0);
    return;
  }
  const int head = xq < 4 ? 8 * xq + ((r - 16) >> 1) : 8 * (xq - 4) + (r >> 2);
  const int j = xq < 4 ? 4 + (r & 1) : (r & 3);
  if (mask & 1) {
    if (b == 31 * 8) probe_role(smem, ws, w, l);
    qkv_role(smem, ws, head, j, w, l, x, Wq, Wk, Wv);
    const int wi = head * 6 + j;
    if (wi < 182) ffn1_role(smem, ws, wsp, wi / 13, wi % 13, w, l, x, w1, b1, gw);
  }
  if (xq < 4) {
    if (mask & 4) attn_role(ws, wsp, (xq * 16 + (r - 16)) * 4 + w, l);
  } else {
    if (mask & 8) out_role(smem, ws, xq - 4, r >> 4, r & 15, w, l, Wo, w2, b2, out);
  }
}

#define SPLIT 0
extern "C" void kernel_launch(void* const* d_in, const int* in_sizes, int n_in, void* d_out, int out_size, void* d_ws,
                              size_t ws_size, hipStream_t stream) {
  char* base = (char*)(((size_t)d_ws + 2 * MB - 1) & ~(size_t)(2 * MB - 1));
  (void)hipFuncSetAttribute((const void*)fused_kernel, hipFuncAttributeMaxDynamicSharedMemorySize, LDS_BYTES);
  (void)hipMemsetAsync(base + INIT_LO, 0xFF, C_END - INIT_LO, stream);
  for (int ph = 0; ph < (SPLIT ? 4 : 1); ph++)
    fused_kernel<<<256, 256, LDS_BYTES, stream>>>((const char*)d_in[0], (const bf16*)d_in[1], (const char*)d_in[2], (const char*)d_in[3],
                                                  (const char*)d_in[4], (const char*)d_in[5], (const char*)d_in[8], (const bf16*)d_in[9],
                                                  (const char*)d_in[10], (const bf16*)d_in[11], (const bf16*)d_in[12], (bf16*)d_out, base,
                                                  SPLIT ? (1 << ph) : 15);
}
```

```cpp
#include <hip/hip_runtime.h>
#include <hip/hip_bf16.h>

typedef unsigned u32;
typedef unsigned long long u64;
typedef __bf16 bf16;
typedef bf16 bf16x2 __attribute__((ext_vector_type(2)));
typedef bf16 bf16x8 __attribute__((ext_vector_type(8)));
typedef bf16 bf16x16 __attribute__((ext_vector_type(16)));
typedef short s16x4 __attribute__((ext_vector_type(4)));
typedef short s16x8 __attribute__((ext_vector_type(8)));
typedef short s16x16 __attribute__((ext_vector_type(16)));
typedef float f32x2 __attribute__((ext_vector_type(2)));
typedef float f32x4 __attribute__((ext_vector_type(4)));
typedef u32 u32x2 __attribute__((ext_vector_type(2)));
typedef u32 u32x4 __attribute__((ext_vector_type(4)));
typedef __amdgpu_buffer_rsrc_t rsrc_t;
#define LAS __attribute__((address_space(3)))
typedef LAS char* lptr;
#define DI __device__ __forceinline__

constexpr u32 MB = 1u << 20;
constexpr u32 OFF_PART = 0;
constexpr u32 OFF_MEM = 2 * MB;
constexpr u32 OFF_CTRL = 3 * MB;
constexpr u32 C_SEL = OFF_CTRL;
constexpr u32 C_KV = OFF_CTRL + 1024;
constexpr u32 C_KM = C_KV + 8192;
constexpr u32 C_OC = C_KM + 16384;
constexpr u32 C_HC = C_OC + 8192;
constexpr u32 C_END = C_HC + 8192;
constexpr u32 OFF_PROBE = OFF_CTRL + 512 * 1024;
constexpr u32 OFF_QX = 4 * MB, OFF_KX = 20 * MB, OFF_VX = 36 * MB;
constexpr u32 OFF_KM = 52 * MB, OFF_VM = 60 * MB, OFF_OF = 68 * MB, OFF_HF = 84 * MB, OFF_G = 92 * MB;
constexpr u32 WS_BYTES = 93 * MB;
constexpr u32 INIT_LO = OFF_MEM + 63 * 16384;
constexpr int LDS_BYTES = 144 * 1024;
constexpr int SPIN_MAX = 1 << 15;
constexpr int AUX = 16;

DI u32x4 ld16(rsrc_t r, u32 v, u32 s) { return __builtin_amdgcn_raw_buffer_load_b128(r, v, s, AUX); }
DI u32x2 ld8(rsrc_t r, u32 v, u32 s) { return __builtin_amdgcn_raw_buffer_load_b64(r, v, s, AUX); }
DI u32 ld4(rsrc_t r, u32 v, u32 s) { return __builtin_amdgcn_raw_buffer_load_b32(r, v, s, AUX); }
DI void st16(rsrc_t r, u32x4 x, u32 v, u32 s) {
  __builtin_amdgcn_raw_buffer_store_b128(x, r, v, s, AUX);
  asm volatile("s_nop 1" ::"v"(x) : "memory");
}
DI void st8(rsrc_t r, u32x2 x, u32 v, u32 s) { __builtin_amdgcn_raw_buffer_store_b64(x, r, v, s, AUX); }
DI void st4(rsrc_t r, u32 x, u32 v, u32 s) { __builtin_amdgcn_raw_buffer_store_b32(x, r, v, s, AUX); }
#define MEMBAR() asm volatile("" ::: "memory")
#define WAITVM(n) asm volatile("s_waitcnt vmcnt(" #n ")" ::: "memory")
#define WAITVM0() do { MEMBAR(); __builtin_amdgcn_s_waitcnt(0x0F70); MEMBAR(); } while (0)
#define BAR() asm volatile("s_waitcnt lgkmcnt(0)\n\ts_barrier" ::: "memory")
#define SCHED() __builtin_amdgcn_sched_barrier(0)
#define PINA(x) asm volatile("" : "+a"(x))
#define PINV(x) asm volatile("" : "+v"(x))
constexpr u32 OOB = 0x80000000u;

DI void dma16(const char* sbase, u32 voff, u32 ldsdst) {
  u32 keep;
  asm volatile("s_mov_b32 %0, m0\n\ts_mov_b32 m0, %3\n\ts_nop 0\n\tglobal_load_lds_dwordx4 %1, %2\n\ts_mov_b32 m0, %0"
               : "=&s"(keep) : "v"(voff), "s"(sbase), "s"(ldsdst) : "memory");
}
DI s16x4 trr(lptr p) { return __builtin_amdgcn_ds_read_tr16_b64_v4i16((LAS s16x4*)p); }
DI bf16x8 cat2(s16x4 a, s16x4 b) { return __builtin_bit_cast(bf16x8, __builtin_shufflevector(a, b, 0, 1, 2, 3, 4, 5, 6, 7)); }
DI bf16x16 cat4(s16x4 a, s16x4 b, s16x4 c, s16x4 d) {
  s16x8 x = __builtin_shufflevector(a, b, 0, 1, 2, 3, 4, 5, 6, 7), y = __builtin_shufflevector(c, d, 0, 1, 2, 3, 4, 5, 6, 7);
  return __builtin_bit_cast(bf16x16, __builtin_shufflevector(x, y, 0, 1, 2, 3, 4, 5, 6, 7, 8, 9, 10, 11, 12, 13, 14, 15));
}
DI u32 pk(float lo, float hi) { f32x2 v = {lo, hi}; return __builtin_bit_cast(u32, __builtin_convertvector(v, bf16x2)); }
DI float rbf(float x) { return (float)(bf16)x; }
DI bf16x8 as8(u32x4 x) { return __builtin_bit_cast(bf16x8, x); }
DI f32x4 mfma32(bf16x8 a, bf16x8 b, f32x4 c) { return __builtin_amdgcn_mfma_f32_16x16x32_bf16(a, b, c, 0, 0, 0); }
DI f32x4 mfma16(u32x2 a, u32x2 b, f32x4 c) {
  return __builtin_amdgcn_mfma_f32_16x16x16bf16_1k(__builtin_bit_cast(s16x4, a), __builtin_bit_cast(s16x4, b), c, 0, 0, 0);
}
DI f32x4 smf(u32x4 a, bf16x16 b, f32x4 c, int idx) { return __builtin_amdgcn_smfmac_f32_16x16x64_bf16(as8(a), b, c, idx, 0, 0); }
DI float comb(float x, float y) {
  auto r = __builtin_amdgcn_permlane32_swap(__builtin_bit_cast(u32, x), __builtin_bit_cast(u32, y), false, false);
  return __builtin_bit_cast(float, (u32)r[0]) + __builtin_bit_cast(float, (u32)r[1]);
}
DI float maxall(float m) {
  auto r = __builtin_amdgcn_permlane32_swap(__builtin_bit_cast(u32, m), __builtin_bit_cast(u32, m), false, false);
  m = fmaxf(__builtin_bit_cast(float, (u32)r[0]), __builtin_bit_cast(float, (u32)r[1]));
  auto q = __builtin_amdgcn_permlane16_swap(__builtin_bit_cast(u32, m), __builtin_bit_cast(u32, m), false, false);
  return fmaxf(__builtin_bit_cast(float, (u32)q[0]), __builtin_bit_cast(float, (u32)q[1]));
}
DI f32x4 zero4() {
  u64 a, b;
  asm volatile("v_mov_b64 %0, 0" : "=v"(a));
  asm volatile("v_mov_b64 %0, 0" : "=v"(b));
  typedef u64 u64x2 __attribute__((ext_vector_type(2)));
  u64x2 r = {a, b};
  return __builtin_bit_cast(f32x4, r);
}
DI u32 umax8(u32x4 a, u32x4 b) { return max(max(max(a[0], a[1]), max(a[2], a[3])), max(max(b[0], b[1]), max(b[2], b[3]))); }
DI bool anyl(bool p) { return __builtin_amdgcn_ballot_w64(p) != 0; }
template <bool EQ>
DI void waitword(rsrc_t r, u32 s, u32 val, bool& dead, int sleep) {
  int n = 0;
#pragma unroll 1
  while (!dead) {
    MEMBAR();
    u32 x = __builtin_amdgcn_readfirstlane(ld4(r, 0, s));
    if (EQ ? (x == val) : (x != val)) break;
    if (++n > SPIN_MAX) dead = true;
    for (int i = 0; i < sleep; i++) __builtin_amdgcn_s_sleep(4);
  }
  MEMBAR();
}
constexpr float NEG = -3.0e38f;
constexpr float SCL = 0.08838834764831845f * 1.4426950408889634f;

#define STAMP(i)
#define TL(slot)

DI void chain_role(lptr smem, rsrc_t ws, int h, int a, int w, int l, const char* Wq, const char* Wk, const char* Wv,
                   const char* Wo, const bf16* mem0) {
  const int gb = l >> 4, q = (l >> 2) & 3, p = l & 3, c = l & 15;
  const u32 lds0 = (u32)(size_t)smem;
  bf16x16 Bq[2][8], Bk[2][8], Bv[8], Bo[8];
  bool dead = false;
  {
    const u32 bx = (u32)((l & 15) ^ ((l >> 5) << 1));
    const u32 rowoff = (u32)(64 * w + (l >> 4)) * 8192u + (u32)h * 256u;
    auto issueQK = [&](const char* W, int half, int buf) {
#pragma unroll
      for (int it = 0; it < 16; it++) {
        const u32 cit = (u32)((((it >> 3) & 1) << 3) | ((it & 1) << 2));
        dma16(W, rowoff + (u32)(half * 256 + it * 4) * 8192u + ((bx ^ cit) << 4), lds0 + buf * 65536 + (w * 16 + it) * 1024);
      }
    };
    auto issueV = [&](int buf) {
      const u32 bv = (u32)((l & 7) ^ ((l >> 5) << 1));
      const u32 ro = (u32)(128 * w + ((l >> 3) & 1) * 32 + (l >> 4)) * 8192u + (u32)h * 256u + (u32)a * 128u;
#pragma unroll
      for (int it = 0; it < 16; it++)
        dma16(Wv, ro + (u32)((it >> 3) * 64 + (it & 7) * 4) * 8192u + ((bv ^ (u32)((it & 1) << 2)) << 4),
              lds0 + buf * 65536 + (w * 16 + it) * 1024);
    };
    auto issueO = [&](int buf) {
      const u32 ro = (u32)(h * 128 + a * 64 + w * 16) * 1024u;
#pragma unroll
      for (int it = 0; it < 16; it++) {
        const u32 f = (u32)((((it >> 3) & 1) << 2) | (((it >> 2) & 1) << 1) | (it & 1));
        dma16(Wo, ro + (u32)it * 1024u + (((u32)l ^ (f << 1)) << 4), lds0 + buf * 65536 + (w * 16 + it) * 1024);
      }
    };
    const int rowl = 32 * (q & 1) + 8 * (gb >> 1) + 4 * (q >> 1) + 2 * (gb & 1);
    const int fq = ((q & 1) << 2) | ((q >> 1) << 1) | (gb & 1);
    auto readQK = [&](bf16x16(&B)[2][8], int half, int buf) {
#pragma unroll
      for (int y = 0; y < 2; y++) {
        lptr b = smem + buf * 65536 + rowl * 256 + (((4 * w + 2 * y + (p >> 1)) ^ (fq << 1)) << 4) + (p & 1) * 8;
#pragma unroll
        for (int t = 0; t < 4; t++)
        {
          B[y][half * 4 + t] = cat4(trr(b + t * 16384), trr(b + t * 16384 + 256), trr(b + t * 16384 + 4096), trr(b + t * 16384 + 4352));
          PINA(B[y][half * 4 + t]);
        }
      }
    };
    issueQK(Wq, 0, 0);
    issueQK(Wq, 1, 1);
    WAITVM(16); BAR(); readQK(Bq, 0, 0); BAR(); issueQK(Wk, 0, 0);
    WAITVM(16); BAR(); readQK(Bq, 1, 1); BAR(); issueQK(Wk, 1, 1);
    WAITVM(16); BAR(); readQK(Bk, 0, 0); BAR(); issueV(0);
    WAITVM(16); BAR(); readQK(Bk, 1, 1); BAR(); issueO(1);
    WAITVM(16); BAR();
    {
      const int f2 = ((q >> 1) << 1) | (gb & 1);
      lptr b = smem + (8 * (gb >> 1) + 4 * (q >> 1) + 2 * (gb & 1)) * 256 + (q & 1) * 128 + (((2 * w + (p >> 1)) ^ (f2 << 1)) << 4) + (p & 1) * 8;
#pragma unroll
      for (int t = 0; t < 8; t++)
      {
        Bv[t] = cat4(trr(b + t * 8192), trr(b + t * 8192 + 256), trr(b + t * 8192 + 4096), trr(b + t * 8192 + 4352));
        PINV(Bv[t]);
      }
    }
    WAITVM0(); BAR();
    {
      const int fo = ((q >> 1) << 2) | ((gb & 1) << 1) | (q & 1);
      const int er = 16 * (gb >> 1) + 8 * (q >> 1) + 4 * (gb & 1) + (q & 1);
#pragma unroll
      for (int T = 0; T < 8; T++) {
        lptr b = smem + 65536 + er * 1024 + (((16 * w + 2 * T + (p >> 1)) ^ (fo << 1)) << 4) + (p & 1) * 8;
        Bo[T] = cat4(trr(b), trr(b + 2048), trr(b + 32768), trr(b + 32768 + 2048));
        PINV(Bo[T]);
      }
    }
    BAR();
  }
  TL(1);
  waitword<false>(ws, C_SEL, 0xFFFFFFFFu, dead, 1);
  const u32 selw = __builtin_amdgcn_readfirstlane(ld4(ws, 0, C_SEL));
  const u32 sel0 = (selw & 1u) * 8192u, sel1 = ((selw >> 1) & 1u) * 8192u;
  const int ci = 2 * h + a;
  const int rho = 2 * ci + (w & 1);
  const u32 rblk = (u32)(rho >> 1) * 16384u + (u32)(rho & 1) * 4096u + sel0;
  const u32 rgat = rblk + (u32)(4 * gb + (c & 3)) * 64u + (u32)(c >> 2) * 16u;
  const int rt = rho >> 4, ry = (rho >> 3) & 1, rq4 = (rho >> 2) & 1, ru = rho & 3;
  const u32 rpiece = (u32)rt * 1024u + (u32)(16 * (2 * ry + (ru >> 1)) + 8 * (ru & 1) + 4 * rq4) * 16u;
  const u32 rpub = rpiece + (u32)(c & 3) * 16u + (u32)gb * 4u;
  const u32 pst = (u32)(w * 16 + (l >> 3)) * 16384u + (u32)((l >> 2) & 1) * 4096u + sel0 + (u32)ci * 64u + (u32)(l & 3) * 16u;
  const u32x4 ones = {~0u, ~0u, ~0u, ~0u};
  const u32 rrst = rblk + (u32)(w >> 1) * 2048u + (u32)l * 16u;
  const u32 rpub4 = c < 4 ? rpub : OOB;
  const u32 lane0 = l == 0 ? 0u : OOB;
  if (w < 2) {
#pragma unroll
    for (int k = 0; k < 8; k++) st16(ws, ones, rblk + (u32)l * 16u + (k & 3) * 1024u, OFF_PART + (k >> 2) * MB);
#pragma unroll
    for (int k = 0; k < 4; k++) {
      const int s = 1 + k * 16 + (l >> 2);
      if (s < 64) st16(ws, ones, (u32)(63 - s) * 16384u + rpiece + (u32)(l & 3) * 16u, OFF_MEM + sel1);
    }
    const int d0 = 64 * rt + 16 * ry + 4 * ru + gb, r0 = 4 * rq4 + (c & 3);
    const unsigned short* m0 = (const unsigned short*)mem0;
    const u32 mv = (u32)m0[r0 * 512 + d0] | ((u32)m0[r0 * 512 + d0 + 32] << 16);
    WAITVM0();
    if (c < 4) st4(ws, mv, rpub, OFF_MEM + sel1 + 63u * 16384u);
  }
  const u32 kvlane = (u32)l * 16u;
  lptr Lq = smem, Lk = smem + 2048, Lo = smem + 4096, Lx = smem + 8192, Lv = smem + 16384, La = smem + 20480;
  u32 flg = 0xFFFFFFFFu;
  auto fetch_kv = [&](int sn) {
    if (__builtin_amdgcn_readfirstlane(flg) == 0xFFFFFFFFu) waitword<false>(ws, C_KV + (u32)(h * 64 + sn) * 4u, 0xFFFFFFFFu, dead, 1);
    const u32 so = (u32)(sn * 32 + h) * 8192u;
    u32x4 t[6];
#pragma unroll
    for (int i = 0; i < 4; i++) t[i] = ld16(ws, kvlane, OFF_KX + so + (u32)(w - 2) * 4096u + i * 1024u);
#pragma unroll
    for (int i = 0; i < 2; i++) t[4 + i] = ld16(ws, kvlane, OFF_VX + so + (u32)(4 * a + 2 * (w - 2) + i) * 1024u);
    flg = ld4(ws, 0, C_KV + (u32)(h * 64 + sn + 1) * 4u);
#pragma unroll
    for (int i = 0; i < 4; i++) *(LAS u32x4*)(Lx + (w - 2) * 4096 + i * 1024 + l * 16) = t[i];
#pragma unroll
    for (int i = 0; i < 2; i++) *(LAS u32x4*)(Lv + (w - 2) * 2048 + i * 1024 + l * 16) = t[4 + i];
  };
  if (w >= 2) fetch_kv(0);
  WAITVM0();
  BAR();
  const u32 kmoff = (l >> 5) == a ? kvlane + (u32)w * 1024u : OOB;
  const u32 flgoff = w == 2 ? lane0 : OOB;
  const int idx = (l & 8) ? 0xEEEE : 0x4444;
  const int wq = (32 * w + 8 * (c >> 2) + 4 * (gb >> 1) + (c & 3)) * 16 + (gb & 1) * 8;
  const int rq = (8 * gb + q) * 16 + (p & 1) * 8;
  u32x4 selB;
  {
    const int j = c & 7;
    const u32 one = (j & 1) ? 0x3F800000u : 0x00003F80u;
    selB[0] = (j >> 1) == 0 ? one : 0u; selB[1] = (j >> 1) == 1 ? one : 0u;
    selB[2] = (j >> 1) == 2 ? one : 0u; selB[3] = (j >> 1) == 3 ? one : 0u;
  }
  u32x4 one8 = {0x3F803F80u, 0x3F803F80u, 0x3F803F80u, 0x3F803F80u};
  f32x4 cM = gb >= 2 ? (f32x4){NEG, NEG, NEG, NEG} : (f32x4){0.f, 0.f, 0.f, 0.f};
  PINV(one8); PINV(selB); PINV(cM);
  const u32x2 one4 = __builtin_shufflevector(one8, one8, 0, 1);
#define QKSTEP(t)                                                                                                   \
  asm volatile("v_smfmac_f32_16x16x64_bf16 %0, %4, %5, %9\n\tv_smfmac_f32_16x16x64_bf16 %1, %4, %6, %9\n\t"         \
               "v_smfmac_f32_16x16x64_bf16 %2, %4, %7, %9\n\tv_smfmac_f32_16x16x64_bf16 %3, %4, %8, %9"             \
               : "+v"(q0), "+v"(q1), "+v"(k0), "+v"(k1)                                                             \
               : "v"(A[t]), "a"(Bq[0][t]), "a"(Bq[1][t]), "a"(Bk[0][t]), "a"(Bk[1][t]), "v"(idx))
#define VSTEP(t) asm volatile("v_smfmac_f32_16x16x64_bf16 %0, %1, %2, %3" : "+v"(v0) : "v"(A[t]), "v"(Bv[t]), "v"(idx))

  TL(2);
#define NSTEP 64
  for (int s = 0; s < NSTEP; s++) {
    const u32 mslot = OFF_MEM + sel1 + (u32)(63 - s) * 16384u;
    STAMP(0);
    u32x4 A[8];
    u32x4 g0 = ld16(ws, kvlane, mslot + (u32)w * 2048u); SCHED();
    u32x4 g1 = ld16(ws, kvlane, mslot + (u32)w * 2048u + 1024u); SCHED();
    st16(ws, ones, rrst, OFF_PART + ((s + 1) & 1) * MB);
    st16(ws, ones, rrst, OFF_PART + ((s + 1) & 1) * MB + 1024u);
    st4(ws, 1u, s > 0 ? flgoff : OOB, C_KM - 8u + (u32)(h * 128 + a) * 4u + (u32)s * 8u);
    f32x4 q0 = zero4(), q1 = zero4(), k0 = zero4(), k1 = zero4(), v0 = zero4();
    asm volatile("s_nop 1" : "+v"(q0), "+v"(q1), "+v"(k0), "+v"(k1), "+v"(v0));
    SCHED();
    *(LAS u32x4*)(La + w * 2048 + l * 16) = g0;
    *(LAS u32x4*)(La + w * 2048 + 1024 + l * 16) = g1;
    SCHED();
    if (__builtin_expect(anyl(umax8(g0, g1) == 0xFFFFFFFFu) && !dead, 0)) {
      int n = 0;
#pragma unroll 1
      do {
        MEMBAR();
        g0 = ld16(ws, kvlane, mslot + (u32)w * 2048u);
        g1 = ld16(ws, kvlane, mslot + (u32)w * 2048u + 1024u);
        WAITVM0();
        if (++n > SPIN_MAX) dead = true;
      } while (anyl(umax8(g0, g1) == 0xFFFFFFFFu) && !dead);
      *(LAS u32x4*)(La + w * 2048 + l * 16) = g0;
      *(LAS u32x4*)(La + w * 2048 + 1024 + l * 16) = g1;
    }
    STAMP(1);
    BAR();
#pragma unroll
    for (int t = 0; t < 8; t++) { A[t] = *(LAS u32x4*)(La + t * 1024 + l * 16); SCHED(); }
    {
      QKSTEP(0); QKSTEP(1); QKSTEP(2); QKSTEP(3); QKSTEP(4); QKSTEP(5); QKSTEP(6); QKSTEP(7);
      asm volatile("s_nop 5" : "+v"(q0), "+v"(q1));
      u32x2 a2 = {pk(comb(q0[0], q1[0]), comb(q0[1], q1[1])), pk(comb(q0[2], q1[2]), comb(q0[3], q1[3]))};
      asm volatile("" : "+v"(k0), "+v"(k1), "+v"(a2));
      *(LAS u32x2*)(Lq + wq) = a2;
      u32x2 b2 = {pk(comb(k0[0], k1[0]), comb(k0[1], k1[1])), pk(comb(k0[2], k1[2]), comb(k0[3], k1[3]))};
      *(LAS u32x2*)(Lk + wq) = b2;
    }
    STAMP(2);
    BAR();
    STAMP(3);
    bf16x8 Fq[4], Fk[4];
    u32x4 X0[4], X1[4];
#pragma unroll
    for (int ks = 0; ks < 4; ks++) {
      Fk[ks] = cat2(trr(Lk + rq + ks * 512), trr(Lk + rq + ks * 512 + 64));
      Fq[ks] = cat2(trr(Lq + rq + ks * 512), trr(Lq + rq + ks * 512 + 64));
    }
    SCHED();
    VSTEP(0); VSTEP(1); VSTEP(2); VSTEP(3);
#pragma unroll
    for (int ks = 0; ks < 4; ks++) X0[ks] = *(LAS u32x4*)(Lx + ks * 1024 + l * 16);
    SCHED();
    VSTEP(4); VSTEP(5); VSTEP(6); VSTEP(7);
#pragma unroll
    for (int ks = 0; ks < 4; ks++) X1[ks] = *(LAS u32x4*)(Lx + 4096 + ks * 1024 + l * 16);
    const u32x4 vx = *(LAS u32x4*)(Lv + w * 1024 + l * 16);
    SCHED();
    STAMP(11);
    f32x4 sM, s0, s1;
#define STILE(D, F, C0)                                                                                              \
  asm volatile("v_mfma_f32_16x16x32_bf16 %0, %1, %5, " C0 "\n\tv_mfma_f32_16x16x32_bf16 %0, %2, %6, %0\n\t"               \
               "v_mfma_f32_16x16x32_bf16 %0, %3, %7, %0\n\tv_mfma_f32_16x16x32_bf16 %0, %4, %8, %0"                  \
               : "=&v"(D) : "v"(F[0]), "v"(F[1]), "v"(F[2]), "v"(F[3]), "v"(Fq[0]), "v"(Fq[1]), "v"(Fq[2]), "v"(Fq[3]), "v"(cM))
    STILE(sM, Fk, "%9");
    asm volatile("s_nop 3" : "+v"(v0));
    const u32x2 vm = {pk(comb(v0[0], v0[0]), comb(v0[1], v0[1])), pk(comb(v0[2], v0[2]), comb(v0[3], v0[3]))};
    STILE(s0, X0, "0");
    const u32 kmso = (u32)(s * 32 + h) * 4096u;
    st8(ws, vm, (u32)l * 8u + (u32)(4 * a + w) * 512u, OFF_VM + kmso);
    const bf16x8 kf = cat2(trr(Lk + rq + w * 512), trr(Lk + rq + w * 512 + 64));
    if (s == 63) {
      st16(ws, __builtin_bit_cast(u32x4, kf), kmoff, OFF_KM + kmso);
      break;
    }
    SCHED();
    STAMP(12);
    float m, mt;
    asm volatile(
        "v_mfma_f32_16x16x32_bf16 %0, %3, %7, 0\n\tv_max3_f32 %1, %11, %12, %13\n\t"
        "v_mfma_f32_16x16x32_bf16 %0, %4, %8, %0\n\tv_max_f32 %1, %1, %14\n\t"
        "v_mfma_f32_16x16x32_bf16 %0, %5, %9, %0\n\tv_mfma_f32_16x16x32_bf16 %0, %6, %10, %0\n\ts_nop 1\n\t"
        "v_max3_f32 %1, %1, %15, %16\n\tv_max3_f32 %1, %1, %17, %18\n\ts_nop 3"
        : "=&v"(s1), "=&v"(m), "=&v"(mt)
        : "v"(X1[0]), "v"(X1[1]), "v"(X1[2]), "v"(X1[3]), "v"(Fq[0]), "v"(Fq[1]), "v"(Fq[2]), "v"(Fq[3]), "v"(sM[0]), "v"(sM[1]),
          "v"(sM[2]), "v"(sM[3]), "v"(s0[0]), "v"(s0[1]), "v"(s0[2]), "v"(s0[3]));
    asm volatile(
        "v_max3_f32 %0, %0, %2, %3\n\tv_max3_f32 %0, %0, %4, %5\n\tv_mov_b32 %1, %0\n\ts_nop 1\n\t"
        "v_permlane32_swap_b32 %0, %1\n\tv_max_f32 %0, %0, %1\n\tv_mov_b32 %1, %0\n\ts_nop 1\n\t"
        "v_permlane16_swap_b32 %0, %1\n\tv_max_f32 %0, %0, %1"
        : "+v"(m), "+v"(mt) : "v"(s1[0]), "v"(s1[1]), "v"(s1[2]), "v"(s1[3]));
    STAMP(13);
    m *= -SCL;
    float pM[4], p0[4], p1[4];
#pragma unroll
    for (int i = 0; i < 4; i++) pM[i] = __builtin_amdgcn_exp2f(__builtin_fmaf(sM[i], SCL, m));
    const u32x2 PM = {pk(pM[0], pM[1]), pk(pM[2], pM[3])};
    f32x4 sum, o;
    asm volatile("s_nop 1\n\tv_mfma_f32_16x16x16_bf16 %0, %3, %4, 0\n\tv_mfma_f32_16x16x16_bf16 %1, %5, %4, 0"
                 : "=&v"(sum), "=&v"(o), "+v"(m) : "v"(one4), "v"(PM), "v"(vm));
    st16(ws, __builtin_bit_cast(u32x4, kf), kmoff, OFF_KM + kmso);
#pragma unroll
    for (int i = 0; i < 4; i++) {
      p0[i] = __builtin_amdgcn_exp2f(__builtin_fmaf(s0[i], SCL, m));
      p1[i] = __builtin_amdgcn_exp2f(__builtin_fmaf(s1[i], SCL, m));
    }
    const u32x4 PX = {pk(p0[0], p0[1]), pk(p0[2], p0[3]), pk(p1[0], p1[1]), pk(p1[2], p1[3])};
    asm volatile("s_nop 1\n\tv_mfma_f32_16x16x32_bf16 %0, %2, %3, %0\n\tv_mfma_f32_16x16x32_bf16 %1, %4, %3, %1\n\ts_nop 6"
                 : "+v"(sum), "+v"(o) : "v"(one8), "v"(PX), "v"(vx));
    float rs = __builtin_amdgcn_rcpf(sum[0]);
    asm volatile("s_nop 0" : "+v"(o), "+v"(rs));
    {
      u32x2 ob = {pk(o[0] * rs, o[1] * rs), pk(o[2] * rs, o[3] * rs)};
      *(LAS u32x2*)(Lo + c * 144 + w * 32 + gb * 8) = ob;
    }
    STAMP(4);
    WAITVM(2);
    BAR();
    STAMP(5);
    const u32x4 Ao = *(LAS u32x4*)(Lo + (c & 7) * 144 + gb * 32 + (c >> 3) * 16);
    SCHED();
    f32x4 D[8] = {zero4(), zero4(), zero4(), zero4(), zero4(), zero4(), zero4(), zero4()};
    asm volatile("s_nop 1" : "+v"(D[0]), "+v"(D[1]), "+v"(D[2]), "+v"(D[3]), "+v"(D[4]), "+v"(D[5]), "+v"(D[6]), "+v"(D[7]));
#define OSTEP(T)                                                                                                     \
  asm volatile("v_smfmac_f32_16x16x64_bf16 %0, %4, %5, %9\n\tv_smfmac_f32_16x16x64_bf16 %1, %4, %6, %9\n\t"          \
               "v_smfmac_f32_16x16x64_bf16 %2, %4, %7, %9\n\tv_smfmac_f32_16x16x64_bf16 %3, %4, %8, %9"              \
               : "+v"(D[T]), "+v"(D[T + 1]), "+v"(D[T + 2]), "+v"(D[T + 3])                                          \
               : "v"(Ao), "v"(Bo[T]), "v"(Bo[T + 1]), "v"(Bo[T + 2]), "v"(Bo[T + 3]), "v"(idx))
    OSTEP(0);
    OSTEP(4);
    asm volatile("s_nop 3" : "+v"(D[0]), "+v"(D[1]), "+v"(D[2]), "+v"(D[3]));
#pragma unroll
    for (int mm = 0; mm < 2; mm++) {
      u32x4 pv;
#pragma unroll
      for (int x = 0; x < 2; x++) {
        const f32x4 da = D[4 * mm + 2 * x], db = D[4 * mm + 2 * x + 1];
        pv[2 * x] = pk(comb(da[0], db[0]), comb(da[1], db[1]));
        pv[2 * x + 1] = pk(comb(da[2], db[2]), comb(da[3], db[3]));
      }
      st16(ws, pv, pst, OFF_PART + (s & 1) * MB + mm * 8 * 16384u);
      if (mm == 0) asm volatile("" : "+v"(D[4]), "+v"(D[5]), "+v"(D[6]), "+v"(D[7])::"memory");
    }
    STAMP(6);
    WAITVM0();
    STAMP(7);
    if (w < 2) {
      int m2 = 0;
      float tot;
      while (true) {
        u32x4 g[4];
#pragma unroll
        for (int k = 0; k < 4; k++) { g[k] = ld16(ws, rgat, OFF_PART + (s & 1) * MB + k * 1024u); SCHED(); }
        f32x4 acc;
        asm volatile("v_mfma_f32_16x16x32_bf16 %0, %1, %2, 0" : "=&v"(acc) : "v"(g[0]), "v"(selB));
        asm volatile("v_mfma_f32_16x16x32_bf16 %0, %1, %2, %0" : "+v"(acc) : "v"(g[1]), "v"(selB));
        asm volatile("v_mfma_f32_16x16x32_bf16 %0, %1, %2, %0" : "+v"(acc) : "v"(g[2]), "v"(selB));
        asm volatile("v_mfma_f32_16x16x32_bf16 %0, %1, %2, %0\n\ts_nop 7" : "+v"(acc) : "v"(g[3]), "v"(selB));
        tot = (acc[0] + acc[1]) + (acc[2] + acc[3]);
        if (__builtin_expect(!anyl(tot != tot) || dead, 1)) break;
        if (++m2 > SPIN_MAX) dead = true;
        MEMBAR();
      }
      STAMP(8);
      const float hi = __builtin_bit_cast(float, __builtin_amdgcn_update_dpp(0, __builtin_bit_cast(int, tot), 0x104, 0xf, 0xf, true));
      st4(ws, pk(tot, hi), rpub4, mslot - 16384u);
      WAITVM0();
      STAMP(9);
    } else if (s < 62) {
      __builtin_amdgcn_s_sleep(1);
      fetch_kv(s + 1);
      STAMP(10);
    }
    BAR();
  }
  WAITVM0();
  BAR();
  if (w == 2 && l == 0) st4(ws, 1u, 0, C_KM + (u32)((h * 64 + 63) * 2 + a) * 4u);
  TL(3);
}

DI void stageW(const char* W, u32 off0, u32 rs, u32 ldsbuf, int w, int l) {
  const u32 bx = (u32)((l & 15) ^ ((l >> 4) << 1));
  const u32 ro = off0 + (u32)(64 * w + (l >> 4)) * rs;
#pragma unroll
  for (int it = 0; it < 16; it++)
    dma16(W, ro + (u32)(it * 4) * rs + ((bx ^ (u32)(((it >> 1) & 1) << 3)) << 4), ldsbuf + (w * 16 + it) * 1024);
}
template <bool AG>
DI void readW(bf16x8 (&B)[2][16], int half, lptr buf, int w, int l) {
  const int gb = l >> 4, q = (l >> 2) & 3, p = l & 3;
  const int f = ((gb & 1) << 2) | q;
#pragma unroll
  for (int y = 0; y < 2; y++) {
    lptr b = buf + (8 * gb + q) * 256 + (((4 * w + 2 * y + (p >> 1)) ^ (f << 1)) << 4) + (p & 1) * 8;
#pragma unroll
    for (int k = 0; k < 8; k++) {
      B[y][half * 8 + k] = cat2(trr(b + k * 8192), trr(b + k * 8192 + 1024));
    }
  }
}
DI void stageX(const char* x, int s, u32 ldsbuf, int w, int l) {
  const u32 ro = (u32)(32 * s + 16 * (w >> 1) + (l & 15)) * 1024u + (u32)((w & 1) * 8) * 64u + (u32)(l >> 4) * 16u;
#pragma unroll
  for (int it = 0; it < 8; it++) dma16(x, ro + it * 64u, ldsbuf + (w * 8 + it) * 1024);
}

DI void qkv_role(lptr smem, rsrc_t ws, int h, int j, int w, int l, const char* x, const char* Wq, const char* Wk, const char* Wv) {
  const u32 lds0 = (u32)(size_t)smem;
  bf16x8 Bq[2][16], Bk[2][16], Bv[2][16];
  const u32 ho = (u32)h * 256u, hh = 256u * 8192u;
  stageW(Wk, ho, 8192u, lds0, w, l);
  stageW(Wk, ho + hh, 8192u, lds0 + 65536, w, l);
  WAITVM(16); BAR(); readW<true>(Bk, 0, smem, w, l); BAR(); stageW(Wv, ho, 8192u, lds0, w, l);
  WAITVM(16); BAR(); readW<true>(Bk, 1, smem + 65536, w, l); BAR(); stageW(Wv, ho + hh, 8192u, lds0 + 65536, w, l);
  WAITVM(16); BAR(); readW<false>(Bv, 0, smem, w, l); BAR(); stageW(Wq, ho, 8192u, lds0, w, l);
  WAITVM(16); BAR(); readW<false>(Bv, 1, smem + 65536, w, l); BAR(); stageW(Wq, ho + hh, 8192u, lds0 + 65536, w, l);
  WAITVM(16); BAR(); readW<true>(Bq, 0, smem, w, l); BAR(); stageX(x, j, lds0, w, l);
  WAITVM(8); BAR(); readW<true>(Bq, 1, smem + 65536, w, l);
  WAITVM(0); BAR();
  TL(1);
  const f32x4 z4 = {0.f, 0.f, 0.f, 0.f};
  int buf = 0;
  for (int s = j; s < 64; s += 6) {
    if (s + 6 < 64) stageX(x, s + 6, lds0 + (buf ^ 1) * 32768, w, l);
    lptr xb = smem + buf * 32768 + l * 16;
    f32x4 aq[2][2], ak[2][2], av[2][2];
#pragma unroll
    for (int i = 0; i < 4; i++) { aq[i >> 1][i & 1] = z4; ak[i >> 1][i & 1] = z4; av[i >> 1][i & 1] = z4; }
    bf16x8 n0 = *(LAS bf16x8*)(xb), n1 = *(LAS bf16x8*)(xb + 16384);
#pragma unroll
    for (int ks = 0; ks < 16; ks++) {
      bf16x8 x0 = n0, x1 = n1;
      if (ks < 15) { n0 = *(LAS bf16x8*)(xb + (ks + 1) * 1024); n1 = *(LAS bf16x8*)(xb + 16384 + (ks + 1) * 1024); }
      asm volatile("" : "+v"(x0), "+v"(x1));
#pragma unroll
      for (int y = 0; y < 2; y++) {
        ak[0][y] = mfma32(Bk[y][ks], x0, ak[0][y]); ak[1][y] = mfma32(Bk[y][ks], x1, ak[1][y]);
        av[0][y] = mfma32(x0, Bv[y][ks], av[0][y]); av[1][y] = mfma32(x1, Bv[y][ks], av[1][y]);
        aq[0][y] = mfma32(Bq[y][ks], x0, aq[0][y]); aq[1][y] = mfma32(Bq[y][ks], x1, aq[1][y]);
      }
      asm volatile("" : "+a"(ak[0][0]), "+a"(ak[0][1]), "+a"(ak[1][0]), "+a"(ak[1][1]), "+a"(av[0][0]), "+a"(av[0][1]), "+a"(av[1][0]),
                   "+a"(av[1][1]), "+a"(aq[0][0]), "+a"(aq[0][1]), "+a"(aq[1][0]), "+a"(aq[1][1]));
    }
    const u32 so = (u32)(s * 32 + h) * 8192u;
#pragma unroll
    for (int t = 0; t < 2; t++) {
      u32x4 kk = {pk(ak[t][0][0], ak[t][0][1]), pk(ak[t][0][2], ak[t][0][3]), pk(ak[t][1][0], ak[t][1][1]), pk(ak[t][1][2], ak[t][1][3])};
      u32x4 vv = {pk(av[0][t][0], av[0][t][1]), pk(av[0][t][2], av[0][t][3]), pk(av[1][t][0], av[1][t][1]), pk(av[1][t][2], av[1][t][3])};
      u32x4 qq = {pk(aq[t][0][0], aq[t][0][1]), pk(aq[t][0][2], aq[t][0][3]), pk(aq[t][1][0], aq[t][1][1]), pk(aq[t][1][2], aq[t][1][3])};
      st16(ws, kk, (u32)l * 16u + (u32)w * 1024u, OFF_KX + so + t * 4096u);
      st16(ws, vv, (u32)l * 16u + (u32)(2 * w + t) * 1024u, OFF_VX + so);
      st16(ws, qq, (u32)l * 16u + (u32)w * 1024u, OFF_QX + so + t * 4096u);
    }
    WAITVM(0); BAR();
    if (w == 0 && l == 0) st4(ws, 1u, 0, C_KV + (u32)(h * 64 + s) * 4u);
    if (s == j) TL(2);
    buf ^= 1;
  }
}

DI void ffn1_role(lptr smem, rsrc_t ws, u32* wsp, int cg, int jj, int w, int l, const char* x, const char* w1, const bf16* b1, const bf16* gw) {
  const u32 lds0 = (u32)(size_t)smem;
  bf16x8 B[2][16], Gf[16];
  stageW(w1, (u32)cg * 256u, 3584u, lds0, w, l);
  stageW(w1, (u32)cg * 256u + 256u * 3584u, 3584u, lds0 + 65536, w, l);
  float bias[8];
#pragma unroll
  for (int i = 0; i < 8; i++) bias[i] = (float)b1[cg * 128 + 32 * w + 16 * (i >> 2) + 4 * (l >> 4) + (i & 3)];
#pragma unroll
  for (int ks = 0; ks < 16; ks++) Gf[ks] = *(const bf16x8*)(gw + 32 * ks + 8 * (l >> 4));
  WAITVM(0); BAR(); readW<true>(B, 0, smem, w, l); readW<true>(B, 1, smem + 65536, w, l); BAR();
  stageX(x, jj, lds0, w, l);
  WAITVM(0); BAR();
  const f32x4 z4 = {0.f, 0.f, 0.f, 0.f};
  const bool dog = (cg == 0 && w == 0);
  int buf = 0;
  for (int s = jj; s < 64; s += 13) {
    if (s + 13 < 64) stageX(x, s + 13, lds0 + (buf ^ 1) * 32768, w, l);
    lptr xb = smem + buf * 32768 + l * 16;
    f32x4 a[2][2] = {{z4, z4}, {z4, z4}};
#pragma unroll
    for (int ks = 0; ks < 16; ks++) {
      const bf16x8 x0 = *(LAS bf16x8*)(xb + ks * 1024), x1 = *(LAS bf16x8*)(xb + 16384 + ks * 1024);
#pragma unroll
      for (int y = 0; y < 2; y++) { a[0][y] = mfma32(B[y][ks], x0, a[0][y]); a[1][y] = mfma32(B[y][ks], x1, a[1][y]); }
    }
#pragma unroll
    for (int t = 0; t < 2; t++) {
      float r[8];
#pragma unroll
      for (int i = 0; i < 8; i++) {
        const float hv = rbf(rbf(a[t][i >> 2][i & 3]) + bias[i]);
        const float z = hv > 0.f ? hv : 0.f;
        r[i] = z * z;
      }
      u32x4 o = {pk(r[0], r[1]), pk(r[2], r[3]), pk(r[4], r[5]), pk(r[6], r[7])};
      st16(ws, o, (u32)l * 16u + (u32)(cg * 4 + w) * 1024u, OFF_HF + (u32)(s * 2 + t) * 57344u);
    }
    if (dog) {
      f32x4 g0 = z4, g1 = z4;
#pragma unroll
      for (int ks = 0; ks < 16; ks++) {
        g0 = mfma32(Gf[ks], *(LAS bf16x8*)(xb + ks * 1024), g0);
        g1 = mfma32(Gf[ks], *(LAS bf16x8*)(xb + 16384 + ks * 1024), g1);
      }
      const float zz = rbf(l < 16 ? g0[0] : g1[0]);
      const float gg = rbf(1.f / (1.f + __expf(-zz)));
      if (l < 32) st4(ws, __builtin_bit_cast(u32, gg), (u32)l * 4u, OFF_G + (u32)s * 128u);
    }
    WAITVM(0); BAR();
    if (w == 0 && l == 0) __hip_atomic_fetch_add(wsp + (C_HC + (u32)s * 128u) / 4, 1u, __ATOMIC_RELAXED, __HIP_MEMORY_SCOPE_AGENT);
    buf ^= 1;
  }
}

DI void attn_role(rsrc_t ws, u32* wsp, int u, int l) {
  const int h = u & 31, gb = l >> 4, c = l & 15;
  bool dead = false;
  const f32x4 z4 = {0.f, 0.f, 0.f, 0.f};
  const u32x4 one8 = {0x3F803F80u, 0x3F803F80u, 0x3F803F80u, 0x3F803F80u};
  const u32x2 one4 = {0x3F803F80u, 0x3F803F80u};
  for (int s = u >> 5; s < 64; s += 8) {
    const u32 so = (u32)(s * 32 + h) * 8192u, sm = (u32)(s * 32 + h) * 4096u, lo = (u32)l * 16u;
    waitword<false>(ws, C_KV + (u32)(h * 64 + s) * 4u, 0xFFFFFFFFu, dead, 2);
    u32x4 Q[2][4], K[2][4], V[8], KM[4];
    u32x2 VM[8];
#pragma unroll
    for (int i = 0; i < 8; i++) {
      Q[i >> 2][i & 3] = ld16(ws, lo + (i & 3) * 1024u, OFF_QX + so + (i >> 2) * 4096u);
      K[i >> 2][i & 3] = ld16(ws, lo + (i & 3) * 1024u, OFF_KX + so + (i >> 2) * 4096u);
      V[i] = ld16(ws, lo + (i & 3) * 1024u, OFF_VX + so + (i >> 2) * 4096u);
    }
    SCHED();
    f32x4 s00 = z4, s01 = z4, s11 = z4;
#pragma unroll
    for (int ks = 0; ks < 4; ks++) {
      s00 = mfma32(as8(K[0][ks]), as8(Q[0][ks]), s00);
      s01 = mfma32(as8(K[0][ks]), as8(Q[1][ks]), s01);
      s11 = mfma32(as8(K[1][ks]), as8(Q[1][ks]), s11);
    }
#pragma unroll
    for (int i = 0; i < 4; i++)
      if (4 * gb + i > c) { s00[i] = NEG; s11[i] = NEG; }
    waitword<false>(ws, C_KM + (u32)((h * 64 + s) * 2) * 4u, 0xFFFFFFFFu, dead, 2);
    waitword<false>(ws, C_KM + (u32)((h * 64 + s) * 2 + 1) * 4u, 0xFFFFFFFFu, dead, 2);
#pragma unroll
    for (int i = 0; i < 4; i++) KM[i] = ld16(ws, lo + i * 1024u, OFF_KM + sm);
#pragma unroll
    for (int i = 0; i < 8; i++) VM[i] = ld8(ws, (u32)l * 8u + i * 512u, OFF_VM + sm);
    SCHED();
    f32x4 m0 = z4, m1 = z4;
#pragma unroll
    for (int ks = 0; ks < 4; ks++) { m0 = mfma32(as8(KM[ks]), as8(Q[0][ks]), m0); m1 = mfma32(as8(KM[ks]), as8(Q[1][ks]), m1); }
    if (gb >= 2) { m0 = (f32x4){NEG, NEG, NEG, NEG}; m1 = m0; }
#pragma unroll
    for (int t = 0; t < 2; t++) {
      const f32x4 sM = t ? m1 : m0, sa = t ? s01 : s00;
      float m = fmaxf(fmaxf(fmaxf(sM[0], sM[1]), fmaxf(sM[2], sM[3])), fmaxf(fmaxf(sa[0], sa[1]), fmaxf(sa[2], sa[3])));
      if (t) m = fmaxf(m, fmaxf(fmaxf(s11[0], s11[1]), fmaxf(s11[2], s11[3])));
      m = maxall(m) * SCL;
      float pM[4], p0[4], p1[4];
#pragma unroll
      for (int i = 0; i < 4; i++) {
        pM[i] = __builtin_amdgcn_exp2f(__builtin_fmaf(sM[i], SCL, -m));
        p0[i] = __builtin_amdgcn_exp2f(__builtin_fmaf(sa[i], SCL, -m));
        p1[i] = t ? __builtin_amdgcn_exp2f(__builtin_fmaf(s11[i], SCL, -m)) : 0.f;
      }
      const u32x2 PM = {pk(pM[0], pM[1]), pk(pM[2], pM[3])};
      const u32x4 PX = {pk(p0[0], p0[1]), pk(p0[2], p0[3]), pk(p1[0], p1[1]), pk(p1[2], p1[3])};
      const float rs = __builtin_amdgcn_rcpf(mfma16(one4, PM, z4)[0] + mfma32(as8(one8), as8(PX), z4)[0]);
#pragma unroll
      for (int ks = 0; ks < 4; ks++) {
        f32x4 oa = mfma32(as8(V[2 * ks]), as8(PX), z4) + mfma16(VM[2 * ks], PM, z4);
        f32x4 ob = mfma32(as8(V[2 * ks + 1]), as8(PX), z4) + mfma16(VM[2 * ks + 1], PM, z4);
        u32x4 o = {pk(oa[0] * rs, oa[1] * rs), pk(oa[2] * rs, oa[3] * rs), pk(ob[0] * rs, ob[1] * rs), pk(ob[2] * rs, ob[3] * rs)};
        st16(ws, o, lo + ks * 1024u, OFF_OF + (u32)(s * 2 + t) * 131072u + (u32)h * 4096u);
      }
    }
    WAITVM(0);
    if (l == 0) __hip_atomic_fetch_add(wsp + (C_OC + (u32)s * 128u) / 4, 1u, __ATOMIC_RELAXED, __HIP_MEMORY_SCOPE_AGENT);
  }
}

DI void out_role(lptr smem, rsrc_t ws, int grp, int xt, int sl, int w, int l, const char* Wo, const char* w2, const bf16* b2, bf16* out) {
  const u32 lds0 = (u32)(size_t)smem + (u32)w * 32768u;
  lptr wb = smem + w * 32768;
  const int gb = l >> 4, q = (l >> 2) & 3, p = l & 3, c = l & 15;
  bf16x8 A[2][32], A2[2][14];
  bool dead = false;
  {
    const u32 ro = (u32)(l >> 2) * 1024u + (u32)sl * 64u + (u32)(l & 3) * 16u;
    auto issue = [&](const char* W, int row0, int n, int buf) {
#pragma unroll
      for (int it = 0; it < 16; it++)
        if (it < n) dma16(W, ro + (u32)(row0 + it * 16) * 1024u, lds0 + buf * 16384 + it * 1024);
    };
    lptr b = wb + (4 * gb + q) * 64 + p * 16;
    issue(w2, w * 448, 16, 0);
    issue(w2, w * 448 + 256, 12, 1);
    WAITVM(12);
#pragma unroll
    for (int k = 0; k < 8; k++)
#pragma unroll
      for (int y = 0; y < 2; y++) { A2[y][k] = cat2(trr(b + k * 2048 + y * 8), trr(b + k * 2048 + 1024 + y * 8)); }
    asm volatile("s_waitcnt lgkmcnt(0)" ::: "memory");
    issue(Wo, w * 1024, 16, 0);
    WAITVM(16);
#pragma unroll
    for (int k = 0; k < 6; k++)
#pragma unroll
      for (int y = 0; y < 2; y++) { A2[y][8 + k] = cat2(trr(b + 16384 + k * 2048 + y * 8), trr(b + 16384 + k * 2048 + 1024 + y * 8)); }
#pragma unroll
    for (int r = 0; r < 4; r++) {
      asm volatile("s_waitcnt lgkmcnt(0)" ::: "memory");
      if (r < 3) { issue(Wo, w * 1024 + (r + 1) * 256, 16, (r + 1) & 1); WAITVM(16); } else { WAITVM(0); }
#pragma unroll
      for (int k = 0; k < 8; k++)
#pragma unroll
        for (int y = 0; y < 2; y++)
        { A[y][r * 8 + k] = cat2(trr(b + (r & 1) * 16384 + k * 2048 + y * 8), trr(b + (r & 1) * 16384 + k * 2048 + 1024 + y * 8)); }
    }
  }
  float bias[8];
#pragma unroll
  for (int i = 0; i < 8; i++) bias[i] = (float)b2[sl * 32 + 8 * gb + i];
  BAR();
  TL(5);
  const f32x4 z4 = {0.f, 0.f, 0.f, 0.f};
  const u32 lo = (u32)l * 16u;
  for (int s = grp; s < 64; s += 4) {
    f32x4 a0 = z4, a1 = z4, h0 = z4, h1 = z4;
    waitword<true>(ws, C_HC + (u32)s * 128u, 13u, dead, 2);
    {
      const u32 so = OFF_HF + (u32)(s * 2 + xt) * 57344u + (u32)w * 14336u;
      u32x4 F[14];
#pragma unroll
      for (int k = 0; k < 14; k++) F[k] = ld16(ws, lo, so + k * 1024u);
      SCHED();
#pragma unroll
      for (int k = 0; k < 14; k++) { h0 = mfma32(A2[0][k], as8(F[k]), h0); h1 = mfma32(A2[1][k], as8(F[k]), h1); }
    }
    const float g = __builtin_bit_cast(float, ld4(ws, (u32)c * 4u, OFF_G + (u32)(s * 2 + xt) * 64u));
    waitword<true>(ws, C_OC + (u32)s * 128u, 31u, dead, 1);
    {
      const u32 so = OFF_OF + (u32)(s * 2 + xt) * 131072u + (u32)w * 32768u;
      u32x4 F[16];
#pragma unroll
      for (int k = 0; k < 16; k++) F[k] = ld16(ws, lo, so + k * 1024u);
      SCHED();
#pragma unroll
      for (int k = 0; k < 32; k++) {
        a0 = mfma32(A[0][k], as8(F[k & 15]), a0); a1 = mfma32(A[1][k], as8(F[k & 15]), a1);
        if (k < 16) {
          asm volatile("" : "+a"(a0), "+a"(a1));
          F[k] = ld16(ws, lo, so + 16384u + k * 1024u);
          SCHED();
        }
      }
    }
    lptr rb = smem + w * 4096 + lo;
    *(LAS f32x4*)(rb) = a0; *(LAS f32x4*)(rb + 1024) = a1; *(LAS f32x4*)(rb + 2048) = h0; *(LAS f32x4*)(rb + 3072) = h1;
    BAR();
    if (w == 0) {
#pragma unroll
      for (int k = 1; k < 4; k++) {
        a0 += *(LAS f32x4*)(rb + k * 4096); a1 += *(LAS f32x4*)(rb + k * 4096 + 1024);
        h0 += *(LAS f32x4*)(rb + k * 4096 + 2048); h1 += *(LAS f32x4*)(rb + k * 4096 + 3072);
      }
      const float g1 = rbf(1.f - g);
      float r[8];
#pragma unroll
      for (int i = 0; i < 8; i++) {
        const float av = rbf(i < 4 ? a0[i & 3] : a1[i & 3]);
        const float bv = rbf(rbf(i < 4 ? h0[i & 3] : h1[i & 3]) + bias[i]);
        r[i] = rbf(g * av) + rbf(g1 * bv);
      }
      u32x4 o = {pk(r[0], r[1]), pk(r[2], r[3]), pk(r[4], r[5]), pk(r[6], r[7])};
      *(u32x4*)(out + (size_t)(32 * s + 16 * xt + c) * 512 + sl * 32 + 8 * gb) = o;
    }
    BAR();
  }
}

DI void probe_role(lptr smem, rsrc_t ws, int w, int l) {
  const u32 base = ((w & 1) ? OFF_PROBE : OFF_PART) + (u32)(w >> 1) * 131072u;
  u32 best[2] = {0xFFFFFFFFu, 0xFFFFFFFFu};
#pragma unroll 1
  for (int i = 0; i < 8; i++) {
#pragma unroll
    for (int sd = 0; sd < 2; sd++) {
      u64 t0 = __builtin_amdgcn_s_memtime();
      asm volatile("s_waitcnt lgkmcnt(0)" ::: "memory");
      st4(ws, 0xFFFFFFFFu, l == 0 ? 0u : OOB, base + (u32)i * 16384u + (u32)sd * 8192u);
      WAITVM(0);
      u64 t1 = __builtin_amdgcn_s_memtime();
      best[sd] = min(best[sd], (u32)(t1 - t0));
    }
  }
  if (l == 0) { *(LAS u32*)(smem + w * 8) = best[0]; *(LAS u32*)(smem + w * 8 + 4) = best[1]; }
  BAR();
  if (w == 0 && l == 0) {
    LAS u32* t = (LAS u32*)smem;
    const u32 a = min(t[0], t[4]), b = min(t[1], t[5]), c = min(t[2], t[6]), d = min(t[3], t[7]);
    st4(ws, (b < a ? 1u : 0u) | (d < c ? 2u : 0u), 0, C_SEL);
  }
  BAR();
}

__global__ __launch_bounds__(256, 1) void fused_kernel(const char* x, const bf16* mem0, const char* Wq, const char* Wk, const char* Wv,
                                                      const char* Wo, const char* w1, const bf16* b1, const char* w2, const bf16* b2,
                                                      const bf16* gw, bf16* out, char* wsb, int mask) {
  extern __shared__ __attribute__((aligned(16))) char smem_[];
  lptr smem = (lptr)smem_;
  const int b = blockIdx.x, xq = b & 7, r = b >> 3;
  const int w = __builtin_amdgcn_readfirstlane(threadIdx.x >> 6), l = threadIdx.x & 63;
  const rsrc_t ws = __builtin_amdgcn_make_buffer_rsrc(wsb, 0, (int)WS_BYTES, 0x00020000);
  u32* wsp = (u32*)wsb;
  TL(0);
  if (xq < 4 && r < 16) {
    if (mask & 2) chain_role(smem, ws, 8 * xq + (r >> 1), r & 1, w, l, Wq, Wk, Wv, Wo, mem0);
    return;
  }
  const int head = xq < 4 ? 8 * xq + ((r - 16) >> 1) : 8 * (xq - 4) + (r >> 2);
  const int j = xq < 4 ? 4 + (r & 1) : (r & 3);
  if (mask & 1) {
    if (b == 31 * 8) probe_role(smem, ws, w, l);
    qkv_role(smem, ws, head, j, w, l, x, Wq, Wk, Wv);
    TL(3);
    const int wi = head * 6 + j;
    if (wi < 182) ffn1_role(smem, ws, wsp, wi / 13, wi % 13, w, l, x, w1, b1, gw);
  }
  TL(4);
  if (xq < 4) {
    if (mask & 4) attn_role(ws, wsp, (xq * 16 + (r - 16)) * 4 + w, l);
  } else {
    if (mask & 8) out_role(smem, ws, xq - 4, r >> 4, r & 15, w, l, Wo, w2, b2, out);
  }
  TL(6);
}

#define SPLIT 0
extern "C" void kernel_launch(void* const* d_in, const int* in_sizes, int n_in, void* d_out, int out_size, void* d_ws,
                              size_t ws_size, hipStream_t stream) {
  char* base = (char*)(((size_t)d_ws + 2 * MB - 1) & ~(size_t)(2 * MB - 1));
  (void)hipFuncSetAttribute((const void*)fused_kernel, hipFuncAttributeMaxDynamicSharedMemorySize, LDS_BYTES);
  (void)hipMemsetAsync(base + INIT_LO, 0xFF, C_END - INIT_LO, stream);
  for (int ph = 0; ph < (SPLIT ? 4 : 1); ph++)
    fused_kernel<<<256, 256, LDS_BYTES, stream>>>((const char*)d_in[0], (const bf16*)d_in[1], (const char*)d_in[2], (const char*)d_in[3],
                                                  (const char*)d_in[4], (const char*)d_in[5], (const char*)d_in[8], (const bf16*)d_in[9],
                                                  (const char*)d_in[10], (const bf16*)d_in[11], (const bf16*)d_in[12], (bf16*)d_out, base,
                                                  SPLIT ? (1 << ph) : 15);
}
```

```cpp
#include <hip/hip_runtime.h>
#include <hip/hip_bf16.h>

typedef unsigned u32;
typedef unsigned long long u64;
typedef __bf16 bf16;
typedef bf16 bf16x2 __attribute__((ext_vector_type(2)));
typedef bf16 bf16x8 __attribute__((ext_vector_type(8)));
typedef bf16 bf16x16 __attribute__((ext_vector_type(16)));
typedef short s16x4 __attribute__((ext_vector_type(4)));
typedef short s16x8 __attribute__((ext_vector_type(8)));
typedef short s16x16 __attribute__((ext_vector_type(16)));
typedef float f32x2 __attribute__((ext_vector_type(2)));
typedef float f32x4 __attribute__((ext_vector_type(4)));
typedef u32 u32x2 __attribute__((ext_vector_type(2)));
typedef u32 u32x4 __attribute__((ext_vector_type(4)));
typedef __amdgpu_buffer_rsrc_t rsrc_t;
#define LAS __attribute__((address_space(3)))
typedef LAS char* lptr;
#define DI __device__ __forceinline__

constexpr u32 MB = 1u << 20;
constexpr u32 OFF_PART = 0;
constexpr u32 OFF_MEM = 94 * MB;
constexpr u32 OFF_CTRL = 96 * MB;
constexpr u32 C_SEL = OFF_CTRL;
constexpr u32 C_KV = OFF_CTRL + 1024;
constexpr u32 C_KM = C_KV + 8192;
constexpr u32 C_OC = C_KM + 16384;
constexpr u32 C_HC = C_OC + 8192;
constexpr u32 C_END = C_HC + 8192;
constexpr u32 OFF_QX = 4 * MB, OFF_KX = 20 * MB, OFF_VX = 36 * MB;
constexpr u32 OFF_KM = 52 * MB, OFF_VM = 60 * MB, OFF_OF = 68 * MB, OFF_HF = 84 * MB, OFF_G = 92 * MB;
constexpr u32 WS_BYTES = 97 * MB;
constexpr u32 INIT_LO = OFF_MEM + 31 * 65536;
DI u32 slot_off(int s) { const u32 r = (u32)(63 - s); return (r >> 1) * 65536u + (r & 1u) * 2048u; }
DI u32 tile_off(int t) { return (u32)(t >> 2) * 16384u + (u32)((t >> 1) & 1) * 4096u + (u32)(t & 1) * 1024u; }
constexpr int LDS_BYTES = 144 * 1024;
constexpr int SPIN_MAX = 1 << 15;
constexpr int AUX = 16;

DI u32x4 ld16(rsrc_t r, u32 v, u32 s) { return __builtin_amdgcn_raw_buffer_load_b128(r, v, s, AUX); }
DI u32x2 ld8(rsrc_t r, u32 v, u32 s) { return __builtin_amdgcn_raw_buffer_load_b64(r, v, s, AUX); }
DI u32 ld4(rsrc_t r, u32 v, u32 s) { return __builtin_amdgcn_raw_buffer_load_b32(r, v, s, AUX); }
DI void st16(rsrc_t r, u32x4 x, u32 v, u32 s) {
  __builtin_amdgcn_raw_buffer_store_b128(x, r, v, s, AUX);
  asm volatile("s_nop 1" ::"v"(x) : "memory");
}
DI void st8(rsrc_t r, u32x2 x, u32 v, u32 s) { __builtin_amdgcn_raw_buffer_store_b64(x, r, v, s, AUX); }
DI void st4(rsrc_t r, u32 x, u32 v, u32 s) { __builtin_amdgcn_raw_buffer_store_b32(x, r, v, s, AUX); }
#define MEMBAR() asm volatile("" ::: "memory")
#define WAITVM(n) asm volatile("s_waitcnt vmcnt(" #n ")" ::: "memory")
#define WAITVM0() do { MEMBAR(); __builtin_amdgcn_s_waitcnt(0x0F70); MEMBAR(); } while (0)
#define BAR() asm volatile("s_waitcnt lgkmcnt(0)\n\ts_barrier" ::: "memory")
#define SCHED() __builtin_amdgcn_sched_barrier(0)
#define PINA(x) asm volatile("" : "+a"(x))
#define PINV(x) asm volatile("" : "+v"(x))
constexpr u32 OOB = 0x80000000u;

DI void dma16(const char* sbase, u32 voff, u32 ldsdst) {
  u32 keep;
  asm volatile("s_mov_b32 %0, m0\n\ts_mov_b32 m0, %3\n\ts_nop 0\n\tglobal_load_lds_dwordx4 %1, %2\n\ts_mov_b32 m0, %0"
               : "=&s"(keep) : "v"(voff), "s"(sbase), "s"(ldsdst) : "memory");
}
DI void dma16c(const char* sbase, u32 voff, u32 ldsdst) {
  u32 keep;
  asm volatile("s_mov_b32 %0, m0\n\ts_mov_b32 m0, %3\n\ts_nop 0\n\tglobal_load_lds_dwordx4 %1, %2 sc1\n\ts_mov_b32 m0, %0"
               : "=&s"(keep) : "v"(voff), "s"(sbase), "s"(ldsdst) : "memory");
}
DI s16x4 trr(lptr p) { return __builtin_amdgcn_ds_read_tr16_b64_v4i16((LAS s16x4*)p); }
DI bf16x8 cat2(s16x4 a, s16x4 b) { return __builtin_bit_cast(bf16x8, __builtin_shufflevector(a, b, 0, 1, 2, 3, 4, 5, 6, 7)); }
DI bf16x16 cat4(s16x4 a, s16x4 b, s16x4 c, s16x4 d) {
  s16x8 x = __builtin_shufflevector(a, b, 0, 1, 2, 3, 4, 5, 6, 7), y = __builtin_shufflevector(c, d, 0, 1, 2, 3, 4, 5, 6, 7);
  return __builtin_bit_cast(bf16x16, __builtin_shufflevector(x, y, 0, 1, 2, 3, 4, 5, 6, 7, 8, 9, 10, 11, 12, 13, 14, 15));
}
DI u32 pk(float lo, float hi) { f32x2 v = {lo, hi}; return __builtin_bit_cast(u32, __builtin_convertvector(v, bf16x2)); }
DI u32 pk2(f32x2 v) { return __builtin_bit_cast(u32, __builtin_convertvector(v, bf16x2)); }
DI float rbf(float x) { return (float)(bf16)x; }
DI bf16x8 as8(u32x4 x) { return __builtin_bit_cast(bf16x8, x); }
DI f32x4 mfma32(bf16x8 a, bf16x8 b, f32x4 c) { return __builtin_amdgcn_mfma_f32_16x16x32_bf16(a, b, c, 0, 0, 0); }
DI f32x4 mfma16(u32x2 a, u32x2 b, f32x4 c) {
  return __builtin_amdgcn_mfma_f32_16x16x16bf16_1k(__builtin_bit_cast(s16x4, a), __builtin_bit_cast(s16x4, b), c, 0, 0, 0);
}
DI f32x4 smf(u32x4 a, bf16x16 b, f32x4 c, int idx) { return __builtin_amdgcn_smfmac_f32_16x16x64_bf16(as8(a), b, c, idx, 0, 0); }
DI float comb(float x, float y) {
  auto r = __builtin_amdgcn_permlane32_swap(__builtin_bit_cast(u32, x), __builtin_bit_cast(u32, y), false, false);
  return __builtin_bit_cast(float, (u32)r[0]) + __builtin_bit_cast(float, (u32)r[1]);
}
DI float maxall(float m) {
  auto r = __builtin_amdgcn_permlane32_swap(__builtin_bit_cast(u32, m), __builtin_bit_cast(u32, m), false, false);
  m = fmaxf(__builtin_bit_cast(float, (u32)r[0]), __builtin_bit_cast(float, (u32)r[1]));
  auto q = __builtin_amdgcn_permlane16_swap(__builtin_bit_cast(u32, m), __builtin_bit_cast(u32, m), false, false);
  return fmaxf(__builtin_bit_cast(float, (u32)q[0]), __builtin_bit_cast(float, (u32)q[1]));
}
DI f32x4 zero4() {
  u64 a, b;
  asm volatile("v_mov_b64 %0, 0" : "=v"(a));
  asm volatile("v_mov_b64 %0, 0" : "=v"(b));
  typedef u64 u64x2 __attribute__((ext_vector_type(2)));
  u64x2 r = {a, b};
  return __builtin_bit_cast(f32x4, r);
}
DI u32 umax8(u32x4 a, u32x4 b) { return max(max(max(a[0], a[1]), max(a[2], a[3])), max(max(b[0], b[1]), max(b[2], b[3]))); }
DI bool anyl(bool p) { return __builtin_amdgcn_ballot_w64(p) != 0; }
template <bool EQ>
DI void waitword(rsrc_t r, u32 s, u32 val, bool& dead, int sleep) {
  int n = 0;
#pragma unroll 1
  while (!dead) {
    MEMBAR();
    u32 x = __builtin_amdgcn_readfirstlane(ld4(r, 0, s));
    if (EQ ? (x == val) : (x != val)) break;
    if (++n > SPIN_MAX) dead = true;
    for (int i = 0; i < sleep; i++) __builtin_amdgcn_s_sleep(4);
  }
  MEMBAR();
}
constexpr float NEG = -3.0e38f;
constexpr float SCL = 0.08838834764831845f * 1.4426950408889634f;

#define STAMP(i)
#define TL(slot)

DI void chain_role(lptr smem, rsrc_t ws, int h, int a, int w, int l, const char* Wq, const char* Wk, const char* Wv,
                   const char* Wo, const bf16* mem0, const char* wsb) {
  const int gb = l >> 4, q = (l >> 2) & 3, p = l & 3, c = l & 15;
  const u32 lds0 = (u32)(size_t)smem;
  bf16x16 Bq[2][8], Bk[2][8], Bv[8], Bo[8];
  bool dead = false;
  {
    const u32 bx = (u32)((l & 15) ^ ((l >> 5) << 1));
    const u32 rowoff = (u32)(64 * w + (l >> 4)) * 8192u + (u32)h * 256u;
    auto issueQK = [&](const char* W, int half, int buf) {
#pragma unroll
      for (int it = 0; it < 16; it++) {
        const u32 cit = (u32)((((it >> 3) & 1) << 3) | (((it >> 2) & 1) << 2));
        dma16(W, rowoff + (u32)(half * 256 + it * 4) * 8192u + ((bx ^ cit) << 4), lds0 + buf * 65536 + (w * 16 + it) * 1024);
      }
    };
    auto issueV = [&](int buf) {
      const u32 bv = (u32)((l & 7) ^ ((l >> 5) << 1));
      const u32 ro = (u32)(128 * w + ((l >> 3) & 1) * 32 + (l >> 4)) * 8192u + (u32)h * 256u + (u32)a * 128u;
#pragma unroll
      for (int it = 0; it < 16; it++)
        dma16(Wv, ro + (u32)((it >> 3) * 64 + (it & 7) * 4) * 8192u + ((bv ^ (u32)(((it >> 2) & 1) << 2)) << 4),
              lds0 + buf * 65536 + (w * 16 + it) * 1024);
    };
    auto issueO = [&](int buf) {
      const u32 ro = (u32)(h * 128 + a * 64 + w * 16) * 1024u;
#pragma unroll
      for (int it = 0; it < 16; it++) {
        const u32 f = (u32)((((it >> 3) & 1) << 2) | (((it >> 2) & 1) << 1) | (it & 1));
        dma16(Wo, ro + (u32)it * 1024u + (((u32)l ^ (f << 1)) << 4), lds0 + buf * 65536 + (w * 16 + it) * 1024);
      }
    };
    const int rowl = 32 * (q & 1) + 16 * (q >> 1) + 2 * gb;
    const int fq = ((q & 1) << 2) | ((q >> 1) << 1) | (gb & 1);
    auto readQK = [&](bf16x16(&B)[2][8], int half, int buf) {
#pragma unroll
      for (int y = 0; y < 2; y++) {
        lptr b = smem + buf * 65536 + rowl * 256 + (((4 * w + 2 * y + (p >> 1)) ^ (fq << 1)) << 4) + (p & 1) * 8;
#pragma unroll
        for (int t = 0; t < 4; t++)
        {
          B[y][half * 4 + t] = cat4(trr(b + t * 16384), trr(b + t * 16384 + 256), trr(b + t * 16384 + 2048), trr(b + t * 16384 + 2304));
          PINA(B[y][half * 4 + t]);
        }
      }
    };
    issueQK(Wq, 0, 0);
    issueQK(Wq, 1, 1);
    WAITVM(16); BAR(); readQK(Bq, 0, 0); BAR(); issueQK(Wk, 0, 0);
    WAITVM(16); BAR(); readQK(Bq, 1, 1); BAR(); issueQK(Wk, 1, 1);
    WAITVM(16); BAR(); readQK(Bk, 0, 0); BAR(); issueV(0);
    WAITVM(16); BAR(); readQK(Bk, 1, 1); BAR(); issueO(1);
    WAITVM(16); BAR();
    {
      const int f2 = ((q >> 1) << 1) | (gb & 1);
      lptr b = smem + (16 * (q >> 1) + 2 * gb) * 256 + (q & 1) * 128 + (((2 * w + (p >> 1)) ^ (f2 << 1)) << 4) + (p & 1) * 8;
#pragma unroll
      for (int t = 0; t < 8; t++)
      {
        Bv[t] = cat4(trr(b + t * 8192), trr(b + t * 8192 + 256), trr(b + t * 8192 + 2048), trr(b + t * 8192 + 2304));
        PINV(Bv[t]);
      }
    }
    WAITVM0(); BAR();
    {
      const int fo = ((q >> 1) << 2) | ((gb & 1) << 1) | (q & 1);
      const int er = 16 * (gb >> 1) + 8 * (q >> 1) + 4 * (gb & 1) + (q & 1);
#pragma unroll
      for (int T = 0; T < 8; T++) {
        lptr b = smem + 65536 + er * 1024 + (((16 * w + 2 * T + (p >> 1)) ^ (fo << 1)) << 4) + (p & 1) * 8;
        Bo[T] = cat4(trr(b), trr(b + 2048), trr(b + 32768), trr(b + 32768 + 2048));
        PINV(Bo[T]);
      }
    }
    BAR();
  }
  TL(1);
  waitword<false>(ws, C_SEL, 0xFFFFFFFFu, dead, 1);
  const u32 selw = __builtin_amdgcn_readfirstlane(ld4(ws, 0, C_SEL));
  const u32 sel0 = (selw & 1u) * 8192u, sel1 = ((selw >> 1) & 1u) * 8192u;
  const int ci = 2 * h + a;
  const int rho = 2 * ci + (w & 1);
  const u32 rblk = (u32)(rho >> 1) * 16384u + (u32)(rho & 1) * 4096u + sel0;
  const u32 rgat = rblk + (u32)(4 * gb + (c & 3)) * 64u + (u32)(c >> 2) * 16u;
  constexpr u32 GK = 1024u;
  const int rt = rho >> 4, gs = (rho >> 2) & 3, ru = rho & 3;
  const u32 rpiece = tile_off(rt) + (u32)ru * 256u + (u32)gs * 64u;
  const u32 rpub = rpiece + (u32)(c & 3) * 16u + (u32)gb * 4u;
  const u32 pst = (u32)(w * 16 + (l >> 3)) * 16384u + (u32)((l >> 2) & 1) * 4096u + sel0 + (u32)ci * 64u + (u32)(l & 3) * 16u;
  const u32x4 ones = {~0u, ~0u, ~0u, ~0u};
  const u32 rpub4 = c < 4 ? rpub : OOB;
  const u32 lane0 = l == 0 ? 0u : OOB;
  if (w < 2) {
#pragma unroll
    for (int k = 0; k < 8; k++) st16(ws, ones, rblk + (u32)l * 16u + (k & 3) * 1024u, OFF_PART + (k >> 2) * MB);
#pragma unroll
    for (int k = 0; k < 4; k++) {
      const int s = 1 + k * 16 + (l >> 2);
      if (s < 64) st16(ws, ones, slot_off(s) + rpiece + (u32)(l & 3) * 16u, OFF_MEM + sel1);
    }
    const int d0 = 64 * rt + 16 * ((c >> 1) & 1) + 4 * ru + gb, r0 = 2 * gs + (c & 1);
    const unsigned short* m0 = (const unsigned short*)mem0;
    const u32 mv = (u32)m0[r0 * 512 + d0] | ((u32)m0[r0 * 512 + d0 + 32] << 16);
    WAITVM0();
    st4(ws, mv, rpub4, OFF_MEM + sel1 + slot_off(0));
  }
  const u32 kvlane = (u32)l * 16u;
  lptr Lq = smem, Lk = smem + 6144, Lx = smem + 12288, Lv = smem + 20480, La = smem + 24576, Lo = smem + 49152;
#pragma unroll
  for (int i = 0; i < 3; i++) *(LAS u32x4*)(smem + i * 4096 + w * 1024 + l * 16) = (u32x4){0u, 0u, 0u, 0u};
  u32 flg = 0xFFFFFFFFu;
  auto fetch_kv = [&](int sn) {
    if (flg == 0xFFFFFFFFu) waitword<false>(ws, C_KV + (u32)(h * 64 + sn) * 4u, 0xFFFFFFFFu, dead, 1);
    const u32 so = (u32)(sn * 32 + h) * 8192u;
    u32x4 t[6];
    const u32 fl = ld4(ws, 0, C_KV + (u32)(h * 64 + sn + 1) * 4u);
    SCHED();
#pragma unroll
    for (int i = 0; i < 4; i++) t[i] = ld16(ws, kvlane, OFF_KX + so + (u32)(w - 2) * 4096u + i * 1024u);
#pragma unroll
    for (int i = 0; i < 2; i++) t[4 + i] = ld16(ws, kvlane, OFF_VX + so + (u32)(4 * a + 2 * (w - 2) + i) * 1024u);
    SCHED();
    flg = __builtin_amdgcn_readfirstlane(fl);
#pragma unroll
    for (int i = 0; i < 4; i++) *(LAS u32x4*)(Lx + (w - 2) * 4096 + i * 1024 + l * 16) = t[i];
#pragma unroll
    for (int i = 0; i < 2; i++) *(LAS u32x4*)(Lv + (w - 2) * 2048 + i * 1024 + l * 16) = t[4 + i];
  };
  if (w >= 2) fetch_kv(0);
  WAITVM0();
  BAR();
  const u32 kmoff = (l >> 5) == a ? kvlane + (u32)w * 1024u : OOB;
  const u32 flgoff = w == 2 ? lane0 : OOB;
  const int idx = (l & 2) ? 0xEEEE : 0x4444;
  const int wq = (4 * w + (c >> 2)) * 384 + (c & 3) * 32 + gb * 8;
  const int rq = (4 * w + gb) * 384 + q * 32 + p * 8;
  const bool up = (c & 2) != 0;
  u32 pmask = up ? 0u : ~0u;
  PINV(pmask);
  lptr Ls = smem + 53248;
  const int orow = 2 * (c >> 2) + (c & 1);
  u32x4 selB;
  {
    const int j = c & 7;
    const u32 one = (j & 1) ? 0x3F800000u : 0x00003F80u;
    selB[0] = (j >> 1) == 0 ? one : 0u; selB[1] = (j >> 1) == 1 ? one : 0u;
    selB[2] = (j >> 1) == 2 ? one : 0u; selB[3] = (j >> 1) == 3 ? one : 0u;
  }
  u32x4 one8 = {0x3F803F80u, 0x3F803F80u, 0x3F803F80u, 0x3F803F80u};
  PINV(one8); PINV(selB);
#define SM2(d0, d1, B, t)                                                                                            \
  asm volatile("v_smfmac_f32_16x16x64_bf16 %0, %2, %3, %5\n\tv_smfmac_f32_16x16x64_bf16 %1, %2, %4, %5"              \
               : "+v"(d0), "+v"(d1) : "v"(A[t]), "a"(B[0][t]), "a"(B[1][t]), "v"(idx));                              \
  SCHED()
#define VSTEP(t)                                                                                                     \
  asm volatile("v_smfmac_f32_16x16x64_bf16 %0, %1, %2, %3" : "+v"(v0) : "v"(A[t]), "v"(Bv[t]), "v"(idx));            \
  SCHED()
#define MF0(D, F, G) asm volatile("v_mfma_f32_16x16x32_bf16 %0, %1, %2, 0" : "=&v"(D) : "v"(F), "v"(G)); SCHED()
#define MF1(D, F, G) asm volatile("v_mfma_f32_16x16x32_bf16 %0, %1, %2, %0" : "+v"(D) : "v"(F), "v"(G)); SCHED()
#define FOLD(D) pk2(__builtin_shufflevector(D, D, 0, 1) + __builtin_shufflevector(D, D, 2, 3))
#define UNDEF(x) asm volatile("" : "=v"(x))
#define NAP(n) do { if (n) __builtin_amdgcn_s_sleep(n); } while (0)
#define KVD 1
#define D1 10
#define D2 8
#define D3 9
#define D4 0

#define LS(i)
#define RT(i)
  TL(2);
  for (int s = 0; s < 64; s++) {
    const u32 mslot = OFF_MEM + sel1 + slot_off(s) + tile_off(2 * w);
    STAMP(0);
    RT(3);
    u32x4 A[8];
    u32x4 g0 = ld16(ws, kvlane, mslot); SCHED();
    u32x4 g1 = ld16(ws, kvlane, mslot + 1024u); SCHED();
    f32x4 q0 = zero4(), q1 = zero4(), k0 = zero4(), k1 = zero4(), v0 = zero4();
    u32 parS = OFF_PART + (u32)(s & 1) * MB, parT = parS + 8 * 16384u, rstS = parS ^ MB, rstT = parT ^ MB;
    u32 kmS = OFF_KM + (u32)(s * 32 + h) * 4096u, vmS = kmS + (OFF_VM - OFF_KM), pubS = OFF_MEM + sel1 + slot_off(s + 1);
    u32 flS = C_KM - 8u + (u32)(h * 128 + a) * 4u + (u32)s * 8u, flV = s > 0 ? flgoff : OOB;
    asm volatile("" : "+s"(parS), "+s"(parT), "+s"(rstS), "+s"(rstT), "+s"(kmS), "+s"(vmS), "+s"(pubS), "+s"(flS), "+v"(flV));
    SCHED();
    *(LAS u32x4*)(La + w * 2048 + l * 16) = g0;
    *(LAS u32x4*)(La + w * 2048 + 1024 + l * 16) = g1;
    SCHED();
    if (__builtin_expect(anyl(umax8(g0, g1) == 0xFFFFFFFFu), 0)) {
      int n = 0;
      asm volatile("" : "+v"(g0), "+v"(g1));
#pragma unroll 1
      while (anyl(umax8(g0, g1) == 0xFFFFFFFFu) && !dead) {
        MEMBAR();
        g0 = ld16(ws, kvlane, mslot);
        g1 = ld16(ws, kvlane, mslot + 1024u);
        WAITVM0();
        if (++n > SPIN_MAX) dead = true;
      }
      *(LAS u32x4*)(La + w * 2048 + l * 16) = g0;
      *(LAS u32x4*)(La + w * 2048 + 1024 + l * 16) = g1;
    }
    STAMP(1);
    RT(0);
    LS(0);
    BAR();
    LS(1);
#pragma unroll
    for (int t = 0; t < 8; t++) { A[t] = *(LAS u32x4*)(La + t * 1024 + l * 16); SCHED(); }
    SM2(q0, q1, Bq, 0);
    __builtin_amdgcn_raw_buffer_store_b128(ones, ws, pst, rstS, AUX); SCHED();
    SM2(q0, q1, Bq, 1); SM2(q0, q1, Bq, 2);
    __builtin_amdgcn_raw_buffer_store_b128(ones, ws, pst, rstT, AUX); SCHED();
    SM2(q0, q1, Bq, 3); SM2(q0, q1, Bq, 4);
    st4(ws, 1u, flV, flS); SCHED();
    SM2(q0, q1, Bq, 5); SM2(q0, q1, Bq, 6); SM2(q0, q1, Bq, 7);
    LS(2);
    SM2(k0, k1, Bk, 0);
    const u32x4 X0 = *(LAS u32x4*)(Lx + w * 1024 + l * 16), X1 = *(LAS u32x4*)(Lx + 4096 + w * 1024 + l * 16);
    SCHED();
    SM2(k0, k1, Bk, 1);
    asm volatile("" : "+v"(q0), "+v"(q1));
    {
      const u32 a0 = FOLD(q0), a1 = FOLD(q1);
      *(LAS u32*)(Lq + wq) = a0; *(LAS u32*)(Lq + wq + 4) = a0; SCHED();
      SM2(k0, k1, Bk, 2);
      *(LAS u32*)(Lq + wq + 128) = a1; *(LAS u32*)(Lq + wq + 132) = a1; SCHED();
    }
    SM2(k0, k1, Bk, 3);
    const u32x2 vxa = *(LAS u32x2*)(Lv + w * 1024 + l * 16), vxb = *(LAS u32x2*)(Lv + w * 1024 + l * 16 + 8);
    SCHED();
    SM2(k0, k1, Bk, 4);
    const bf16x8 Fq = cat2(trr(Lq + rq), trr(Lq + rq + 128));
    SCHED();
    SM2(k0, k1, Bk, 5); SM2(k0, k1, Bk, 6); SM2(k0, k1, Bk, 7);
    LS(3);
    f32x4 sM, s0, s1;
    MF0(s0, X0, Fq);
    MF0(s1, X1, Fq);
    VSTEP(0);
    asm volatile("" : "+v"(k0), "+v"(k1));
    { const u32 a0 = FOLD(k0), a1 = FOLD(k1); *(LAS u32*)(Lk + wq) = a0; *(LAS u32*)(Lk + wq + 128) = a1; SCHED(); }
    const bf16x8 Fk = cat2(trr(Lk + rq), trr(Lk + rq + 128));
    SCHED();
    VSTEP(1);
    asm volatile("" : "+v"(s0), "+v"(s1));
    {
      const f32x4 x = {up ? s1[0] : s0[0], up ? s1[1] : s0[1], up ? s1[2] : s0[2], up ? s1[3] : s0[3]};
      SCHED();
      VSTEP(2);
      *(LAS f32x4*)(Ls + w * 1536 + l * 16) = x;
      SCHED();
    }
    VSTEP(3); VSTEP(4); VSTEP(5);
    MF0(sM, Fk, Fq);
    VSTEP(6); VSTEP(7);
    st16(ws, __builtin_bit_cast(u32x4, Fk), kmoff, kmS);
    asm volatile("" : "+v"(sM));
    *(LAS f32x2*)(Ls + w * 1536 + 1024 + l * 8) = __builtin_shufflevector(sM, sM, 0, 1);
    SCHED();
    LS(4);
    BAR();
    LS(5);
    f32x4 xs[4];
    f32x2 ms[4];
#pragma unroll
    for (int j = 0; j < 4; j++) { xs[j] = *(LAS f32x4*)(Ls + j * 1536 + l * 16); ms[j] = *(LAS f32x2*)(Ls + j * 1536 + 1024 + l * 8); }
    SCHED();
    asm volatile("" : "+v"(v0));
    const u32 vm = FOLD(v0);
    st4(ws, vm, (u32)l * 4u + (u32)(4 * a + w) * 256u, vmS);
    if (s == 63) break;
    f32x4 D[8];
    D[0] = zero4(); D[1] = zero4(); D[2] = zero4(); D[3] = zero4();
    SCHED();
    const f32x4 sx = (xs[0] + xs[1]) + (xs[2] + xs[3]);
    const f32x2 sm = (ms[0] + ms[1]) + (ms[2] + ms[3]);
    float m, mt;
    LS(6);
    asm volatile("v_max3_f32 %0, %2, %3, %4\n\tv_max3_f32 %1, %5, %6, %7\n\tv_max_f32 %0, %0, %1\n\ts_nop 1\n\t"
                 "v_max_f32_dpp %1, %0, %0 quad_perm:[2,3,0,1] row_mask:0xf bank_mask:0xf\n\t"
                 "v_max_f32_dpp %0, %0, %0 quad_perm:[2,3,0,1] row_mask:0xf bank_mask:0xf\n\ts_nop 1\n\t"
                 "v_permlane32_swap_b32 %0, %1\n\tv_max_f32 %0, %0, %1\n\tv_mov_b32 %1, %0\n\ts_nop 1\n\t"
                 "v_permlane16_swap_b32 %0, %1\n\tv_max_f32 %0, %0, %1"
                 : "=&v"(m), "=&v"(mt) : "v"(sx[0]), "v"(sx[1]), "v"(sx[2]), "v"(sx[3]), "v"(sm[0]), "v"(sm[1]));
    LS(7);
    m *= -SCL;
    const f32x2 m2 = {m, m}, scl2 = {SCL, SCL};
#define EXP(t) pk(__builtin_amdgcn_exp2f(t[0]), __builtin_amdgcn_exp2f(t[1]))
    const f32x2 tM = __builtin_elementwise_fma(sm, scl2, m2), t0 = __builtin_elementwise_fma(__builtin_shufflevector(sx, sx, 0, 1), scl2, m2),
                t1 = __builtin_elementwise_fma(__builtin_shufflevector(sx, sx, 2, 3), scl2, m2);
    SCHED();
    const u32x4 P = {EXP(tM) & pmask, 0u, EXP(t0), EXP(t1)};
    const u32x4 VA = {vm, 0u, vxa[0], vxa[1]};
    f32x4 sum, oa, ob;
    asm volatile("s_nop 1\n\tv_mfma_f32_16x16x32_bf16 %0, %3, %4, 0\n\tv_mfma_f32_16x16x16_bf16 %2, %6, %7, 0\n\t"
                 "v_mfma_f32_16x16x32_bf16 %1, %5, %4, 0"
                 : "=&v"(sum), "=&v"(oa), "=&v"(ob) : "v"(one8), "v"(P), "v"(VA), "v"(vxb), "v"(__builtin_shufflevector(P, P, 2, 3)));
    {
      float rs;
      f32x4 o;
      asm volatile("v_add_f32_dpp %0, %1, %1 quad_perm:[2,3,0,1] row_mask:0xf bank_mask:0xf\n\ts_nop 0\n\tv_rcp_f32 %0, %0" : "=&v"(rs) : "v"(sum[0]));
      D[4] = zero4(); D[5] = zero4(); D[6] = zero4();
      asm volatile("v_add_f32_dpp %0, %4, %8 quad_perm:[2,3,0,1] row_mask:0xf bank_mask:0xf\n\t"
                   "v_add_f32_dpp %1, %5, %9 quad_perm:[2,3,0,1] row_mask:0xf bank_mask:0xf\n\t"
                   "v_add_f32_dpp %2, %6, %10 quad_perm:[2,3,0,1] row_mask:0xf bank_mask:0xf\n\t"
                   "v_add_f32_dpp %3, %7, %11 quad_perm:[2,3,0,1] row_mask:0xf bank_mask:0xf"
                   : "=&v"(o[0]), "=&v"(o[1]), "=&v"(o[2]), "=&v"(o[3])
                   : "v"(ob[0]), "v"(ob[1]), "v"(ob[2]), "v"(ob[3]), "v"(oa[0]), "v"(oa[1]), "v"(oa[2]), "v"(oa[3]));
      const f32x2 r2 = {rs, rs};
      u32x2 oo = {pk2(__builtin_shufflevector(o, o, 0, 1) * r2), pk2(__builtin_shufflevector(o, o, 2, 3) * r2)};
      *(LAS u32x2*)(Lo + (orow + ((c & 2) << 2)) * 144 + w * 32 + gb * 8) = oo;
    }
    STAMP(4);
    LS(8);
    WAITVM(2);
    BAR();
    STAMP(5);
    LS(9);
    const u32x4 Ao = *(LAS u32x4*)(Lo + orow * 144 + gb * 32 + ((c >> 1) & 1) * 16);
    SCHED();
    D[7] = zero4();
    SCHED();
#define OST(T) asm volatile("v_smfmac_f32_16x16x64_bf16 %0, %1, %2, %3" : "+v"(D[T]) : "v"(Ao), "v"(Bo[T]), "v"(idx)); SCHED()
#define OFD(P_, i, T) asm volatile("" : "+v"(D[T])); P_[i] = FOLD(D[T]); SCHED()
    {
      u32x4 pv, pw;
      OST(0); OST(1); OST(2); OST(3);
      OFD(pv, 0, 0); OST(4);
      OFD(pv, 1, 1); OST(5);
      OFD(pv, 2, 2); OST(6);
      OFD(pv, 3, 3); OST(7);
      st16(ws, pv, pst, parS); SCHED();
      OFD(pw, 0, 4); OFD(pw, 1, 5);
      asm volatile("s_nop 1" ::: "memory");
      OFD(pw, 2, 6); OFD(pw, 3, 7);
      st16(ws, pw, pst, parT);
    }
    STAMP(6);
    RT(1);
    LS(10);
    if (w < 2) {
      u32x4 g[4], gl[4];
      NAP(D1);
      SCHED();
#pragma unroll
      for (int k = 0; k < 4; k++) { g[k] = ld16(ws, rgat, parS + k * GK); SCHED(); }
      NAP(D2);
      SCHED();
#pragma unroll
      for (int k = 0; k < 4; k++) { gl[k] = ld16(ws, rgat, parS + k * GK); SCHED(); }
      float tot;
#define REDUCE(G)                                                                                                    \
  {                                                                                                                  \
    f32x4 acc;                                                                                                       \
    MF0(acc, G[0], selB); MF1(acc, G[1], selB); MF1(acc, G[2], selB); MF1(acc, G[3], selB);                           \
    asm volatile("s_nop 7" : "+v"(acc));                                                                             \
    const f32x2 t_ = __builtin_shufflevector(acc, acc, 0, 1) + __builtin_shufflevector(acc, acc, 2, 3); tot = t_[0] + t_[1];                                                               \
  }
      REDUCE(g);
      if (__builtin_expect(anyl(tot != tot), 0)) {
        REDUCE(gl);
        int n = 0;
#pragma unroll 1
        while (anyl(tot != tot) && !dead) {
          MEMBAR();
#pragma unroll
          for (int k = 0; k < 4; k++) { g[k] = ld16(ws, rgat, parS + k * GK); SCHED(); }
          REDUCE(g);
          if (++n > SPIN_MAX) dead = true;
        }
      }
      STAMP(8);
      RT(2);
      const float hi = __builtin_bit_cast(float, __builtin_amdgcn_update_dpp(0, __builtin_bit_cast(int, tot), 0x104, 0xf, 0xf, true));
      st4(ws, pk(tot, hi), rpub4, pubS);
      NAP(D3);
      asm volatile("" ::"v"(gl[0]), "v"(gl[1]), "v"(gl[2]), "v"(gl[3]));
      STAMP(9);
    } else {
      if (s < 62) {
        NAP(KVD);
        fetch_kv(s + 1);
      } else {
        WAITVM0();
      }
      STAMP(10);
    }
    BAR();
  }
  WAITVM0();
  BAR();
  if (w == 2 && l == 0) st4(ws, 1u, 0, C_KM + (u32)((h * 64 + 63) * 2 + a) * 4u);
  TL(3);
}

DI void stageW(const char* W, u32 off0, u32 rs, u32 ldsbuf, int w, int l) {
  const u32 bx = (u32)((l & 15) ^ ((l >> 4) << 1));
  const u32 ro = off0 + (u32)(64 * w + (l >> 4)) * rs;
#pragma unroll
  for (int it = 0; it < 16; it++)
    dma16(W, ro + (u32)(it * 4) * rs + ((bx ^ (u32)(((it >> 1) & 1) << 3)) << 4), ldsbuf + (w * 16 + it) * 1024);
}
template <bool AG>
DI void readW(bf16x8 (&B)[2][16], int half, lptr buf, int w, int l) {
  const int gb = l >> 4, q = (l >> 2) & 3, p = l & 3;
  const int f = ((gb & 1) << 2) | q;
#pragma unroll
  for (int y = 0; y < 2; y++) {
    lptr b = buf + (8 * gb + q) * 256 + (((4 * w + 2 * y + (p >> 1)) ^ (f << 1)) << 4) + (p & 1) * 8;
#pragma unroll
    for (int k = 0; k < 8; k++) {
      B[y][half * 8 + k] = cat2(trr(b + k * 8192), trr(b + k * 8192 + 1024));
    }
  }
}
DI void stageX(const char* x, int s, u32 ldsbuf, int w, int l) {
  const u32 ro = (u32)(32 * s + 16 * (w >> 1) + (l & 15)) * 1024u + (u32)((w & 1) * 8) * 64u + (u32)(l >> 4) * 16u;
#pragma unroll
  for (int it = 0; it < 8; it++) dma16(x, ro + it * 64u, ldsbuf + (w * 8 + it) * 1024);
}

DI void qkv_role(lptr smem, rsrc_t ws, int h, int j, int w, int l, const char* x, const char* Wq, const char* Wk, const char* Wv) {
  const u32 lds0 = (u32)(size_t)smem;
  bf16x8 Bq[2][16], Bk[2][16], Bv[2][16];
  const u32 ho = (u32)h * 256u, hh = 256u * 8192u;
  stageW(Wk, ho, 8192u, lds0, w, l);
  stageW(Wk, ho + hh, 8192u, lds0 + 65536, w, l);
  WAITVM(16); BAR(); readW<true>(Bk, 0, smem, w, l); BAR(); stageW(Wv, ho, 8192u, lds0, w, l);
  WAITVM(16); BAR(); readW<true>(Bk, 1, smem + 65536, w, l); BAR(); stageW(Wv, ho + hh, 8192u, lds0 + 65536, w, l);
  WAITVM(16); BAR(); readW<false>(Bv, 0, smem, w, l); BAR(); stageW(Wq, ho, 8192u, lds0, w, l);
  WAITVM(16); BAR(); readW<false>(Bv, 1, smem + 65536, w, l); BAR(); stageW(Wq, ho + hh, 8192u, lds0 + 65536, w, l);
  WAITVM(16); BAR(); readW<true>(Bq, 0, smem, w, l); BAR(); stageX(x, j, lds0, w, l);
  WAITVM(8); BAR(); readW<true>(Bq, 1, smem + 65536, w, l);
  WAITVM(0); BAR();
  TL(1);
  const f32x4 z4 = {0.f, 0.f, 0.f, 0.f};
  int buf = 0;
  for (int s = j; s < 64; s += 6) {
    if (s + 6 < 64) stageX(x, s + 6, lds0 + (buf ^ 1) * 32768, w, l);
    lptr xb = smem + buf * 32768 + l * 16;
    f32x4 aq[2][2], ak[2][2], av[2][2];
#pragma unroll
    for (int i = 0; i < 4; i++) { aq[i >> 1][i & 1] = z4; ak[i >> 1][i & 1] = z4; av[i >> 1][i & 1] = z4; }
    bf16x8 n0 = *(LAS bf16x8*)(xb), n1 = *(LAS bf16x8*)(xb + 16384);
#pragma unroll
    for (int ks = 0; ks < 16; ks++) {
      bf16x8 x0 = n0, x1 = n1;
      if (ks < 15) { n0 = *(LAS bf16x8*)(xb + (ks + 1) * 1024); n1 = *(LAS bf16x8*)(xb + 16384 + (ks + 1) * 1024); }
      asm volatile("" : "+v"(x0), "+v"(x1));
#pragma unroll
      for (int y = 0; y < 2; y++) {
        ak[0][y] = mfma32(Bk[y][ks], x0, ak[0][y]); ak[1][y] = mfma32(Bk[y][ks], x1, ak[1][y]);
        av[0][y] = mfma32(x0, Bv[y][ks], av[0][y]); av[1][y] = mfma32(x1, Bv[y][ks], av[1][y]);
        aq[0][y] = mfma32(Bq[y][ks], x0, aq[0][y]); aq[1][y] = mfma32(Bq[y][ks], x1, aq[1][y]);
      }
      asm volatile("" : "+a"(ak[0][0]), "+a"(ak[0][1]), "+a"(ak[1][0]), "+a"(ak[1][1]), "+a"(av[0][0]), "+a"(av[0][1]), "+a"(av[1][0]),
                   "+a"(av[1][1]), "+a"(aq[0][0]), "+a"(aq[0][1]), "+a"(aq[1][0]), "+a"(aq[1][1]));
    }
    const u32 so = (u32)(s * 32 + h) * 8192u;
#pragma unroll
    for (int t = 0; t < 2; t++) {
      u32x4 kk = {pk(ak[t][0][0], ak[t][0][1]), pk(ak[t][0][2], ak[t][0][3]), pk(ak[t][1][0], ak[t][1][1]), pk(ak[t][1][2], ak[t][1][3])};
      u32x4 vv = {pk(av[0][t][0], av[0][t][1]), pk(av[0][t][2], av[0][t][3]), pk(av[1][t][0], av[1][t][1]), pk(av[1][t][2], av[1][t][3])};
      u32x4 qq = {pk(aq[t][0][0], aq[t][0][1]), pk(aq[t][0][2], aq[t][0][3]), pk(aq[t][1][0], aq[t][1][1]), pk(aq[t][1][2], aq[t][1][3])};
      st16(ws, kk, (u32)l * 16u + (u32)w * 1024u, OFF_KX + so + t * 4096u);
      st16(ws, vv, (u32)l * 16u + (u32)(2 * w + t) * 1024u, OFF_VX + so);
      st16(ws, qq, (u32)l * 16u + (u32)w * 1024u, OFF_QX + so + t * 4096u);
    }
    WAITVM(0); BAR();
    if (w == 0 && l == 0) st4(ws, 1u, 0, C_KV + (u32)(h * 64 + s) * 4u);
    if (s == j) TL(2);
    buf ^= 1;
  }
}

DI void ffn1_role(lptr smem, rsrc_t ws, u32* wsp, int cg, int jj, int w, int l, const char* x, const char* w1, const bf16* b1, const bf16* gw) {
  const u32 lds0 = (u32)(size_t)smem;
  bf16x8 B[2][16], Gf[16];
  stageW(w1, (u32)cg * 256u, 3584u, lds0, w, l);
  stageW(w1, (u32)cg * 256u + 256u * 3584u, 3584u, lds0 + 65536, w, l);
  float bias[8];
#pragma unroll
  for (int i = 0; i < 8; i++) bias[i] = (float)b1[cg * 128 + 32 * w + 16 * (i >> 2) + 4 * (l >> 4) + (i & 3)];
#pragma unroll
  for (int ks = 0; ks < 16; ks++) Gf[ks] = *(const bf16x8*)(gw + 32 * ks + 8 * (l >> 4));
  WAITVM(0); BAR(); readW<true>(B, 0, smem, w, l); readW<true>(B, 1, smem + 65536, w, l); BAR();
  stageX(x, jj, lds0, w, l);
  WAITVM(0); BAR();
  const f32x4 z4 = {0.f, 0.f, 0.f, 0.f};
  const bool dog = (cg == 0 && w == 0);
  int buf = 0;
  for (int s = jj; s < 64; s += 9) {
    if (s + 9 < 64) stageX(x, s + 9, lds0 + (buf ^ 1) * 32768, w, l);
    lptr xb = smem + buf * 32768 + l * 16;
    f32x4 a[2][2] = {{z4, z4}, {z4, z4}};
#pragma unroll
    for (int ks = 0; ks < 16; ks++) {
      const bf16x8 x0 = *(LAS bf16x8*)(xb + ks * 1024), x1 = *(LAS bf16x8*)(xb + 16384 + ks * 1024);
#pragma unroll
      for (int y = 0; y < 2; y++) { a[0][y] = mfma32(B[y][ks], x0, a[0][y]); a[1][y] = mfma32(B[y][ks], x1, a[1][y]); }
    }
#pragma unroll
    for (int t = 0; t < 2; t++) {
      float r[8];
#pragma unroll
      for (int i = 0; i < 8; i++) {
        const float hv = rbf(rbf(a[t][i >> 2][i & 3]) + bias[i]);
        const float z = hv > 0.f ? hv : 0.f;
        r[i] = z * z;
      }
      u32x4 o = {pk(r[0], r[1]), pk(r[2], r[3]), pk(r[4], r[5]), pk(r[6], r[7])};
      st16(ws, o, (u32)l * 16u + (u32)(cg * 4 + w) * 1024u, OFF_HF + (u32)(s * 2 + t) * 57344u);
    }
    if (dog) {
      f32x4 g0 = z4, g1 = z4;
#pragma unroll
      for (int ks = 0; ks < 16; ks++) {
        g0 = mfma32(Gf[ks], *(LAS bf16x8*)(xb + ks * 1024), g0);
        g1 = mfma32(Gf[ks], *(LAS bf16x8*)(xb + 16384 + ks * 1024), g1);
      }
      const float zz = rbf(l < 16 ? g0[0] : g1[0]);
      const float gg = rbf(1.f / (1.f + __expf(-zz)));
      if (l < 32) st4(ws, __builtin_bit_cast(u32, gg), (u32)l * 4u, OFF_G + (u32)s * 128u);
    }
    WAITVM(0); BAR();
    if (w == 0 && l == 0) __hip_atomic_fetch_add(wsp + (C_HC + (u32)s * 128u) / 4, 1u, __ATOMIC_RELAXED, __HIP_MEMORY_SCOPE_AGENT);
    buf ^= 1;
  }
}

#define G3 0
#define THR 0
DI void attn_role(rsrc_t ws, u32* wsp, int u, int l) {
  const int h = u & 31, gb = l >> 4, c = l & 15;
  bool dead = false;
  const f32x4 z4 = {0.f, 0.f, 0.f, 0.f};
  const u32x4 one8 = {0x3F803F80u, 0x3F803F80u, 0x3F803F80u, 0x3F803F80u};
  const u32x2 one4 = {0x3F803F80u, 0x3F803F80u};
  for (int s = u >> 5; s < 64; s += 4) {
    const u32 so = (u32)(s * 32 + h) * 8192u, sm = (u32)(s * 32 + h) * 4096u, lo = (u32)l * 16u;
    waitword<false>(ws, C_KV + (u32)(h * 64 + s) * 4u, 0xFFFFFFFFu, dead, 2);
    u32x4 Q[2][4], K[2][4], V[8], KM[4];
    u32x2 VM[8];
#pragma unroll
    for (int i = 0; i < 8; i++) {
      Q[i >> 2][i & 3] = ld16(ws, lo + (i & 3) * 1024u, OFF_QX + so + (i >> 2) * 4096u);
      K[i >> 2][i & 3] = ld16(ws, lo + (i & 3) * 1024u, OFF_KX + so + (i >> 2) * 4096u);
      V[i] = ld16(ws, lo + (i & 3) * 1024u, OFF_VX + so + (i >> 2) * 4096u);
    }
    SCHED();
    f32x4 s00 = z4, s01 = z4, s11 = z4;
#pragma unroll
    for (int ks = 0; ks < 4; ks++) {
      s00 = mfma32(as8(K[0][ks]), as8(Q[0][ks]), s00);
      s01 = mfma32(as8(K[0][ks]), as8(Q[1][ks]), s01);
      s11 = mfma32(as8(K[1][ks]), as8(Q[1][ks]), s11);
    }
#pragma unroll
    for (int i = 0; i < 4; i++)
      if (4 * gb + i > c) { s00[i] = NEG; s11[i] = NEG; }
    waitword<false>(ws, C_KM + (u32)((h * 64 + s) * 2) * 4u, 0xFFFFFFFFu, dead, 2);
    waitword<false>(ws, C_KM + (u32)((h * 64 + s) * 2 + 1) * 4u, 0xFFFFFFFFu, dead, 2);
#pragma unroll
    for (int i = 0; i < 4; i++) KM[i] = ld16(ws, lo + i * 1024u, OFF_KM + sm);
#pragma unroll
    for (int i = 0; i < 8; i++) VM[i] = (u32x2){ld4(ws, (u32)l * 4u + i * 256u, OFF_VM + sm), 0u};
    SCHED();
    f32x4 m0 = z4, m1 = z4;
#pragma unroll
    for (int ks = 0; ks < 4; ks++) { m0 = mfma32(as8(KM[ks]), as8(Q[0][ks]), m0); m1 = mfma32(as8(KM[ks]), as8(Q[1][ks]), m1); }
#pragma unroll
    for (int t = 0; t < 2; t++) {
      const f32x4 sM = t ? m1 : m0, sa = t ? s01 : s00;
      float m = fmaxf(fmaxf(sM[0], sM[1]), fmaxf(fmaxf(sa[0], sa[1]), fmaxf(sa[2], sa[3])));
      if (t) m = fmaxf(m, fmaxf(fmaxf(s11[0], s11[1]), fmaxf(s11[2], s11[3])));
      m = maxall(m) * SCL;
      float pM[4], p0[4], p1[4];
#pragma unroll
      for (int i = 0; i < 4; i++) {
        pM[i] = i < 2 ? __builtin_amdgcn_exp2f(__builtin_fmaf(sM[i], SCL, -m)) : 0.f;
        p0[i] = __builtin_amdgcn_exp2f(__builtin_fmaf(sa[i], SCL, -m));
        p1[i] = t ? __builtin_amdgcn_exp2f(__builtin_fmaf(s11[i], SCL, -m)) : 0.f;
      }
      const u32x2 PM = {pk(pM[0], pM[1]), 0u};
      const u32x4 PX = {pk(p0[0], p0[1]), pk(p0[2], p0[3]), pk(p1[0], p1[1]), pk(p1[2], p1[3])};
      const float rs = __builtin_amdgcn_rcpf(mfma16(one4, PM, z4)[0] + mfma32(as8(one8), as8(PX), z4)[0]);
#pragma unroll
      for (int ks = 0; ks < 4; ks++) {
        f32x4 oa = mfma32(as8(V[2 * ks]), as8(PX), z4) + mfma16(VM[2 * ks], PM, z4);
        f32x4 ob = mfma32(as8(V[2 * ks + 1]), as8(PX), z4) + mfma16(VM[2 * ks + 1], PM, z4);
        u32x4 o = {pk(oa[0] * rs, oa[1] * rs), pk(oa[2] * rs, oa[3] * rs), pk(ob[0] * rs, ob[1] * rs), pk(ob[2] * rs, ob[3] * rs)};
        st16(ws, o, lo + ks * 1024u, OFF_OF + (u32)(s * 2 + t) * 131072u + (u32)h * 4096u);
      }
    }
    WAITVM(0);
    if (l == 0) __hip_atomic_fetch_add(wsp + (C_OC + (u32)s * 128u) / 4, 1u, __ATOMIC_RELAXED, __HIP_MEMORY_SCOPE_AGENT);
  }
}

DI void out_role(lptr smem, rsrc_t ws, int grp, int xt, int sl, int w, int l, const char* Wo, const char* w2, const bf16* b2, bf16* out) {
  const u32 lds0 = (u32)(size_t)smem + (u32)w * 32768u;
  lptr wb = smem + w * 32768;
  const int gb = l >> 4, q = (l >> 2) & 3, p = l & 3, c = l & 15;
  bf16x8 A[2][32], A2[2][14];
  bool dead = false;
  {
    const u32 ro = (u32)(l >> 2) * 1024u + (u32)sl * 64u + (u32)(l & 3) * 16u;
    auto issue = [&](const char* W, int row0, int n, int buf) {
#pragma unroll
      for (int it = 0; it < 16; it++)
        if (it < n) dma16(W, ro + (u32)(row0 + it * 16) * 1024u, lds0 + buf * 16384 + it * 1024);
    };
    lptr b = wb + (4 * gb + q) * 64 + p * 16;
    issue(w2, w * 448, 16, 0);
    issue(w2, w * 448 + 256, 12, 1);
    WAITVM(12);
#pragma unroll
    for (int k = 0; k < 8; k++)
#pragma unroll
      for (int y = 0; y < 2; y++) { A2[y][k] = cat2(trr(b + k * 2048 + y * 8), trr(b + k * 2048 + 1024 + y * 8)); }
    asm volatile("s_waitcnt lgkmcnt(0)" ::: "memory");
    issue(Wo, w * 1024, 16, 0);
    WAITVM(16);
#pragma unroll
    for (int k = 0; k < 6; k++)
#pragma unroll
      for (int y = 0; y < 2; y++) { A2[y][8 + k] = cat2(trr(b + 16384 + k * 2048 + y * 8), trr(b + 16384 + k * 2048 + 1024 + y * 8)); }
#pragma unroll
    for (int r = 0; r < 4; r++) {
      asm volatile("s_waitcnt lgkmcnt(0)" ::: "memory");
      if (r < 3) { issue(Wo, w * 1024 + (r + 1) * 256, 16, (r + 1) & 1); WAITVM(16); } else { WAITVM(0); }
#pragma unroll
      for (int k = 0; k < 8; k++)
#pragma unroll
        for (int y = 0; y < 2; y++)
        { A[y][r * 8 + k] = cat2(trr(b + (r & 1) * 16384 + k * 2048 + y * 8), trr(b + (r & 1) * 16384 + k * 2048 + 1024 + y * 8)); }
    }
  }
  float bias[8];
#pragma unroll
  for (int i = 0; i < 8; i++) bias[i] = (float)b2[sl * 32 + 8 * gb + i];
#define LDO(v, s) ld16(ws, v, s)
  BAR();
  TL(5);
  const f32x4 z4 = {0.f, 0.f, 0.f, 0.f};
  const u32 lo = (u32)l * 16u;
  for (int s = grp; s < 64; s += 3) {
    f32x4 a0 = z4, a1 = z4, h0 = z4, h1 = z4;
    waitword<true>(ws, C_HC + (u32)s * 128u, 13u, dead, 2);
    {
      const u32 so = OFF_HF + (u32)(s * 2 + xt) * 57344u + (u32)w * 14336u;
      u32x4 F[14];
#pragma unroll
      for (int k = 0; k < 14; k++) F[k] = LDO(lo, so + k * 1024u);
      SCHED();
#pragma unroll
      for (int k = 0; k < 14; k++) { h0 = mfma32(A2[0][k], as8(F[k]), h0); h1 = mfma32(A2[1][k], as8(F[k]), h1); }
    }
    const float g = __builtin_bit_cast(float, ld4(ws, (u32)c * 4u, OFF_G + (u32)(s * 2 + xt) * 64u));
    waitword<true>(ws, C_OC + (u32)s * 128u, 31u, dead, 1);
    {
      const u32 so = OFF_OF + (u32)(s * 2 + xt) * 131072u + (u32)w * 32768u;
      u32x4 F[16];
#pragma unroll
      for (int k = 0; k < 16; k++) F[k] = LDO(lo, so + k * 1024u);
      SCHED();
#pragma unroll
      for (int k = 0; k < 32; k++) {
        a0 = mfma32(A[0][k], as8(F[k & 15]), a0); a1 = mfma32(A[1][k], as8(F[k & 15]), a1);
        if (k < 16) {
          asm volatile("" : "+a"(a0), "+a"(a1));
          F[k] = LDO(lo, so + 16384u + k * 1024u);
          SCHED();
        }
      }
    }
    lptr rb = smem + w * 4096 + lo;
    *(LAS f32x4*)(rb) = a0; *(LAS f32x4*)(rb + 1024) = a1; *(LAS f32x4*)(rb + 2048) = h0; *(LAS f32x4*)(rb + 3072) = h1;
    BAR();
    if (w == 0) {
#pragma unroll
      for (int k = 1; k < 4; k++) {
        a0 += *(LAS f32x4*)(rb + k * 4096); a1 += *(LAS f32x4*)(rb + k * 4096 + 1024);
        h0 += *(LAS f32x4*)(rb + k * 4096 + 2048); h1 += *(LAS f32x4*)(rb + k * 4096 + 3072);
      }
      const float g1 = rbf(1.f - g);
      float r[8];
#pragma unroll
      for (int i = 0; i < 8; i++) {
        const float av = rbf(i < 4 ? a0[i & 3] : a1[i & 3]);
        const float bv = rbf(rbf(i < 4 ? h0[i & 3] : h1[i & 3]) + bias[i]);
        r[i] = rbf(g * av) + rbf(g1 * bv);
      }
      u32x4 o = {pk(r[0], r[1]), pk(r[2], r[3]), pk(r[4], r[5]), pk(r[6], r[7])};
      *(u32x4*)(out + (size_t)(32 * s + 16 * xt + c) * 512 + sl * 32 + 8 * gb) = o;
    }
    BAR();
  }
}

DI void probe_role(lptr smem, rsrc_t ws, int w, int l) {
  const u32 base = ((w & 1) ? OFF_MEM : OFF_PART) + (u32)(w >> 1) * 131072u;
  u32 best[2] = {0xFFFFFFFFu, 0xFFFFFFFFu};
#pragma unroll 1
  for (int i = 0; i < 8; i++) {
#pragma unroll
    for (int sd = 0; sd < 2; sd++) {
      u64 t0 = __builtin_amdgcn_s_memtime();
      asm volatile("s_waitcnt lgkmcnt(0)" ::: "memory");
      st4(ws, 0xFFFFFFFFu, l == 0 ? 0u : OOB, base + (u32)i * 16384u + (u32)sd * 8192u);
      WAITVM(0);
      u64 t1 = __builtin_amdgcn_s_memtime();
      best[sd] = min(best[sd], (u32)(t1 - t0));
    }
  }
  if (l == 0) { *(LAS u32*)(smem + w * 8) = best[0]; *(LAS u32*)(smem + w * 8 + 4) = best[1]; }
  BAR();
  if (w == 0 && l == 0) {
    LAS u32* t = (LAS u32*)smem;
    const u32 a = min(t[0], t[4]), b = min(t[1], t[5]), c = min(t[2], t[6]), d = min(t[3], t[7]);
    st4(ws, (b < a ? 1u : 0u) | (d < c ? 2u : 0u), 0, C_SEL);
  }
  BAR();
}

__global__ __launch_bounds__(256, 1) void fused_kernel(const char* x, const bf16* mem0, const char* Wq, const char* Wk, const char* Wv,
                                                      const char* Wo, const char* w1, const bf16* b1, const char* w2, const bf16* b2,
                                                      const bf16* gw, bf16* out, char* wsb, int mask) {
  extern __shared__ __attribute__((aligned(16))) char smem_[];
  lptr smem = (lptr)smem_;
  const int b = blockIdx.x, xq = b & 7, r = b >> 3;
  const int w = __builtin_amdgcn_readfirstlane(threadIdx.x >> 6), l = threadIdx.x & 63;
  const rsrc_t ws = __builtin_amdgcn_make_buffer_rsrc(wsb, 0, (int)WS_BYTES, 0x00020000);
  u32* wsp = (u32*)wsb;
  TL(0);
  if (xq < 4 && r < 16) {
    if (mask & 2) chain_role(smem, ws, 8 * xq + (r >> 1), r & 1, w, l, Wq, Wk, Wv, Wo, mem0, wsb);
    return;
  }
  const int head = xq < 4 ? 8 * xq + ((r - 16) >> 1) : 8 * (xq - 4) + (r >> 2);
  const int j = xq < 4 ? 4 + (r & 1) : (r & 3);
  if (mask & 1) {
    if (b == 31 * 8) probe_role(smem, ws, w, l);
    qkv_role(smem, ws, head, j, w, l, x, Wq, Wk, Wv);
    TL(3);
    const int wi = (r - 16) * 8 + xq;
    if (r >= 16 && wi < 126) ffn1_role(smem, ws, wsp, wi / 9, wi % 9, w, l, x, w1, b1, gw);
  }
  TL(4);
  if (xq >= 4) {
    if (r >= 24) {
      if (mask & 4) attn_role(ws, wsp, ((xq - 4) * 8 + (r - 24)) * 4 + w, l);
    } else {
      const int g2 = (xq - 4) >> 1;
      if (mask & 8) out_role(smem, ws, r < 16 ? g2 + G3 : (G3 ? 0 : 2), xq & 1, r < 16 ? r : r - 16 + 8 * g2, w, l, Wo, w2, b2, out);
    }
  }
  TL(6);
}

#define SPLIT 0
extern "C" void kernel_launch(void* const* d_in, const int* in_sizes, int n_in, void* d_out, int out_size, void* d_ws,
                              size_t ws_size, hipStream_t stream) {
  char* base = (char*)(((size_t)d_ws + 2 * MB - 1) & ~(size_t)(2 * MB - 1));
  (void)hipFuncSetAttribute((const void*)fused_kernel, hipFuncAttributeMaxDynamicSharedMemorySize, LDS_BYTES);
  (void)hipMemsetAsync(base + INIT_LO, 0xFF, C_END - INIT_LO, stream);
  const int masks[4][4] = {{15, 0, 0, 0}, {1, 2, 4, 8}, {1, 6, 8, 0}, {1, 14, 0, 0}};
  for (int ph = 0; ph < 4 && masks[SPLIT][ph]; ph++)
    fused_kernel<<<256, 256, LDS_BYTES, stream>>>((const char*)d_in[0], (const bf16*)d_in[1], (const char*)d_in[2], (const char*)d_in[3],
                                                  (const char*)d_in[4], (const char*)d_in[5], (const char*)d_in[8], (const bf16*)d_in[9],
                                                  (const char*)d_in[10], (const bf16*)d_in[11], (const bf16*)d_in[12], (bf16*)d_out, base,
                                                  masks[SPLIT][ph]);
}
```

```cpp
#include <hip/hip_runtime.h>
#include <hip/hip_bf16.h>

typedef unsigned u32;
typedef unsigned long long u64;
typedef __bf16 bf16;
typedef bf16 bf16x2 __attribute__((ext_vector_type(2)));
typedef bf16 bf16x8 __attribute__((ext_vector_type(8)));
typedef bf16 bf16x16 __attribute__((ext_vector_type(16)));
typedef short s16x4 __attribute__((ext_vector_type(4)));
typedef short s16x8 __attribute__((ext_vector_type(8)));
typedef short s16x16 __attribute__((ext_vector_type(16)));
typedef float f32x2 __attribute__((ext_vector_type(2)));
typedef float f32x4 __attribute__((ext_vector_type(4)));
typedef u32 u32x2 __attribute__((ext_vector_type(2)));
typedef u32 u32x4 __attribute__((ext_vector_type(4)));
typedef __amdgpu_buffer_rsrc_t rsrc_t;
#define LAS __attribute__((address_space(3)))
typedef LAS char* lptr;
#define DI __device__ __forceinline__

constexpr u32 MB = 1u << 20;
constexpr u32 OFF_PART = 0;
constexpr u32 OFF_MEM = 94 * MB;
constexpr u32 OFF_CTRL = 96 * MB;
constexpr u32 C_SEL = OFF_CTRL;
constexpr u32 C_EP = OFF_CTRL + 256;
constexpr u32 C_RDY = OFF_CTRL + 512;
constexpr u32 C_KV = OFF_CTRL + 1024;
constexpr u32 C_KM = C_KV + 8192;
constexpr u32 C_OC = C_KM + 16384;
constexpr u32 C_HC = C_OC + 8192;
constexpr u32 C_END = C_HC + 8192;
constexpr u32 OFF_QX = 4 * MB, OFF_KX = 20 * MB, OFF_VX = 36 * MB;
constexpr u32 OFF_KM = 52 * MB, OFF_VM = 60 * MB, OFF_OF = 68 * MB, OFF_HF = 84 * MB, OFF_G = 92 * MB;
constexpr u32 WS_BYTES = 97 * MB;
DI u32 slot_off(int s) { const u32 r = (u32)(63 - s); return (r >> 1) * 65536u + (r & 1u) * 2048u; }
DI u32 tile_off(int t) { return (u32)(t >> 2) * 16384u + (u32)((t >> 1) & 1) * 4096u + (u32)(t & 1) * 1024u; }
constexpr int LDS_BYTES = 160 * 1024;
constexpr int SPIN_MAX = 1 << 15;
constexpr int AUX = 16;

DI u32x4 ld16(rsrc_t r, u32 v, u32 s) { return __builtin_amdgcn_raw_buffer_load_b128(r, v, s, AUX); }
DI u32x2 ld8(rsrc_t r, u32 v, u32 s) { return __builtin_amdgcn_raw_buffer_load_b64(r, v, s, AUX); }
DI u32 ld4(rsrc_t r, u32 v, u32 s) { return __builtin_amdgcn_raw_buffer_load_b32(r, v, s, AUX); }
DI void st16(rsrc_t r, u32x4 x, u32 v, u32 s) {
  __builtin_amdgcn_raw_buffer_store_b128(x, r, v, s, AUX);
  asm volatile("s_nop 1" ::"v"(x) : "memory");
}
DI void st8(rsrc_t r, u32x2 x, u32 v, u32 s) { __builtin_amdgcn_raw_buffer_store_b64(x, r, v, s, AUX); }
DI void st4(rsrc_t r, u32 x, u32 v, u32 s) { __builtin_amdgcn_raw_buffer_store_b32(x, r, v, s, AUX); }
#define MEMBAR() asm volatile("" ::: "memory")
#define WAITVM(n) asm volatile("s_waitcnt vmcnt(" #n ")" ::: "memory")
#define WAITVM0() do { MEMBAR(); __builtin_amdgcn_s_waitcnt(0x0F70); MEMBAR(); } while (0)
#define BAR() asm volatile("s_waitcnt lgkmcnt(0)\n\ts_barrier" ::: "memory")
#define SCHED() __builtin_amdgcn_sched_barrier(0)
#define PINA(x) asm volatile("" : "+a"(x))
#define PINV(x) asm volatile("" : "+v"(x))
constexpr u32 OOB = 0x80000000u;

DI void dma16(const char* sbase, u32 voff, u32 ldsdst) {
  u32 keep;
  asm volatile("s_mov_b32 %0, m0\n\ts_mov_b32 m0, %3\n\ts_nop 0\n\tglobal_load_lds_dwordx4 %1, %2\n\ts_mov_b32 m0, %0"
               : "=&s"(keep) : "v"(voff), "s"(sbase), "s"(ldsdst) : "memory");
}
DI s16x4 trr(lptr p) { return __builtin_amdgcn_ds_read_tr16_b64_v4i16((LAS s16x4*)p); }
DI bf16x8 cat2(s16x4 a, s16x4 b) { return __builtin_bit_cast(bf16x8, __builtin_shufflevector(a, b, 0, 1, 2, 3, 4, 5, 6, 7)); }
DI bf16x16 cat4(s16x4 a, s16x4 b, s16x4 c, s16x4 d) {
  s16x8 x = __builtin_shufflevector(a, b, 0, 1, 2, 3, 4, 5, 6, 7), y = __builtin_shufflevector(c, d, 0, 1, 2, 3, 4, 5, 6, 7);
  return __builtin_bit_cast(bf16x16, __builtin_shufflevector(x, y, 0, 1, 2, 3, 4, 5, 6, 7, 8, 9, 10, 11, 12, 13, 14, 15));
}
DI u32 pk(float lo, float hi) { f32x2 v = {lo, hi}; return __builtin_bit_cast(u32, __builtin_convertvector(v, bf16x2)); }
DI u32 pk2(f32x2 v) { return __builtin_bit_cast(u32, __builtin_convertvector(v, bf16x2)); }
DI float rbf(float x) { return (float)(bf16)x; }
DI bf16x8 as8(u32x4 x) { return __builtin_bit_cast(bf16x8, x); }
DI f32x4 mfma32(bf16x8 a, bf16x8 b, f32x4 c) { return __builtin_amdgcn_mfma_f32_16x16x32_bf16(a, b, c, 0, 0, 0); }
DI f32x4 mfma16(u32x2 a, u32x2 b, f32x4 c) {
  return __builtin_amdgcn_mfma_f32_16x16x16bf16_1k(__builtin_bit_cast(s16x4, a), __builtin_bit_cast(s16x4, b), c, 0, 0, 0);
}
DI f32x4 smf(u32x4 a, bf16x16 b, f32x4 c, int idx) { return __builtin_amdgcn_smfmac_f32_16x16x64_bf16(as8(a), b, c, idx, 0, 0); }
DI float comb(float x, float y) {
  auto r = __builtin_amdgcn_permlane32_swap(__builtin_bit_cast(u32, x), __builtin_bit_cast(u32, y), false, false);
  return __builtin_bit_cast(float, (u32)r[0]) + __builtin_bit_cast(float, (u32)r[1]);
}
DI float maxall(float m) {
  auto r = __builtin_amdgcn_permlane32_swap(__builtin_bit_cast(u32, m), __builtin_bit_cast(u32, m), false, false);
  m = fmaxf(__builtin_bit_cast(float, (u32)r[0]), __builtin_bit_cast(float, (u32)r[1]));
  auto q = __builtin_amdgcn_permlane16_swap(__builtin_bit_cast(u32, m), __builtin_bit_cast(u32, m), false, false);
  return fmaxf(__builtin_bit_cast(float, (u32)q[0]), __builtin_bit_cast(float, (u32)q[1]));
}
DI f32x4 zero4() {
  u64 a, b;
  asm volatile("v_mov_b64 %0, 0" : "=v"(a));
  asm volatile("v_mov_b64 %0, 0" : "=v"(b));
  typedef u64 u64x2 __attribute__((ext_vector_type(2)));
  u64x2 r = {a, b};
  return __builtin_bit_cast(f32x4, r);
}
DI u32 umax8(u32x4 a, u32x4 b) { return max(max(max(a[0], a[1]), max(a[2], a[3])), max(max(b[0], b[1]), max(b[2], b[3]))); }
DI bool anyl(bool p) { return __builtin_amdgcn_ballot_w64(p) != 0; }
DI void waitflag(rsrc_t r, u32 v, u32 s, u64 lanes, u32 msk, u32 val, bool& dead, int sleep) {
  int n = 0;
#pragma unroll 1
  while (!dead) {
    MEMBAR();
    const u32 x = ld4(r, v, s);
    if ((__builtin_amdgcn_ballot_w64((x & msk) != val) & lanes) == 0) break;
    asm volatile("" : "+s"(n));
    if (++n > SPIN_MAX) dead = true;
    for (int i = 0; i < sleep; i++) __builtin_amdgcn_s_sleep(4);
  }
  MEMBAR();
}
DI u32 gettag(const char* wsb) {
  u32 t = *(const __attribute__((address_space(4))) u32*)(wsb + C_EP);
  t = (t + 4u) & ~3u;
  asm volatile("" : "+s"(t));
  return t;
}
constexpr float NEG = -3.0e38f;
constexpr float SCL = 0.08838834764831845f * 1.4426950408889634f;

#define STAMP(i)
#define TL(slot)

DI void chain_role(lptr smem, rsrc_t ws, int h, int a, int w, int l, const char* Wq, const char* Wk, const char* Wv,
                   const char* Wo, const bf16* mem0, const char* wsb) {
  const int gb = l >> 4, q = (l >> 2) & 3, p = l & 3, c = l & 15;
  const u32 lds0 = (u32)(size_t)smem;
  bf16x16 Bq[2][8], Bk[2][8], Bv[8], Bo[8];
  bool dead = false;
  const int ci = 2 * h + a;
  const int rho = 2 * ci + (w & 1);
  const int rt = rho >> 4, gs = (rho >> 2) & 3, ru = rho & 3;
  const u32 rpiece = tile_off(rt) + (u32)ru * 256u + (u32)gs * 64u;
  const u32 rpub4 = c < 4 ? rpiece + (u32)(c & 3) * 16u + (u32)gb * 4u : OOB;
  const u32 rblk0 = (u32)(rho >> 1) * 16384u + (u32)(rho & 1) * 4096u;
  const u32 lane0 = l == 0 ? 0u : OOB;
  const u32x4 ones = {~0u, ~0u, ~0u, ~0u};
  u32 selw, rdy, flg, tag;
  {
    const int d0 = 64 * rt + 16 * ((c >> 1) & 1) + 4 * ru + gb, r0 = 2 * gs + (c & 1);
    const unsigned short* m0 = (const unsigned short*)mem0;
    const u32 mlo = m0[r0 * 512 + d0], mhi = m0[r0 * 512 + d0 + 32];
    const u32 cp = (u32)(w >> 1) * 8192u;
    SCHED();
#pragma unroll
    for (int k = 0; k < 8; k++) st16(ws, ones, rblk0 + (u32)l * 16u + (k & 3) * 1024u, OFF_PART + (k >> 2) * MB + cp);
#pragma unroll
    for (int k = 0; k < 4; k++) {
      const int s = 1 + k * 16 + (l >> 2);
      st16(ws, ones, s < 64 ? slot_off(s) + rpiece + (u32)(l & 3) * 16u : OOB, OFF_MEM + cp);
    }
    SCHED();
    const u32 bx = (u32)((l & 15) ^ ((l >> 5) << 1));
    const u32 rowoff = (u32)(64 * w + (l >> 4)) * 8192u + (u32)h * 256u;
    auto issueQK = [&](const char* W, int half, int buf) {
#pragma unroll
      for (int it = 0; it < 16; it++) {
        const u32 cit = (u32)((((it >> 3) & 1) << 3) | (((it >> 2) & 1) << 2));
        dma16(W, rowoff + (u32)(half * 256 + it * 4) * 8192u + ((bx ^ cit) << 4), lds0 + buf * 65536 + (w * 16 + it) * 1024);
      }
    };
    auto issueV = [&](int buf) {
      const u32 bv = (u32)((l & 7) ^ ((l >> 5) << 1));
      const u32 ro = (u32)(128 * w + ((l >> 3) & 1) * 32 + (l >> 4)) * 8192u + (u32)h * 256u + (u32)a * 128u;
#pragma unroll
      for (int it = 0; it < 16; it++)
        dma16(Wv, ro + (u32)((it >> 3) * 64 + (it & 7) * 4) * 8192u + ((bv ^ (u32)(((it >> 2) & 1) << 2)) << 4),
              lds0 + buf * 65536 + (w * 16 + it) * 1024);
    };
    auto issueO = [&](int buf) {
      const u32 ro = (u32)(h * 128 + a * 64 + w * 16) * 1024u;
#pragma unroll
      for (int it = 0; it < 16; it++) {
        const u32 f = (u32)((((it >> 3) & 1) << 2) | (((it >> 2) & 1) << 1) | (it & 1));
        dma16(Wo, ro + (u32)it * 1024u + (((u32)l ^ (f << 1)) << 4), lds0 + buf * 65536 + (w * 16 + it) * 1024);
      }
    };
    const int rowl = 32 * (q & 1) + 16 * (q >> 1) + 2 * gb;
    const int fq = ((q & 1) << 2) | ((q >> 1) << 1) | (gb & 1);
    auto readQK = [&](bf16x16(&B)[2][8], int half, int buf) {
#pragma unroll
      for (int y = 0; y < 2; y++) {
        lptr b = smem + buf * 65536 + rowl * 256 + (((4 * w + 2 * y + (p >> 1)) ^ (fq << 1)) << 4) + (p & 1) * 8;
#pragma unroll
        for (int t = 0; t < 4; t++)
        {
          B[y][half * 4 + t] = cat4(trr(b + t * 16384), trr(b + t * 16384 + 256), trr(b + t * 16384 + 2048), trr(b + t * 16384 + 2304));
          PINA(B[y][half * 4 + t]);
        }
      }
    };
    issueQK(Wq, 0, 0);
    issueQK(Wq, 1, 1);
    SCHED();
    tag = gettag(wsb);
    WAITVM(16); BAR(); readQK(Bq, 0, 0); BAR();
    SCHED();
    st4(ws, mlo | (mhi << 16), rpub4, OFF_MEM + cp + slot_off(0));
    SCHED();
    issueQK(Wk, 0, 0);
    WAITVM(16); BAR();
    st4(ws, tag, w == 0 ? lane0 : OOB, C_RDY + (u32)ci * 4u);
    SCHED();
    readQK(Bq, 1, 1); BAR(); issueQK(Wk, 1, 1);
    WAITVM(16); BAR(); readQK(Bk, 0, 0); BAR(); issueV(0);
    WAITVM(16); BAR(); readQK(Bk, 1, 1); BAR(); issueO(1);
    WAITVM(16); BAR();
    {
      const int f2 = ((q >> 1) << 1) | (gb & 1);
      lptr b = smem + (16 * (q >> 1) + 2 * gb) * 256 + (q & 1) * 128 + (((2 * w + (p >> 1)) ^ (f2 << 1)) << 4) + (p & 1) * 8;
#pragma unroll
      for (int t = 0; t < 8; t++)
      {
        Bv[t] = cat4(trr(b + t * 8192), trr(b + t * 8192 + 256), trr(b + t * 8192 + 2048), trr(b + t * 8192 + 2304));
        PINV(Bv[t]);
      }
    }
    SCHED();
    selw = ld4(ws, 0, C_SEL); rdy = ld4(ws, (u32)l * 4u, C_RDY); flg = ld4(ws, 0, C_KV + (u32)(h * 64) * 4u);
    SCHED();
    WAITVM0(); BAR();
    {
      const int fo = ((q >> 1) << 2) | ((gb & 1) << 1) | (q & 1);
      const int er = 16 * (gb >> 1) + 8 * (q >> 1) + 4 * (gb & 1) + (q & 1);
#pragma unroll
      for (int T = 0; T < 8; T++) {
        lptr b = smem + 65536 + er * 1024 + (((16 * w + 2 * T + (p >> 1)) ^ (fo << 1)) << 4) + (p & 1) * 8;
        Bo[T] = cat4(trr(b), trr(b + 2048), trr(b + 32768), trr(b + 32768 + 2048));
        PINV(Bo[T]);
      }
    }
    BAR();
  }
  TL(1);
  if (anyl(rdy != tag)) waitflag(ws, (u32)l * 4u, C_RDY, ~0ull, ~0u, tag, dead, 1);
  if (anyl((selw & ~3u) != tag)) { waitflag(ws, 0, C_SEL, 1ull, ~3u, tag, dead, 1); selw = ld4(ws, 0, C_SEL); }
  selw = __builtin_amdgcn_readfirstlane(selw);
  flg = __builtin_amdgcn_readfirstlane(flg);
  const u32 sel0 = (selw & 1u) * 8192u, sel1 = ((selw >> 1) & 1u) * 8192u;
  const u32 rgat = rblk0 + sel0 + (u32)(4 * gb + (c & 3)) * 64u + (u32)(c >> 2) * 16u;
  constexpr u32 GK = 1024u;
  const u32 pst = (u32)(w * 16 + (l >> 3)) * 16384u + (u32)((l >> 2) & 1) * 4096u + sel0 + (u32)ci * 64u + (u32)(l & 3) * 16u;
  const u32 kvlane = (u32)l * 16u;
  lptr Lq = smem, Lk = smem + 6144, Lx = smem + 12288, Lv = smem + 20480, La = smem + 24576, Lo = smem + 49152;
#pragma unroll
  for (int i = 0; i < 3; i++) *(LAS u32x4*)(smem + i * 4096 + w * 1024 + l * 16) = (u32x4){0u, 0u, 0u, 0u};
  auto fetch_kv = [&](int sn) {
    if ((flg & ~3u) != tag) waitflag(ws, 0, C_KV + (u32)(h * 64 + sn) * 4u, 1ull, ~3u, tag, dead, 1);
    const u32 so = (u32)(sn * 32 + h) * 8192u;
    u32x4 t[6];
    const u32 fl = ld4(ws, 0, C_KV + (u32)(h * 64 + sn + 1) * 4u);
    SCHED();
#pragma unroll
    for (int i = 0; i < 4; i++) t[i] = ld16(ws, kvlane, OFF_KX + so + (u32)(w - 2) * 4096u + i * 1024u);
#pragma unroll
    for (int i = 0; i < 2; i++) t[4 + i] = ld16(ws, kvlane, OFF_VX + so + (u32)(4 * a + 2 * (w - 2) + i) * 1024u);
    SCHED();
    flg = __builtin_amdgcn_readfirstlane(fl);
#pragma unroll
    for (int i = 0; i < 4; i++) *(LAS u32x4*)(Lx + (w - 2) * 4096 + i * 1024 + l * 16) = t[i];
#pragma unroll
    for (int i = 0; i < 2; i++) *(LAS u32x4*)(Lv + (w - 2) * 2048 + i * 1024 + l * 16) = t[4 + i];
  };
  if (w >= 2) fetch_kv(0);
  WAITVM0();
  BAR();
  const u32 kmoff = (l >> 5) == a ? kvlane + (u32)w * 1024u : OOB;
  const u32 flgoff = w == 2 ? lane0 : OOB;
  const int idx = (l & 2) ? 0xEEEE : 0x4444;
  const int wq = (4 * w + (c >> 2)) * 384 + (c & 3) * 32 + gb * 8;
  const int rq = (4 * w + gb) * 384 + q * 32 + p * 8;
  const bool up = (c & 2) != 0;
  u32 pmask = up ? 0u : ~0u;
  u32 tagv = tag;
  PINV(pmask); PINV(tagv);
  lptr Ls = smem + 53248;
  const int orow = 2 * (c >> 2) + (c & 1);
  u32x4 selB;
  {
    const int j = c & 7;
    const u32 one = (j & 1) ? 0x3F800000u : 0x00003F80u;
    selB[0] = (j >> 1) == 0 ? one : 0u; selB[1] = (j >> 1) == 1 ? one : 0u;
    selB[2] = (j >> 1) == 2 ? one : 0u; selB[3] = (j >> 1) == 3 ? one : 0u;
  }
  u32x4 one8 = {0x3F803F80u, 0x3F803F80u, 0x3F803F80u, 0x3F803F80u};
  PINV(one8); PINV(selB);
#define SM2(d0, d1, B, t)                                                                                            \
  asm volatile("v_smfmac_f32_16x16x64_bf16 %0, %2, %3, %5\n\tv_smfmac_f32_16x16x64_bf16 %1, %2, %4, %5"              \
               : "+v"(d0), "+v"(d1) : "v"(A[t]), "a"(B[0][t]), "a"(B[1][t]), "v"(idx));                              \
  SCHED()
#define VSTEP(t)                                                                                                     \
  asm volatile("v_smfmac_f32_16x16x64_bf16 %0, %1, %2, %3" : "+v"(v0) : "v"(A[t]), "v"(Bv[t]), "v"(idx));            \
  SCHED()
#define MF0(D, F, G) asm volatile("v_mfma_f32_16x16x32_bf16 %0, %1, %2, 0" : "=&v"(D) : "v"(F), "v"(G)); SCHED()
#define MF1(D, F, G) asm volatile("v_mfma_f32_16x16x32_bf16 %0, %1, %2, %0" : "+v"(D) : "v"(F), "v"(G)); SCHED()
#define FOLD(D) pk2(__builtin_shufflevector(D, D, 0, 1) + __builtin_shufflevector(D, D, 2, 3))
#define UNDEF(x) asm volatile("" : "=v"(x))
#define NAP(n) do { if (n) __builtin_amdgcn_s_sleep(n); } while (0)
#define KVD 1
#define D1 10
#define D2 8
#define D3 10
#define D4 0

#define LS(i)
#define RT(i)
  TL(2);
  for (int s = 0; s < 64; s++) {
    const u32 mslot = OFF_MEM + sel1 + slot_off(s) + tile_off(2 * w);
    STAMP(0);
    RT(3);
    u32x4 A[8];
    u32x4 g0 = ld16(ws, kvlane, mslot); SCHED();
    u32x4 g1 = ld16(ws, kvlane, mslot + 1024u); SCHED();
    f32x4 q0 = zero4(), q1 = zero4(), k0 = zero4(), k1 = zero4(), v0 = zero4();
    u32 parS = OFF_PART + (u32)(s & 1) * MB, parT = parS + 8 * 16384u, rstS = parS ^ MB, rstT = parT ^ MB;
    u32 kmS = OFF_KM + (u32)(s * 32 + h) * 4096u, vmS = kmS + (OFF_VM - OFF_KM), pubS = OFF_MEM + sel1 + slot_off(s + 1);
    u32 flS = C_KM - 8u + (u32)(h * 128 + a) * 4u + (u32)s * 8u, flV = s > 0 ? flgoff : OOB;
    asm volatile("" : "+s"(parS), "+s"(parT), "+s"(rstS), "+s"(rstT), "+s"(kmS), "+s"(vmS), "+s"(pubS), "+s"(flS), "+v"(flV));
    SCHED();
    *(LAS u32x4*)(La + w * 2048 + l * 16) = g0;
    *(LAS u32x4*)(La + w * 2048 + 1024 + l * 16) = g1;
    SCHED();
    if (__builtin_expect(anyl(umax8(g0, g1) == 0xFFFFFFFFu), 0)) {
      int n = 0;
      asm volatile("" : "+v"(g0), "+v"(g1));
#pragma unroll 1
      while (anyl(umax8(g0, g1) == 0xFFFFFFFFu) && !dead) {
        MEMBAR();
        g0 = ld16(ws, kvlane, mslot);
        g1 = ld16(ws, kvlane, mslot + 1024u);
        WAITVM0();
        if (++n > SPIN_MAX) dead = true;
      }
      *(LAS u32x4*)(La + w * 2048 + l * 16) = g0;
      *(LAS u32x4*)(La + w * 2048 + 1024 + l * 16) = g1;
    }
    STAMP(1);
    RT(0);
    LS(0);
    BAR();
    LS(1);
#pragma unroll
    for (int t = 0; t < 8; t++) { A[t] = *(LAS u32x4*)(La + t * 1024 + l * 16); SCHED(); }
    SM2(q0, q1, Bq, 0);
    __builtin_amdgcn_raw_buffer_store_b128(ones, ws, pst, rstS, AUX); SCHED();
    SM2(q0, q1, Bq, 1); SM2(q0, q1, Bq, 2);
    __builtin_amdgcn_raw_buffer_store_b128(ones, ws, pst, rstT, AUX); SCHED();
    SM2(q0, q1, Bq, 3); SM2(q0, q1, Bq, 4);
    st4(ws, tagv, flV, flS); SCHED();
    SM2(q0, q1, Bq, 5); SM2(q0, q1, Bq, 6); SM2(q0, q1, Bq, 7);
    LS(2);
    SM2(k0, k1, Bk, 0);
    const u32x4 X0 = *(LAS u32x4*)(Lx + w * 1024 + l * 16), X1 = *(LAS u32x4*)(Lx + 4096 + w * 1024 + l * 16);
    SCHED();
    SM2(k0, k1, Bk, 1);
    asm volatile("" : "+v"(q0), "+v"(q1));
    {
      const u32 a0 = FOLD(q0), a1 = FOLD(q1);
      *(LAS u32*)(Lq + wq) = a0; *(LAS u32*)(Lq + wq + 4) = a0; SCHED();
      SM2(k0, k1, Bk, 2);
      *(LAS u32*)(Lq + wq + 128) = a1; *(LAS u32*)(Lq + wq + 132) = a1; SCHED();
    }
    SM2(k0, k1, Bk, 3);
    const u32x2 vxa = *(LAS u32x2*)(Lv + w * 1024 + l * 16), vxb = *(LAS u32x2*)(Lv + w * 1024 + l * 16 + 8);
    SCHED();
    SM2(k0, k1, Bk, 4);
    const bf16x8 Fq = cat2(trr(Lq + rq), trr(Lq + rq + 128));
    SCHED();
    SM2(k0, k1, Bk, 5); SM2(k0, k1, Bk, 6); SM2(k0, k1, Bk, 7);
    LS(3);
    f32x4 sM, s0, s1;
    MF0(s0, X0, Fq);
    MF0(s1, X1, Fq);
    VSTEP(0);
    asm volatile("" : "+v"(k0), "+v"(k1));
    { const u32 a0 = FOLD(k0), a1 = FOLD(k1); *(LAS u32*)(Lk + wq) = a0; *(LAS u32*)(Lk + wq + 128) = a1; SCHED(); }
    const bf16x8 Fk = cat2(trr(Lk + rq), trr(Lk + rq + 128));
    SCHED();
    VSTEP(1);
    asm volatile("" : "+v"(s0), "+v"(s1));
    {
      const f32x4 x = {up ? s1[0] : s0[0], up ? s1[1] : s0[1], up ? s1[2] : s0[2], up ? s1[3] : s0[3]};
      SCHED();
      VSTEP(2);
      *(LAS f32x4*)(Ls + w * 1536 + l * 16) = x;
      SCHED();
    }
    VSTEP(3); VSTEP(4); VSTEP(5);
    MF0(sM, Fk, Fq);
    VSTEP(6); VSTEP(7);
    st16(ws, __builtin_bit_cast(u32x4, Fk), kmoff, kmS);
    asm volatile("" : "+v"(sM));
    *(LAS f32x2*)(Ls + w * 1536 + 1024 + l * 8) = __builtin_shufflevector(sM, sM, 0, 1);
    SCHED();
    LS(4);
    BAR();
    LS(5);
    f32x4 xs[4];
    f32x2 ms[4];
#pragma unroll
    for (int j = 0; j < 4; j++) { xs[j] = *(LAS f32x4*)(Ls + j * 1536 + l * 16); ms[j] = *(LAS f32x2*)(Ls + j * 1536 + 1024 + l * 8); }
    SCHED();
    asm volatile("" : "+v"(v0));
    const u32 vm = FOLD(v0);
    st4(ws, vm, (u32)l * 4u + (u32)(4 * a + w) * 256u, vmS);
    if (s == 63) break;
    f32x4 D[8];
    D[0] = zero4(); D[1] = zero4(); D[2] = zero4(); D[3] = zero4();
    SCHED();
    const f32x4 sx = (xs[0] + xs[1]) + (xs[2] + xs[3]);
    const f32x2 sm = (ms[0] + ms[1]) + (ms[2] + ms[3]);
    float m, mt;
    LS(6);
    asm volatile("v_max3_f32 %0, %2, %3, %4\n\tv_max3_f32 %1, %5, %6, %7\n\tv_max_f32 %0, %0, %1\n\ts_nop 1\n\t"
                 "v_max_f32_dpp %1, %0, %0 quad_perm:[2,3,0,1] row_mask:0xf bank_mask:0xf\n\t"
                 "v_max_f32_dpp %0, %0, %0 quad_perm:[2,3,0,1] row_mask:0xf bank_mask:0xf\n\ts_nop 1\n\t"
                 "v_permlane32_swap_b32 %0, %1\n\tv_max_f32 %0, %0, %1\n\tv_mov_b32 %1, %0\n\ts_nop 1\n\t"
                 "v_permlane16_swap_b32 %0, %1\n\tv_max_f32 %0, %0, %1"
                 : "=&v"(m), "=&v"(mt) : "v"(sx[0]), "v"(sx[1]), "v"(sx[2]), "v"(sx[3]), "v"(sm[0]), "v"(sm[1]));
    LS(7);
    m *= -SCL;
    const f32x2 m2 = {m, m}, scl2 = {SCL, SCL};
#define EXP(t) pk(__builtin_amdgcn_exp2f(t[0]), __builtin_amdgcn_exp2f(t[1]))
    const f32x2 tM = __builtin_elementwise_fma(sm, scl2, m2), t0 = __builtin_elementwise_fma(__builtin_shufflevector(sx, sx, 0, 1), scl2, m2),
                t1 = __builtin_elementwise_fma(__builtin_shufflevector(sx, sx, 2, 3), scl2, m2);
    SCHED();
    const u32x4 P = {EXP(tM) & pmask, 0u, EXP(t0), EXP(t1)};
    const u32x4 VA = {vm, 0u, vxa[0], vxa[1]};
    f32x4 sum, oa, ob;
    asm volatile("s_nop 1\n\tv_mfma_f32_16x16x32_bf16 %0, %3, %4, 0\n\tv_mfma_f32_16x16x16_bf16 %2, %6, %7, 0\n\t"
                 "v_mfma_f32_16x16x32_bf16 %1, %5, %4, 0"
                 : "=&v"(sum), "=&v"(oa), "=&v"(ob) : "v"(one8), "v"(P), "v"(VA), "v"(vxb), "v"(__builtin_shufflevector(P, P, 2, 3)));
    {
      float rs;
      f32x4 o;
      asm volatile("v_add_f32_dpp %0, %1, %1 quad_perm:[2,3,0,1] row_mask:0xf bank_mask:0xf\n\ts_nop 0\n\tv_rcp_f32 %0, %0" : "=&v"(rs) : "v"(sum[0]));
      D[4] = zero4(); D[5] = zero4(); D[6] = zero4();
      asm volatile("v_add_f32_dpp %0, %4, %8 quad_perm:[2,3,0,1] row_mask:0xf bank_mask:0xf\n\t"
                   "v_add_f32_dpp %1, %5, %9 quad_perm:[2,3,0,1] row_mask:0xf bank_mask:0xf\n\t"
                   "v_add_f32_dpp %2, %6, %10 quad_perm:[2,3,0,1] row_mask:0xf bank_mask:0xf\n\t"
                   "v_add_f32_dpp %3, %7, %11 quad_perm:[2,3,0,1] row_mask:0xf bank_mask:0xf"
                   : "=&v"(o[0]), "=&v"(o[1]), "=&v"(o[2]), "=&v"(o[3])
                   : "v"(ob[0]), "v"(ob[1]), "v"(ob[2]), "v"(ob[3]), "v"(oa[0]), "v"(oa[1]), "v"(oa[2]), "v"(oa[3]));
      const f32x2 r2 = {rs, rs};
      u32x2 oo = {pk2(__builtin_shufflevector(o, o, 0, 1) * r2), pk2(__builtin_shufflevector(o, o, 2, 3) * r2)};
      *(LAS u32x2*)(Lo + (orow + ((c & 2) << 2)) * 144 + w * 32 + gb * 8) = oo;
    }
    STAMP(4);
    LS(8);
    WAITVM(2);
    BAR();
    STAMP(5);
    LS(9);
    const u32x4 Ao = *(LAS u32x4*)(Lo + orow * 144 + gb * 32 + ((c >> 1) & 1) * 16);
    SCHED();
    D[7] = zero4();
    SCHED();
#define OST(T) asm volatile("v_smfmac_f32_16x16x64_bf16 %0, %1, %2, %3" : "+v"(D[T]) : "v"(Ao), "v"(Bo[T]), "v"(idx)); SCHED()
#define OFD(P_, i, T) asm volatile("" : "+v"(D[T])); P_[i] = FOLD(D[T]); SCHED()
    {
      u32x4 pv, pw;
      OST(0); OST(1); OST(2); OST(3);
      OFD(pv, 0, 0); OST(4);
      OFD(pv, 1, 1); OST(5);
      OFD(pv, 2, 2); OST(6);
      OFD(pv, 3, 3); OST(7);
      st16(ws, pv, pst, parS); SCHED();
      OFD(pw, 0, 4); OFD(pw, 1, 5);
      asm volatile("s_nop 1" ::: "memory");
      OFD(pw, 2, 6); OFD(pw, 3, 7);
      st16(ws, pw, pst, parT);
    }
    STAMP(6);
    RT(1);
    LS(10);
    if (w < 2) {
      u32x4 g[4], gl[4];
      NAP(D1);
      SCHED();
#pragma unroll
      for (int k = 0; k < 4; k++) { g[k] = ld16(ws, rgat, parS + k * GK); SCHED(); }
      NAP(D2);
      SCHED();
#pragma unroll
      for (int k = 0; k < 4; k++) { gl[k] = ld16(ws, rgat, parS + k * GK); SCHED(); }
      float tot;
#define REDUCE(G)                                                                                                    \
  {                                                                                                                  \
    f32x4 acc;                                                                                                       \
    MF0(acc, G[0], selB); MF1(acc, G[1], selB); MF1(acc, G[2], selB); MF1(acc, G[3], selB);                           \
    asm volatile("s_nop 7" : "+v"(acc));                                                                             \
    const f32x2 t_ = __builtin_shufflevector(acc, acc, 0, 1) + __builtin_shufflevector(acc, acc, 2, 3); tot = t_[0] + t_[1];                                                               \
  }
      REDUCE(g);
      if (__builtin_expect(anyl(tot != tot), 0)) {
        REDUCE(gl);
        int n = 0;
#pragma unroll 1
        while (anyl(tot != tot) && !dead) {
          MEMBAR();
#pragma unroll
          for (int k = 0; k < 4; k++) { g[k] = ld16(ws, rgat, parS + k * GK); SCHED(); }
          REDUCE(g);
          if (++n > SPIN_MAX) dead = true;
        }
      }
      STAMP(8);
      RT(2);
      const float hi = __builtin_bit_cast(float, __builtin_amdgcn_update_dpp(0, __builtin_bit_cast(int, tot), 0x104, 0xf, 0xf, true));
      st4(ws, pk(tot, hi), rpub4, pubS);
      NAP(D3);
      asm volatile("" ::"v"(gl[0]), "v"(gl[1]), "v"(gl[2]), "v"(gl[3]));
      STAMP(9);
    } else {
      if (s < 62) {
        NAP(KVD);
        fetch_kv(s + 1);
      } else {
        WAITVM0();
      }
      STAMP(10);
    }
    BAR();
  }
  WAITVM0();
  BAR();
  if (w == 2 && l == 0) st4(ws, tag, 0, C_KM + (u32)((h * 64 + 63) * 2 + a) * 4u);
  if (ci == 0 && w == 1 && l == 0) st4(ws, tag, 0, C_EP);
  TL(3);
}

DI void stageW(const char* W, u32 off0, u32 rs, u32 ldsbuf, int w, int l) {
  const u32 bx = (u32)((l & 15) ^ ((l >> 4) << 1));
  const u32 ro = off0 + (u32)(64 * w + (l >> 4)) * rs;
#pragma unroll
  for (int it = 0; it < 16; it++)
    dma16(W, ro + (u32)(it * 4) * rs + ((bx ^ (u32)(((it >> 1) & 1) << 3)) << 4), ldsbuf + (w * 16 + it) * 1024);
}
template <bool AG>
DI void readW(bf16x8 (&B)[2][16], int half, lptr buf, int w, int l) {
  const int gb = l >> 4, q = (l >> 2) & 3, p = l & 3;
  const int f = ((gb & 1) << 2) | q;
#pragma unroll
  for (int y = 0; y < 2; y++) {
    lptr b = buf + (8 * gb + q) * 256 + (((4 * w + 2 * y + (p >> 1)) ^ (f << 1)) << 4) + (p & 1) * 8;
#pragma unroll
    for (int k = 0; k < 8; k++) {
      B[y][half * 8 + k] = cat2(trr(b + k * 8192), trr(b + k * 8192 + 1024));
    }
  }
}
DI void stageX(const char* x, int s, u32 ldsbuf, int w, int l) {
  const u32 ro = (u32)(32 * s + 16 * (w >> 1) + (l & 15)) * 1024u + (u32)((w & 1) * 8) * 64u + (u32)(l >> 4) * 16u;
#pragma unroll
  for (int it = 0; it < 8; it++) dma16(x, ro + it * 64u, ldsbuf + (w * 8 + it) * 1024);
}

template <bool KV, bool Q>
DI void qkv_seg(lptr xb, rsrc_t ws, u32 so, int w, int l, const bf16x8 (&Bq)[2][16], const bf16x8 (&Bk)[2][16], const bf16x8 (&Bv)[2][16]) {
  const f32x4 z4 = {0.f, 0.f, 0.f, 0.f};
  f32x4 aq[2][2], ak[2][2], av[2][2];
#pragma unroll
  for (int i = 0; i < 4; i++) { aq[i >> 1][i & 1] = z4; ak[i >> 1][i & 1] = z4; av[i >> 1][i & 1] = z4; }
  bf16x8 n0 = *(LAS bf16x8*)(xb), n1 = *(LAS bf16x8*)(xb + 16384);
#pragma unroll
  for (int ks = 0; ks < 16; ks++) {
    bf16x8 x0 = n0, x1 = n1;
    if (ks < 15) { n0 = *(LAS bf16x8*)(xb + (ks + 1) * 1024); n1 = *(LAS bf16x8*)(xb + 16384 + (ks + 1) * 1024); }
    asm volatile("" : "+v"(x0), "+v"(x1));
#pragma unroll
    for (int y = 0; y < 2; y++) {
      if (KV) {
        ak[0][y] = mfma32(Bk[y][ks], x0, ak[0][y]); ak[1][y] = mfma32(Bk[y][ks], x1, ak[1][y]);
        av[0][y] = mfma32(x0, Bv[y][ks], av[0][y]); av[1][y] = mfma32(x1, Bv[y][ks], av[1][y]);
      }
      if (Q) { aq[0][y] = mfma32(Bq[y][ks], x0, aq[0][y]); aq[1][y] = mfma32(Bq[y][ks], x1, aq[1][y]); }
    }
    if (KV) asm volatile("" : "+a"(ak[0][0]), "+a"(ak[0][1]), "+a"(ak[1][0]), "+a"(ak[1][1]), "+a"(av[0][0]), "+a"(av[0][1]), "+a"(av[1][0]), "+a"(av[1][1]));
    if (Q) asm volatile("" : "+a"(aq[0][0]), "+a"(aq[0][1]), "+a"(aq[1][0]), "+a"(aq[1][1]));
  }
#pragma unroll
  for (int t = 0; t < 2; t++) {
    if (KV) {
      u32x4 kk = {pk(ak[t][0][0], ak[t][0][1]), pk(ak[t][0][2], ak[t][0][3]), pk(ak[t][1][0], ak[t][1][1]), pk(ak[t][1][2], ak[t][1][3])};
      u32x4 vv = {pk(av[0][t][0], av[0][t][1]), pk(av[0][t][2], av[0][t][3]), pk(av[1][t][0], av[1][t][1]), pk(av[1][t][2], av[1][t][3])};
      st16(ws, kk, (u32)l * 16u + (u32)w * 1024u, OFF_KX + so + t * 4096u);
      st16(ws, vv, (u32)l * 16u + (u32)(2 * w + t) * 1024u, OFF_VX + so);
    }
    if (Q) {
      u32x4 qq = {pk(aq[t][0][0], aq[t][0][1]), pk(aq[t][0][2], aq[t][0][3]), pk(aq[t][1][0], aq[t][1][1]), pk(aq[t][1][2], aq[t][1][3])};
      st16(ws, qq, (u32)l * 16u + (u32)w * 1024u, OFF_QX + so + t * 4096u);
    }
  }
}

DI u32 qkv_role(lptr smem, rsrc_t ws, const char* wsb, int h, int j, int w, int l, const char* x, const char* Wq, const char* Wk, const char* Wv) {
  const u32 lds0 = (u32)(size_t)smem;
  bf16x8 Bq[2][16], Bk[2][16], Bv[2][16];
  const u32 ho = (u32)h * 256u, hh = 256u * 8192u;
  const u32 fl = (w == 0 && l == 0) ? 0u : OOB;
  stageW(Wk, ho, 8192u, lds0, w, l);
  stageW(Wk, ho + hh, 8192u, lds0 + 65536, w, l);
  SCHED();
  const u32 tag = gettag(wsb);
  WAITVM(16); BAR(); readW<true>(Bk, 0, smem, w, l); BAR(); stageW(Wv, ho, 8192u, lds0, w, l);
  WAITVM(16); BAR(); readW<true>(Bk, 1, smem + 65536, w, l); BAR(); stageX(x, j, lds0 + 131072, w, l); stageW(Wv, ho + hh, 8192u, lds0 + 65536, w, l);
  WAITVM(24); BAR(); readW<false>(Bv, 0, smem, w, l);
  WAITVM(0); BAR(); readW<false>(Bv, 1, smem + 65536, w, l);
  TL(1);
  qkv_seg<true, false>(smem + 131072 + l * 16, ws, (u32)(j * 32 + h) * 8192u, w, l, Bq, Bk, Bv);
  BAR();
  stageW(Wq, ho, 8192u, lds0, w, l);
  WAITVM(16); BAR();
  st4(ws, tag + 1u, fl, C_KV + (u32)(h * 64 + j) * 4u);
  TL(2);
  stageW(Wq, ho + hh, 8192u, lds0 + 65536, w, l);
  WAITVM(16); BAR(); readW<true>(Bq, 0, smem, w, l); BAR(); stageX(x, j + 6, lds0, w, l);
  WAITVM(8); BAR(); readW<true>(Bq, 1, smem + 65536, w, l);
  qkv_seg<false, true>(smem + 131072 + l * 16, ws, (u32)(j * 32 + h) * 8192u, w, l, Bq, Bk, Bv);
  WAITVM(0); BAR();
  st4(ws, tag + 2u, fl, C_KV + (u32)(h * 64 + j) * 4u);
  int buf = 0;
  for (int s = j + 6; s < 64; s += 6) {
    if (s + 6 < 64) stageX(x, s + 6, lds0 + (buf ^ 1) * 32768, w, l);
    qkv_seg<true, true>(smem + buf * 32768 + l * 16, ws, (u32)(s * 32 + h) * 8192u, w, l, Bq, Bk, Bv);
    WAITVM(0); BAR();
    st4(ws, tag + 2u, fl, C_KV + (u32)(h * 64 + s) * 4u);
    buf ^= 1;
  }
  return tag;
}

DI void ffn1_role(lptr smem, rsrc_t ws, u32 tag, int cg, int jj, int w, int l, const char* x, const char* w1, const bf16* b1, const bf16* gw) {
  const u32 lds0 = (u32)(size_t)smem;
  bf16x8 B[2][16], Gf[16];
  stageW(w1, (u32)cg * 256u, 3584u, lds0, w, l);
  stageW(w1, (u32)cg * 256u + 256u * 3584u, 3584u, lds0 + 65536, w, l);
  float bias[8];
#pragma unroll
  for (int i = 0; i < 8; i++) bias[i] = (float)b1[cg * 128 + 32 * w + 16 * (i >> 2) + 4 * (l >> 4) + (i & 3)];
#pragma unroll
  for (int ks = 0; ks < 16; ks++) Gf[ks] = *(const bf16x8*)(gw + 32 * ks + 8 * (l >> 4));
  WAITVM(0); BAR(); readW<true>(B, 0, smem, w, l); readW<true>(B, 1, smem + 65536, w, l); BAR();
  stageX(x, jj, lds0, w, l);
  WAITVM(0); BAR();
  const f32x4 z4 = {0.f, 0.f, 0.f, 0.f};
  const bool dog = (cg == 0 && w == 0);
  int buf = 0;
  for (int s = jj; s < 64; s += 9) {
    if (s + 9 < 64) stageX(x, s + 9, lds0 + (buf ^ 1) * 32768, w, l);
    lptr xb = smem + buf * 32768 + l * 16;
    f32x4 a[2][2] = {{z4, z4}, {z4, z4}};
#pragma unroll
    for (int ks = 0; ks < 16; ks++) {
      const bf16x8 x0 = *(LAS bf16x8*)(xb + ks * 1024), x1 = *(LAS bf16x8*)(xb + 16384 + ks * 1024);
#pragma unroll
      for (int y = 0; y < 2; y++) { a[0][y] = mfma32(B[y][ks], x0, a[0][y]); a[1][y] = mfma32(B[y][ks], x1, a[1][y]); }
    }
#pragma unroll
    for (int t = 0; t < 2; t++) {
      float r[8];
#pragma unroll
      for (int i = 0; i < 8; i++) {
        const float hv = rbf(rbf(a[t][i >> 2][i & 3]) + bias[i]);
        const float z = hv > 0.f ? hv : 0.f;
        r[i] = z * z;
      }
      u32x4 o = {pk(r[0], r[1]), pk(r[2], r[3]), pk(r[4], r[5]), pk(r[6], r[7])};
      st16(ws, o, (u32)l * 16u + (u32)(cg * 4 + w) * 1024u, OFF_HF + (u32)(s * 2 + t) * 57344u);
    }
    if (dog) {
      f32x4 g0 = z4, g1 = z4;
#pragma unroll
      for (int ks = 0; ks < 16; ks++) {
        g0 = mfma32(Gf[ks], *(LAS bf16x8*)(xb + ks * 1024), g0);
        g1 = mfma32(Gf[ks], *(LAS bf16x8*)(xb + 16384 + ks * 1024), g1);
      }
      const float zz = rbf(l < 16 ? g0[0] : g1[0]);
      const float gg = rbf(1.f / (1.f + __expf(-zz)));
      if (l < 32) st4(ws, __builtin_bit_cast(u32, gg), (u32)l * 4u, OFF_G + (u32)s * 128u);
    }
    WAITVM(0); BAR();
    if (w == 0 && l == 0) st4(ws, tag, 0, C_HC + (u32)s * 128u + (u32)cg * 4u);
    buf ^= 1;
  }
}

DI void attn_role(rsrc_t ws, u32 tag, int u, int l) {
  const int h = u & 31, gb = l >> 4, c = l & 15;
  bool dead = false;
  const f32x4 z4 = {0.f, 0.f, 0.f, 0.f};
  const u32x4 one8 = {0x3F803F80u, 0x3F803F80u, 0x3F803F80u, 0x3F803F80u};
  const u32x2 one4 = {0x3F803F80u, 0x3F803F80u};
  for (int s = u >> 5; s < 64; s += 4) {
    const u32 so = (u32)(s * 32 + h) * 8192u, sm = (u32)(s * 32 + h) * 4096u, lo = (u32)l * 16u;
    waitflag(ws, 0, C_KV + (u32)(h * 64 + s) * 4u, 1ull, ~0u, tag + 2u, dead, 2);
    u32x4 Q[2][4], K[2][4], V[8], KM[4];
    u32x2 VM[8];
#pragma unroll
    for (int i = 0; i < 8; i++) {
      Q[i >> 2][i & 3] = ld16(ws, lo + (i & 3) * 1024u, OFF_QX + so + (i >> 2) * 4096u);
      K[i >> 2][i & 3] = ld16(ws, lo + (i & 3) * 1024u, OFF_KX + so + (i >> 2) * 4096u);
      V[i] = ld16(ws, lo + (i & 3) * 1024u, OFF_VX + so + (i >> 2) * 4096u);
    }
    SCHED();
    f32x4 s00 = z4, s01 = z4, s11 = z4;
#pragma unroll
    for (int ks = 0; ks < 4; ks++) {
      s00 = mfma32(as8(K[0][ks]), as8(Q[0][ks]), s00);
      s01 = mfma32(as8(K[0][ks]), as8(Q[1][ks]), s01);
      s11 = mfma32(as8(K[1][ks]), as8(Q[1][ks]), s11);
    }
#pragma unroll
    for (int i = 0; i < 4; i++)
      if (4 * gb + i > c) { s00[i] = NEG; s11[i] = NEG; }
    waitflag(ws, (u32)(l & 1) * 4u, C_KM + (u32)((h * 64 + s) * 2) * 4u, 3ull, ~0u, tag, dead, s < 60 ? 2 : 0);
#pragma unroll
    for (int i = 0; i < 4; i++) KM[i] = ld16(ws, lo + i * 1024u, OFF_KM + sm);
#pragma unroll
    for (int i = 0; i < 8; i++) VM[i] = (u32x2){ld4(ws, (u32)l * 4u + i * 256u, OFF_VM + sm), 0u};
    SCHED();
    f32x4 m0 = z4, m1 = z4;
#pragma unroll
    for (int ks = 0; ks < 4; ks++) { m0 = mfma32(as8(KM[ks]), as8(Q[0][ks]), m0); m1 = mfma32(as8(KM[ks]), as8(Q[1][ks]), m1); }
#pragma unroll
    for (int t = 0; t < 2; t++) {
      const f32x4 sM = t ? m1 : m0, sa = t ? s01 : s00;
      float m = fmaxf(fmaxf(sM[0], sM[1]), fmaxf(fmaxf(sa[0], sa[1]), fmaxf(sa[2], sa[3])));
      if (t) m = fmaxf(m, fmaxf(fmaxf(s11[0], s11[1]), fmaxf(s11[2], s11[3])));
      m = maxall(m) * SCL;
      float pM[4], p0[4], p1[4];
#pragma unroll
      for (int i = 0; i < 4; i++) {
        pM[i] = i < 2 ? __builtin_amdgcn_exp2f(__builtin_fmaf(sM[i], SCL, -m)) : 0.f;
        p0[i] = __builtin_amdgcn_exp2f(__builtin_fmaf(sa[i], SCL, -m));
        p1[i] = t ? __builtin_amdgcn_exp2f(__builtin_fmaf(s11[i], SCL, -m)) : 0.f;
      }
      const u32x2 PM = {pk(pM[0], pM[1]), 0u};
      const u32x4 PX = {pk(p0[0], p0[1]), pk(p0[2], p0[3]), pk(p1[0], p1[1]), pk(p1[2], p1[3])};
      const float rs = __builtin_amdgcn_rcpf(mfma16(one4, PM, z4)[0] + mfma32(as8(one8), as8(PX), z4)[0]);
#pragma unroll
      for (int ks = 0; ks < 4; ks++) {
        f32x4 oa = mfma32(as8(V[2 * ks]), as8(PX), z4) + mfma16(VM[2 * ks], PM, z4);
        f32x4 ob = mfma32(as8(V[2 * ks + 1]), as8(PX), z4) + mfma16(VM[2 * ks + 1], PM, z4);
        u32x4 o = {pk(oa[0] * rs, oa[1] * rs), pk(oa[2] * rs, oa[3] * rs), pk(ob[0] * rs, ob[1] * rs), pk(ob[2] * rs, ob[3] * rs)};
        st16(ws, o, lo + ks * 1024u, OFF_OF + (u32)(s * 2 + t) * 131072u + (u32)h * 4096u);
      }
    }
    WAITVM(0);
    if (l == 0) st4(ws, tag, 0, C_OC + (u32)s * 128u + (u32)h * 4u);
  }
}

DI void out_role(lptr smem, rsrc_t ws, u32 tag, int grp, int xt, int sl, int w, int l, const char* Wo, const char* w2, const bf16* b2, bf16* out) {
  const u32 lds0 = (u32)(size_t)smem + (u32)w * 32768u;
  lptr wb = smem + w * 32768;
  const int gb = l >> 4, q = (l >> 2) & 3, p = l & 3, c = l & 15;
  bf16x8 A[2][32], A2[2][14];
  bool dead = false;
  {
    const u32 ro = (u32)(l >> 2) * 1024u + (u32)sl * 64u + (u32)(l & 3) * 16u;
    auto issue = [&](const char* W, int row0, int n, int buf) {
#pragma unroll
      for (int it = 0; it < 16; it++)
        if (it < n) dma16(W, ro + (u32)(row0 + it * 16) * 1024u, lds0 + buf * 16384 + it * 1024);
    };
    lptr b = wb + (4 * gb + q) * 64 + p * 16;
    issue(w2, w * 448, 16, 0);
    issue(w2, w * 448 + 256, 12, 1);
    WAITVM(12);
#pragma unroll
    for (int k = 0; k < 8; k++)
#pragma unroll
      for (int y = 0; y < 2; y++) { A2[y][k] = cat2(trr(b + k * 2048 + y * 8), trr(b + k * 2048 + 1024 + y * 8)); }
    asm volatile("s_waitcnt lgkmcnt(0)" ::: "memory");
    issue(Wo, w * 1024, 16, 0);
    WAITVM(16);
#pragma unroll
    for (int k = 0; k < 6; k++)
#pragma unroll
      for (int y = 0; y < 2; y++) { A2[y][8 + k] = cat2(trr(b + 16384 + k * 2048 + y * 8), trr(b + 16384 + k * 2048 + 1024 + y * 8)); }
#pragma unroll
    for (int r = 0; r < 4; r++) {
      asm volatile("s_waitcnt lgkmcnt(0)" ::: "memory");
      if (r < 3) { issue(Wo, w * 1024 + (r + 1) * 256, 16, (r + 1) & 1); WAITVM(16); } else { WAITVM(0); }
#pragma unroll
      for (int k = 0; k < 8; k++)
#pragma unroll
        for (int y = 0; y < 2; y++)
        { A[y][r * 8 + k] = cat2(trr(b + (r & 1) * 16384 + k * 2048 + y * 8), trr(b + (r & 1) * 16384 + k * 2048 + 1024 + y * 8)); }
    }
  }
  float bias[8];
#pragma unroll
  for (int i = 0; i < 8; i++) bias[i] = (float)b2[sl * 32 + 8 * gb + i];
#define LDO(v, s) ld16(ws, v, s)
  BAR();
  TL(5);
  const f32x4 z4 = {0.f, 0.f, 0.f, 0.f};
  const u32 lo = (u32)l * 16u;
  for (int s = grp; s < 64; s += 3) {
    f32x4 a0 = z4, a1 = z4, h0 = z4, h1 = z4;
    waitflag(ws, (u32)(l & 15) * 4u, C_HC + (u32)s * 128u, 0x3FFFull, ~0u, tag, dead, 2);
    {
      const u32 so = OFF_HF + (u32)(s * 2 + xt) * 57344u + (u32)w * 14336u;
      u32x4 F[14];
#pragma unroll
      for (int k = 0; k < 14; k++) F[k] = LDO(lo, so + k * 1024u);
      SCHED();
#pragma unroll
      for (int k = 0; k < 14; k++) { h0 = mfma32(A2[0][k], as8(F[k]), h0); h1 = mfma32(A2[1][k], as8(F[k]), h1); }
    }
    const float g = __builtin_bit_cast(float, ld4(ws, (u32)c * 4u, OFF_G + (u32)(s * 2 + xt) * 64u));
    waitflag(ws, (u32)(l & 31) * 4u, C_OC + (u32)s * 128u, 0xFFFFFFFFull, ~0u, tag, dead, 1);
    {
      const u32 so = OFF_OF + (u32)(s * 2 + xt) * 131072u + (u32)w * 32768u;
      u32x4 F[16];
#pragma unroll
      for (int k = 0; k < 16; k++) F[k] = LDO(lo, so + k * 1024u);
      SCHED();
#pragma unroll
      for (int k = 0; k < 32; k++) {
        a0 = mfma32(A[0][k], as8(F[k & 15]), a0); a1 = mfma32(A[1][k], as8(F[k & 15]), a1);
        if (k < 16) {
          asm volatile("" : "+a"(a0), "+a"(a1));
          F[k] = LDO(lo, so + 16384u + k * 1024u);
          SCHED();
        }
      }
    }
    lptr rb = smem + w * 4096 + lo;
    *(LAS f32x4*)(rb) = a0; *(LAS f32x4*)(rb + 1024) = a1; *(LAS f32x4*)(rb + 2048) = h0; *(LAS f32x4*)(rb + 3072) = h1;
    BAR();
    if (w == 0) {
#pragma unroll
      for (int k = 1; k < 4; k++) {
        a0 += *(LAS f32x4*)(rb + k * 4096); a1 += *(LAS f32x4*)(rb + k * 4096 + 1024);
        h0 += *(LAS f32x4*)(rb + k * 4096 + 2048); h1 += *(LAS f32x4*)(rb + k * 4096 + 3072);
      }
      const float g1 = rbf(1.f - g);
      float r[8];
#pragma unroll
      for (int i = 0; i < 8; i++) {
        const float av = rbf(i < 4 ? a0[i & 3] : a1[i & 3]);
        const float bv = rbf(rbf(i < 4 ? h0[i & 3] : h1[i & 3]) + bias[i]);
        r[i] = rbf(g * av) + rbf(g1 * bv);
      }
      u32x4 o = {pk(r[0], r[1]), pk(r[2], r[3]), pk(r[4], r[5]), pk(r[6], r[7])};
      *(u32x4*)(out + (size_t)(32 * s + 16 * xt + c) * 512 + sl * 32 + 8 * gb) = o;
    }
    BAR();
  }
}

DI void probe_role(lptr smem, rsrc_t ws, u32 tag, int w, int l) {
  const u32 base = ((w & 1) ? OFF_MEM : OFF_PART) + (u32)(w >> 1) * 131072u;
  u32 best[2] = {0xFFFFFFFFu, 0xFFFFFFFFu};
#pragma unroll 1
  for (int i = 0; i < 8; i++) {
#pragma unroll
    for (int sd = 0; sd < 2; sd++) {
      u64 t0 = __builtin_amdgcn_s_memtime();
      asm volatile("s_waitcnt lgkmcnt(0)" ::: "memory");
      st4(ws, 0xFFFFFFFFu, l == 0 ? 0u : OOB, base + (u32)i * 16384u + (u32)sd * 8192u);
      WAITVM(0);
      u64 t1 = __builtin_amdgcn_s_memtime();
      best[sd] = min(best[sd], (u32)(t1 - t0));
    }
  }
  if (l == 0) { *(LAS u32*)(smem + w * 8) = best[0]; *(LAS u32*)(smem + w * 8 + 4) = best[1]; }
  BAR();
  if (w == 0 && l == 0) {
    LAS u32* t = (LAS u32*)smem;
    const u32 a = min(t[0], t[4]), b = min(t[1], t[5]), c = min(t[2], t[6]), d = min(t[3], t[7]);
    st4(ws, tag | (b < a ? 1u : 0u) | (d < c ? 2u : 0u), 0, C_SEL);
  }
  BAR();
}

__global__ __launch_bounds__(256, 1) void fused_kernel(const char* x, const bf16* mem0, const char* Wq, const char* Wk, const char* Wv,
                                                      const char* Wo, const char* w1, const bf16* b1, const char* w2, const bf16* b2,
                                                      const bf16* gw, bf16* out, char* wsb) {
  extern __shared__ __attribute__((aligned(16))) char smem_[];
  lptr smem = (lptr)smem_;
  const int b = blockIdx.x, xq = b & 7, r = b >> 3;
  const int w = __builtin_amdgcn_readfirstlane(threadIdx.x >> 6), l = threadIdx.x & 63;
  const rsrc_t ws = __builtin_amdgcn_make_buffer_rsrc(wsb, 0, (int)WS_BYTES, 0x00020000);
  TL(0);
  if (xq < 4 && r < 16) {
    chain_role(smem, ws, 8 * xq + (r >> 1), r & 1, w, l, Wq, Wk, Wv, Wo, mem0, wsb);
    return;
  }
  const int head = xq < 4 ? 8 * xq + ((r - 16) >> 1) : 8 * (xq - 4) + (r >> 2);
  const int j = xq < 4 ? 4 + (r & 1) : (r & 3);
  if (b == 31 * 8) probe_role(smem, ws, gettag(wsb), w, l);
  const u32 tag = qkv_role(smem, ws, wsb, head, j, w, l, x, Wq, Wk, Wv);
  TL(3);
  const int wi = (r - 16) * 8 + xq;
  if (r >= 16 && wi < 126) ffn1_role(smem, ws, tag, wi / 9, wi % 9, w, l, x, w1, b1, gw);
  TL(4);
  if (xq >= 4) {
    if (r >= 24) {
      attn_role(ws, tag, ((xq - 4) * 8 + (r - 24)) * 4 + w, l);
    } else {
      const int g2 = (xq - 4) >> 1;
      out_role(smem, ws, tag, r < 16 ? g2 : 2, xq & 1, r < 16 ? r : r - 16 + 8 * g2, w, l, Wo, w2, b2, out);
    }
  }
  TL(6);
}

extern "C" void kernel_launch(void* const* d_in, const int* in_sizes, int n_in, void* d_out, int out_size, void* d_ws,
                              size_t ws_size, hipStream_t stream) {
  char* base = (char*)(((size_t)d_ws + 2 * MB - 1) & ~(size_t)(2 * MB - 1));
  (void)hipFuncSetAttribute((const void*)fused_kernel, hipFuncAttributeMaxDynamicSharedMemorySize, LDS_BYTES);
  fused_kernel<<<256, 256, LDS_BYTES, stream>>>((const char*)d_in[0], (const bf16*)d_in[1], (const char*)d_in[2], (const char*)d_in[3],
                                                (const char*)d_in[4], (const char*)d_in[5], (const char*)d_in[8], (const bf16*)d_in[9],
                                                (const char*)d_in[10], (const bf16*)d_in[11], (const bf16*)d_in[12], (bf16*)d_out, base);
}
```

```cpp
#include <hip/hip_runtime.h>
#include <hip/hip_bf16.h>

typedef unsigned u32;
typedef unsigned long long u64;
typedef __bf16 bf16;
typedef bf16 bf16x2 __attribute__((ext_vector_type(2)));
typedef bf16 bf16x8 __attribute__((ext_vector_type(8)));
typedef bf16 bf16x16 __attribute__((ext_vector_type(16)));
typedef short s16x4 __attribute__((ext_vector_type(4)));
typedef short s16x8 __attribute__((ext_vector_type(8)));
typedef short s16x16 __attribute__((ext_vector_type(16)));
typedef float f32x2 __attribute__((ext_vector_type(2)));
typedef float f32x4 __attribute__((ext_vector_type(4)));
typedef u32 u32x2 __attribute__((ext_vector_type(2)));
typedef u32 u32x4 __attribute__((ext_vector_type(4)));
typedef __amdgpu_buffer_rsrc_t rsrc_t;
#define LAS __attribute__((address_space(3)))
typedef LAS char* lptr;
#define DI __device__ __forceinline__

constexpr u32 MB = 1u << 20;
constexpr u32 OFF_PART = 0;
constexpr u32 OFF_MEM = 94 * MB;
constexpr u32 OFF_CTRL = 96 * MB;
constexpr u32 C_SEL = OFF_CTRL;
constexpr u32 C_EP = OFF_CTRL + 256;
constexpr u32 C_RDY = OFF_CTRL + 512;
constexpr u32 C_KV = OFF_CTRL + 1024;
constexpr u32 C_KM = C_KV + 8192;
constexpr u32 C_OC = C_KM + 16384;
constexpr u32 C_HC = C_OC + 8192;
constexpr u32 C_END = C_HC + 8192;
constexpr u32 OFF_QX = 4 * MB, OFF_KX = 20 * MB, OFF_VX = 36 * MB;
constexpr u32 OFF_KM = 52 * MB, OFF_VM = 60 * MB, OFF_OF = 68 * MB, OFF_HF = 84 * MB, OFF_G = 92 * MB;
constexpr u32 WS_BYTES = 97 * MB;
DI u32 slot_off(int s) { const u32 r = (u32)(63 - s); return (r >> 1) * 65536u + (r & 1u) * 2048u; }
DI u32 tile_off(int t) { return (u32)(t >> 2) * 16384u + (u32)((t >> 1) & 1) * 4096u + (u32)(t & 1) * 1024u; }
constexpr int LDS_BYTES = 160 * 1024;
constexpr int SPIN_MAX = 1 << 15;
constexpr int AUX = 16;

DI u32x4 ld16(rsrc_t r, u32 v, u32 s) { return __builtin_amdgcn_raw_buffer_load_b128(r, v, s, AUX); }
DI u32x2 ld8(rsrc_t r, u32 v, u32 s) { return __builtin_amdgcn_raw_buffer_load_b64(r, v, s, AUX); }
DI u32 ld4(rsrc_t r, u32 v, u32 s) { return __builtin_amdgcn_raw_buffer_load_b32(r, v, s, AUX); }
DI void st16(rsrc_t r, u32x4 x, u32 v, u32 s) {
  __builtin_amdgcn_raw_buffer_store_b128(x, r, v, s, AUX);
  asm volatile("s_nop 1" ::"v"(x) : "memory");
}
DI void st8(rsrc_t r, u32x2 x, u32 v, u32 s) { __builtin_amdgcn_raw_buffer_store_b64(x, r, v, s, AUX); }
DI void st4(rsrc_t r, u32 x, u32 v, u32 s) { __builtin_amdgcn_raw_buffer_store_b32(x, r, v, s, AUX); }
#define MEMBAR() asm volatile("" ::: "memory")
#define WAITVM(n) asm volatile("s_waitcnt vmcnt(" #n ")" ::: "memory")
#define WAITVM0() do { MEMBAR(); __builtin_amdgcn_s_waitcnt(0x0F70); MEMBAR(); } while (0)
#define BAR() asm volatile("s_waitcnt lgkmcnt(0)\n\ts_barrier" ::: "memory")
#define SCHED() __builtin_amdgcn_sched_barrier(0)
#define PINA(x) asm volatile("" : "+a"(x))
#define PINV(x) asm volatile("" : "+v"(x))
constexpr u32 OOB = 0x80000000u;

DI void dma16(const char* sbase, u32 voff, u32 ldsdst) {
  u32 keep;
  asm volatile("s_mov_b32 %0, m0\n\ts_mov_b32 m0, %3\n\ts_nop 0\n\tglobal_load_lds_dwordx4 %1, %2\n\ts_mov_b32 m0, %0"
               : "=&s"(keep) : "v"(voff), "s"(sbase), "s"(ldsdst) : "memory");
}
DI s16x4 trr(lptr p) { return __builtin_amdgcn_ds_read_tr16_b64_v4i16((LAS s16x4*)p); }
DI bf16x8 cat2(s16x4 a, s16x4 b) { return __builtin_bit_cast(bf16x8, __builtin_shufflevector(a, b, 0, 1, 2, 3, 4, 5, 6, 7)); }
DI bf16x16 cat4(s16x4 a, s16x4 b, s16x4 c, s16x4 d) {
  s16x8 x = __builtin_shufflevector(a, b, 0, 1, 2, 3, 4, 5, 6, 7), y = __builtin_shufflevector(c, d, 0, 1, 2, 3, 4, 5, 6, 7);
  return __builtin_bit_cast(bf16x16, __builtin_shufflevector(x, y, 0, 1, 2, 3, 4, 5, 6, 7, 8, 9, 10, 11, 12, 13, 14, 15));
}
DI u32 pk(float lo, float hi) { f32x2 v = {lo, hi}; return __builtin_bit_cast(u32, __builtin_convertvector(v, bf16x2)); }
DI u32 pk2(f32x2 v) { return __builtin_bit_cast(u32, __builtin_convertvector(v, bf16x2)); }
DI float rbf(float x) { return (float)(bf16)x; }
DI bf16x8 as8(u32x4 x) { return __builtin_bit_cast(bf16x8, x); }
DI f32x4 mfma32(bf16x8 a, bf16x8 b, f32x4 c) { return __builtin_amdgcn_mfma_f32_16x16x32_bf16(a, b, c, 0, 0, 0); }
DI f32x4 mfma16(u32x2 a, u32x2 b, f32x4 c) {
  return __builtin_amdgcn_mfma_f32_16x16x16bf16_1k(__builtin_bit_cast(s16x4, a), __builtin_bit_cast(s16x4, b), c, 0, 0, 0);
}
DI f32x4 smf(u32x4 a, bf16x16 b, f32x4 c, int idx) { return __builtin_amdgcn_smfmac_f32_16x16x64_bf16(as8(a), b, c, idx, 0, 0); }
DI float comb(float x, float y) {
  auto r = __builtin_amdgcn_permlane32_swap(__builtin_bit_cast(u32, x), __builtin_bit_cast(u32, y), false, false);
  return __builtin_bit_cast(float, (u32)r[0]) + __builtin_bit_cast(float, (u32)r[1]);
}
DI float maxall(float m) {
  auto r = __builtin_amdgcn_permlane32_swap(__builtin_bit_cast(u32, m), __builtin_bit_cast(u32, m), false, false);
  m = fmaxf(__builtin_bit_cast(float, (u32)r[0]), __builtin_bit_cast(float, (u32)r[1]));
  auto q = __builtin_amdgcn_permlane16_swap(__builtin_bit_cast(u32, m), __builtin_bit_cast(u32, m), false, false);
  return fmaxf(__builtin_bit_cast(float, (u32)q[0]), __builtin_bit_cast(float, (u32)q[1]));
}
DI f32x4 zero4() {
  u64 a, b;
  asm volatile("v_mov_b64 %0, 0" : "=v"(a));
  asm volatile("v_mov_b64 %0, 0" : "=v"(b));
  typedef u64 u64x2 __attribute__((ext_vector_type(2)));
  u64x2 r = {a, b};
  return __builtin_bit_cast(f32x4, r);
}
DI u32 umax8(u32x4 a, u32x4 b) { return max(max(max(a[0], a[1]), max(a[2], a[3])), max(max(b[0], b[1]), max(b[2], b[3]))); }
DI bool anyl(bool p) { return __builtin_amdgcn_ballot_w64(p) != 0; }
DI void waitflag(rsrc_t r, u32 v, u32 s, u64 lanes, u32 msk, u32 val, bool& dead, int sleep) {
  int n = 0;
#pragma unroll 1
  while (!dead) {
    MEMBAR();
    const u32 x = ld4(r, v, s);
    if ((__builtin_amdgcn_ballot_w64((x & msk) != val) & lanes) == 0) break;
    asm volatile("" : "+s"(n));
    if (++n > SPIN_MAX) dead = true;
    for (int i = 0; i < sleep; i++) __builtin_amdgcn_s_sleep(4);
  }
  MEMBAR();
}
DI u32 gettag(const char* wsb) {
  u32 t = *(const __attribute__((address_space(4))) u32*)(wsb + C_EP);
  t = (t + 4u) & ~3u;
  asm volatile("" : "+s"(t));
  return t;
}
constexpr float NEG = -3.0e38f;
constexpr float SCL = 0.08838834764831845f * 1.4426950408889634f;

DI void init_unit(rsrc_t ws, u32 tag, int ci, int w, int l, const bf16* mem0) {
  const int gb = l >> 4, c = l & 15, rho = 2 * ci + (w & 1), rt = rho >> 4, gs = (rho >> 2) & 3, ru = rho & 3;
  const u32 rpiece = tile_off(rt) + (u32)ru * 256u + (u32)gs * 64u, cp = (u32)(w >> 1) * 8192u;
  const u32 rblk0 = (u32)(rho >> 1) * 16384u + (u32)(rho & 1) * 4096u;
  const u32x4 ones = {~0u, ~0u, ~0u, ~0u};
  const unsigned short* m0 = (const unsigned short*)mem0 + (2 * gs + (c & 1)) * 512 + 64 * rt + 16 * ((c >> 1) & 1) + 4 * ru + gb;
  const u32 mlo = m0[0], mhi = m0[32];
  SCHED();
#pragma unroll
  for (int k = 0; k < 8; k++) st16(ws, ones, rblk0 + (u32)l * 16u + (k & 3) * 1024u, OFF_PART + (k >> 2) * MB + cp);
#pragma unroll
  for (int k = 0; k < 4; k++) {
    const int s = 1 + k * 16 + (l >> 2);
    st16(ws, ones, s < 64 ? slot_off(s) + rpiece + (u32)(l & 3) * 16u : OOB, OFF_MEM + cp);
  }
  SCHED();
  st4(ws, mlo | (mhi << 16), c < 4 ? rpiece + (u32)(c & 3) * 16u + (u32)gb * 4u : OOB, OFF_MEM + cp + slot_off(0));
  WAITVM(0); BAR();
  st4(ws, tag, (w == 0 && l == 0) ? 0u : OOB, C_RDY + (u32)ci * 4u);
}

DI void chain_role(lptr smem, rsrc_t ws, int h, int a, int w, int l, const char* Wq, const char* Wk, const char* Wv,
                   const char* Wo, const char* wsb) {
  const int gb = l >> 4, q = (l >> 2) & 3, p = l & 3, c = l & 15;
  const u32 lds0 = (u32)(size_t)smem;
  bf16x16 Bq[2][8], Bk[2][8], Bv[8], Bo[8];
  bool dead = false;
  const int ci = 2 * h + a;
  const int rho = 2 * ci + (w & 1);
  const int rt = rho >> 4, gs = (rho >> 2) & 3, ru = rho & 3;
  const u32 rpiece = tile_off(rt) + (u32)ru * 256u + (u32)gs * 64u;
  const u32 rpub4 = c < 4 ? rpiece + (u32)(c & 3) * 16u + (u32)gb * 4u : OOB;
  const u32 rblk0 = (u32)(rho >> 1) * 16384u + (u32)(rho & 1) * 4096u;
  const u32 lane0 = l == 0 ? 0u : OOB;
  const u32x4 ones = {~0u, ~0u, ~0u, ~0u};
  u32 selw, rdy, flg, tag;
  {
    const u32 bx = (u32)((l & 15) ^ ((l >> 5) << 1));
    const u32 rowoff = (u32)(64 * w + (l >> 4)) * 8192u + (u32)h * 256u;
    auto issueQK = [&](const char* W, int half, int buf) {
#pragma unroll
      for (int it = 0; it < 16; it++) {
        const u32 cit = (u32)((((it >> 3) & 1) << 3) | (((it >> 2) & 1) << 2));
        dma16(W, rowoff + (u32)(half * 256 + it * 4) * 8192u + ((bx ^ cit) << 4), lds0 + buf * 65536 + (w * 16 + it) * 1024);
      }
    };
    auto issueV = [&](int buf) {
      const u32 bv = (u32)((l & 7) ^ ((l >> 5) << 1));
      const u32 ro = (u32)(128 * w + ((l >> 3) & 1) * 32 + (l >> 4)) * 8192u + (u32)h * 256u + (u32)a * 128u;
#pragma unroll
      for (int it = 0; it < 16; it++)
        dma16(Wv, ro + (u32)((it >> 3) * 64 + (it & 7) * 4) * 8192u + ((bv ^ (u32)(((it >> 2) & 1) << 2)) << 4),
              lds0 + buf * 65536 + (w * 16 + it) * 1024);
    };
    auto issueO = [&](int buf) {
      const u32 ro = (u32)(h * 128 + a * 64 + w * 16) * 1024u;
#pragma unroll
      for (int it = 0; it < 16; it++) {
        const u32 f = (u32)((((it >> 3) & 1) << 2) | (((it >> 2) & 1) << 1) | (it & 1));
        dma16(Wo, ro + (u32)it * 1024u + (((u32)l ^ (f << 1)) << 4), lds0 + buf * 65536 + (w * 16 + it) * 1024);
      }
    };
    const int rowl = 32 * (q & 1) + 16 * (q >> 1) + 2 * gb;
    const int fq = ((q & 1) << 2) | ((q >> 1) << 1) | (gb & 1);
    auto readQK = [&](bf16x16(&B)[2][8], int half, int buf) {
#pragma unroll
      for (int y = 0; y < 2; y++) {
        lptr b = smem + buf * 65536 + rowl * 256 + (((4 * w + 2 * y + (p >> 1)) ^ (fq << 1)) << 4) + (p & 1) * 8;
#pragma unroll
        for (int t = 0; t < 4; t++)
        {
          lptr bt = b + (t ^ w) * 16384;
          B[y][half * 4 + t] = cat4(trr(bt), trr(bt + 256), trr(bt + 2048), trr(bt + 2304));
          PINA(B[y][half * 4 + t]);
        }
      }
    };
    issueQK(Wq, 0, 0);
    issueQK(Wq, 1, 1);
    SCHED();
    tag = gettag(wsb);
    WAITVM(16); BAR(); readQK(Bq, 0, 0); BAR(); issueQK(Wk, 0, 0);
    WAITVM(16); BAR(); readQK(Bq, 1, 1); BAR(); issueQK(Wk, 1, 1);
    WAITVM(16); BAR(); readQK(Bk, 0, 0); BAR(); issueV(0);
    WAITVM(16); BAR(); readQK(Bk, 1, 1); BAR(); issueO(1);
    WAITVM(16); BAR();
    {
      const int f2 = ((q >> 1) << 1) | (gb & 1);
      lptr b = smem + (16 * (q >> 1) + 2 * gb) * 256 + (q & 1) * 128 + (((2 * w + (p >> 1)) ^ (f2 << 1)) << 4) + (p & 1) * 8;
#pragma unroll
      for (int t = 0; t < 8; t++)
      {
        lptr bt = b + (t ^ w) * 8192;
        Bv[t] = cat4(trr(bt), trr(bt + 256), trr(bt + 2048), trr(bt + 2304));
        PINV(Bv[t]);
      }
    }
    SCHED();
    selw = ld4(ws, 0, C_SEL); rdy = ld4(ws, (u32)l * 4u, C_RDY); flg = ld4(ws, 0, C_KV + (u32)(h * 64) * 4u);
    SCHED();
    WAITVM0(); BAR();
    {
      const int fo = ((q >> 1) << 2) | ((gb & 1) << 1) | (q & 1);
      const int er = 16 * (gb >> 1) + 8 * (q >> 1) + 4 * (gb & 1) + (q & 1);
#pragma unroll
      for (int T = 0; T < 8; T++) {
        lptr b = smem + 65536 + er * 1024 + (((16 * w + 2 * T + (p >> 1)) ^ (fo << 1)) << 4) + (p & 1) * 8;
        Bo[T] = cat4(trr(b), trr(b + 2048), trr(b + 32768), trr(b + 32768 + 2048));
        PINV(Bo[T]);
      }
    }
    BAR();
  }
  if (anyl(rdy != tag)) waitflag(ws, (u32)l * 4u, C_RDY, ~0ull, ~0u, tag, dead, 1);
  if (anyl((selw & ~3u) != tag)) { waitflag(ws, 0, C_SEL, 1ull, ~3u, tag, dead, 1); selw = ld4(ws, 0, C_SEL); }
  selw = __builtin_amdgcn_readfirstlane(selw);
  flg = __builtin_amdgcn_readfirstlane(flg);
  const u32 sel0 = (selw & 1u) * 8192u, sel1 = ((selw >> 1) & 1u) * 8192u;
  const u32 rgat = rblk0 + sel0 + (u32)(4 * gb + (c & 3)) * 64u + (u32)(c >> 2) * 16u;
  constexpr u32 GK = 1024u;
  const u32 pst = (u32)(w * 16 + (l >> 3)) * 16384u + (u32)((l >> 2) & 1) * 4096u + sel0 + (u32)ci * 64u + (u32)(l & 3) * 16u;
  const u32 kvlane = (u32)l * 16u;
  lptr Lq = smem, Lk = smem + 6144, Lx = smem + 12288, Lv = smem + 20480, La = smem + 24576, Lo = smem + 49152;
#pragma unroll
  for (int i = 0; i < 3; i++) *(LAS u32x4*)(smem + i * 4096 + w * 1024 + l * 16) = (u32x4){0u, 0u, 0u, 0u};
  auto fetch_kv = [&](int sn) {
    if ((flg & ~3u) != tag) waitflag(ws, 0, C_KV + (u32)(h * 64 + sn) * 4u, 1ull, ~3u, tag, dead, 1);
    const u32 so = (u32)(sn * 32 + h) * 8192u;
    u32x4 t[6];
    const u32 fl = ld4(ws, 0, C_KV + (u32)(h * 64 + sn + 1) * 4u);
    SCHED();
#pragma unroll
    for (int i = 0; i < 4; i++) t[i] = ld16(ws, kvlane, OFF_KX + so + (u32)(w - 2) * 4096u + i * 1024u);
#pragma unroll
    for (int i = 0; i < 2; i++) t[4 + i] = ld16(ws, kvlane, OFF_VX + so + (u32)(4 * a + 2 * (w - 2) + i) * 1024u);
    SCHED();
    flg = __builtin_amdgcn_readfirstlane(fl);
#pragma unroll
    for (int i = 0; i < 4; i++) *(LAS u32x4*)(Lx + (w - 2) * 4096 + i * 1024 + l * 16) = t[i];
#pragma unroll
    for (int i = 0; i < 2; i++) *(LAS u32x4*)(Lv + (w - 2) * 2048 + i * 1024 + l * 16) = t[4 + i];
  };
  if (w >= 2) fetch_kv(0);
  WAITVM0();
  BAR();
  const u32 kmoff = (l >> 5) == a ? kvlane + (u32)w * 1024u : OOB;
  const u32 flgoff = w == 2 ? lane0 : OOB;
  const int idx = (l & 2) ? 0xEEEE : 0x4444;
  const int wq = (4 * w + (c >> 2)) * 384 + (c & 3) * 32 + gb * 8;
  const int rq = (4 * w + gb) * 384 + q * 32 + p * 8;
  const bool up = (c & 2) != 0;
  u32 pmask = up ? 0xFFFF0000u : 0x0000FFFFu;
  u32 tagv = tag;
  PINV(pmask); PINV(tagv);
  lptr Ls = smem + 53248;
  lptr Lar[4] = {La, La + (w ^ 1) * 1024 + l * 16, La + (w ^ 2) * 1024 + l * 16, La + (w ^ 3) * 1024 + l * 16};
  lptr Lkw = Lk + wq, Lsw = Ls + 4096 + w * 256 + l * 4;
  PINV(Lar[1]); PINV(Lar[2]); PINV(Lar[3]); PINV(Lkw); PINV(Lsw);
  const int orow = 2 * (c >> 2) + (c & 1);
  u32x4 selB;
  {
    const int j = c & 7;
    const u32 one = (j & 1) ? 0x3F800000u : 0x00003F80u;
    selB[0] = (j >> 1) == 0 ? one : 0u; selB[1] = (j >> 1) == 1 ? one : 0u;
    selB[2] = (j >> 1) == 2 ? one : 0u; selB[3] = (j >> 1) == 3 ? one : 0u;
  }
  u32x4 one8 = {0x3F803F80u, 0x3F803F80u, 0x3F803F80u, 0x3F803F80u};
  PINV(one8); PINV(selB);
#define SM2(d0, d1, B, t)                                                                                            \
  asm volatile("v_smfmac_f32_16x16x64_bf16 %0, %2, %3, %5\n\tv_smfmac_f32_16x16x64_bf16 %1, %2, %4, %5"              \
               : "+v"(d0), "+v"(d1) : "v"(A[t]), "a"(B[0][t]), "a"(B[1][t]), "v"(idx));                              \
  SCHED()
#define VSTEP(t)                                                                                                     \
  asm volatile("v_smfmac_f32_16x16x64_bf16 %0, %1, %2, %3" : "+v"(v0) : "v"(A[t]), "v"(Bv[t]), "v"(idx));            \
  SCHED()
#define MF0(D, F, G) asm volatile("v_mfma_f32_16x16x32_bf16 %0, %1, %2, 0" : "=&v"(D) : "v"(F), "v"(G)); SCHED()
#define MF1(D, F, G) asm volatile("v_mfma_f32_16x16x32_bf16 %0, %1, %2, %0" : "+v"(D) : "v"(F), "v"(G)); SCHED()
#define FOLD(D) pk2(__builtin_shufflevector(D, D, 0, 1) + __builtin_shufflevector(D, D, 2, 3))
#define UNDEF(x) asm volatile("" : "=v"(x))
#define NAP(n) do { if (n) __builtin_amdgcn_s_sleep(n); } while (0)
#define KVD 0
#define D1 9
#define D2 8
#define D3 9

  for (int s = 0; s < 64; s++) {
    const u32 mslot = OFF_MEM + sel1 + slot_off(s) + tile_off(w);
    u32x4 A[8];
    u32x4 g0 = ld16(ws, kvlane, mslot); SCHED();
    u32x4 g1 = ld16(ws, kvlane, mslot + 16384u); SCHED();
    f32x4 q0 = zero4(), q1 = zero4(), k0 = zero4(), k1 = zero4(), v0 = zero4();
    u32 parS = OFF_PART + (u32)(s & 1) * MB, parT = parS + 8 * 16384u, rstS = parS ^ MB, rstT = parT ^ MB;
    u32 kmS = OFF_KM + (u32)(s * 32 + h) * 4096u, vmS = kmS + (OFF_VM - OFF_KM), pubS = OFF_MEM + sel1 + slot_off(s + 1);
    u32 flS = C_KM - 8u + (u32)(h * 128 + a) * 4u + (u32)s * 8u, flV = s > 0 ? flgoff : OOB;
    asm volatile("" : "+s"(parS), "+s"(parT), "+s"(rstS), "+s"(rstT), "+s"(kmS), "+s"(vmS), "+s"(pubS), "+s"(flS), "+v"(flV));
    SCHED();
    *(LAS u32x4*)(La + w * 1024 + l * 16) = g0;
    *(LAS u32x4*)(La + w * 1024 + 4096 + l * 16) = g1;
    SCHED();
    if (__builtin_expect(anyl(umax8(g0, g1) == 0xFFFFFFFFu), 0)) {
      int n = 0;
      asm volatile("" : "+v"(g0), "+v"(g1));
#pragma unroll 1
      while (anyl(umax8(g0, g1) == 0xFFFFFFFFu) && !dead) {
        MEMBAR();
        g0 = ld16(ws, kvlane, mslot);
        g1 = ld16(ws, kvlane, mslot + 16384u);
        WAITVM0();
        if (++n > SPIN_MAX) dead = true;
      }
      *(LAS u32x4*)(La + w * 1024 + l * 16) = g0;
      *(LAS u32x4*)(La + w * 1024 + 4096 + l * 16) = g1;
    }
    BAR();
    A[0] = g0; A[4] = g1;
#define RDA(t) A[t] = *(LAS u32x4*)(Lar[t & 3] + (t & 4) * 1024); SCHED()
    RDA(1); RDA(2);
    SM2(q0, q1, Bq, 0);
    RDA(3);
    SM2(q0, q1, Bq, 4);
    RDA(5);
    SM2(k0, k1, Bk, 0);
    RDA(6);
    SM2(k0, k1, Bk, 4);
    RDA(7);
    SM2(q0, q1, Bq, 1);
    __builtin_amdgcn_raw_buffer_store_b128(ones, ws, pst, rstS, AUX); SCHED();
    SM2(q0, q1, Bq, 2);
    __builtin_amdgcn_raw_buffer_store_b128(ones, ws, pst, rstT, AUX); SCHED();
    SM2(q0, q1, Bq, 3);
    st4(ws, tagv, flV, flS); SCHED();
    SM2(q0, q1, Bq, 5);
    const u32x4 X0 = *(LAS u32x4*)(Lx + w * 1024 + l * 16), X1 = *(LAS u32x4*)(Lx + 4096 + w * 1024 + l * 16);
    SCHED();
    SM2(q0, q1, Bq, 6); SM2(q0, q1, Bq, 7);
    const u32x2 vxa = *(LAS u32x2*)(Lv + w * 1024 + l * 16);
    asm volatile("" ::: "memory");
    const u32x2 vxb = *(LAS u32x2*)(Lv + w * 1024 + l * 16 + 8);
    SCHED();
    SM2(k0, k1, Bk, 1);
    asm volatile("" : "+v"(q0), "+v"(q1));
    {
      const u32 a0 = FOLD(q0), a1 = FOLD(q1);
      *(LAS u32*)(Lq + wq) = a0; *(LAS u32*)(Lq + wq + 4) = a0; SCHED();
      SM2(k0, k1, Bk, 2);
      *(LAS u32*)(Lq + wq + 128) = a1; *(LAS u32*)(Lq + wq + 132) = a1; SCHED();
    }
    SM2(k0, k1, Bk, 3);
    const bf16x8 Fq = cat2(trr(Lq + rq), trr(Lq + rq + 128));
    SCHED();
    f32x4 D[8];
    SM2(k0, k1, Bk, 5);
    u32x4 VA = {0u, 0u, vxa[0], vxa[1]}, VB = {0u, 0u, vxb[0], vxb[1]};
    PINV(VA); PINV(VB);
    SCHED();
    SM2(k0, k1, Bk, 6);
    D[0] = zero4(); D[1] = zero4();
    SCHED();
    SM2(k0, k1, Bk, 7);
    f32x4 sM, s0, s1;
    MF0(s0, X0, Fq);
    MF0(s1, X1, Fq);
    VSTEP(0);
    asm volatile("" : "+v"(k0), "+v"(k1));
    { const u32 a0 = FOLD(k0), a1 = FOLD(k1); *(LAS u32*)(Lkw) = a0; *(LAS u32*)(Lkw + 128) = a1; SCHED(); }
    const bf16x8 Fk = cat2(trr(Lk + rq), trr(Lk + rq + 128));
    SCHED();
    VSTEP(1);
    asm volatile("" : "+v"(s0), "+v"(s1));
    {
      const f32x4 x = {up ? s1[0] : s0[0], up ? s1[1] : s0[1], up ? s1[2] : s0[2], up ? s1[3] : s0[3]};
      SCHED();
      VSTEP(2);
      *(LAS f32x4*)(Ls + w * 1024 + l * 16) = x;
      SCHED();
    }
    VSTEP(3);
    D[2] = zero4(); SCHED();
    VSTEP(4);
    D[3] = zero4(); SCHED();
    VSTEP(5);
    MF0(sM, Fk, Fq);
    VSTEP(6); VSTEP(7);
    st16(ws, __builtin_bit_cast(u32x4, Fk), kmoff, kmS);
    asm volatile("" : "+v"(sM));
    *(LAS float*)(Lsw) = up ? sM[1] : sM[0];
    SCHED();
    BAR();
    f32x4 xs[4];
    float ms[4];
#pragma unroll
    for (int j = 0; j < 4; j++) ms[j] = *(LAS float*)(Ls + 4096 + j * 256 + l * 4);
    SCHED();
#pragma unroll
    for (int j = 0; j < 4; j++) { xs[j] = *(LAS f32x4*)(Ls + j * 1024 + l * 16); SCHED(); }
    asm volatile("" : "+v"(v0));
    const u32 vm = FOLD(v0);
    st4(ws, vm, (u32)l * 4u + (u32)(4 * a + w) * 256u, vmS);
    if (s == 63) break;
    VA[0] = vm; VB[0] = vm;
    PINV(VA); PINV(VB);
    SCHED();
    float sm = ms[0] + ms[1], sn = ms[2] + ms[3];
    PINV(sm); PINV(sn);
    sm += sn;
    PINV(sm);
    SCHED();
    const f32x4 sx = (xs[0] + xs[1]) + (xs[2] + xs[3]);
    float m, mt;
    asm volatile("v_max3_f32 %0, %2, %3, %6\n\tv_max3_f32 %0, %0, %4, %5\n\ts_nop 1\n\t"
                 "v_max_f32_dpp %1, %0, %0 quad_perm:[2,3,0,1] row_mask:0xf bank_mask:0xf\n\t"
                 "v_max_f32_dpp %0, %0, %0 quad_perm:[2,3,0,1] row_mask:0xf bank_mask:0xf\n\ts_nop 1\n\t"
                 "v_permlane32_swap_b32 %0, %1\n\tv_max_f32 %0, %0, %1\n\tv_mov_b32 %1, %0\n\ts_nop 1\n\t"
                 "v_permlane16_swap_b32 %0, %1\n\tv_max_f32 %0, %0, %1"
                 : "=&v"(m), "=&v"(mt) : "v"(sx[0]), "v"(sx[1]), "v"(sx[2]), "v"(sx[3]), "v"(sm));
    m *= -SCL;
    const f32x2 m2 = {m, m}, scl2 = {SCL, SCL};
#define EXP(t) pk(__builtin_amdgcn_exp2f(t[0]), __builtin_amdgcn_exp2f(t[1]))
    float tM;
    asm volatile("v_fma_f32 %0, %1, %2, %3" : "=v"(tM) : "v"(sm), "s"(SCL), "v"(m));
    const f32x2 t0 = __builtin_elementwise_fma(__builtin_shufflevector(sx, sx, 0, 1), scl2, m2),
                t1 = __builtin_elementwise_fma(__builtin_shufflevector(sx, sx, 2, 3), scl2, m2);
    SCHED();
    const float eM = __builtin_amdgcn_exp2f(tM);
    const u32x4 P = {pk(eM, eM) & pmask, 0u, EXP(t0), EXP(t1)};
    f32x4 sum, oa, ob;
    asm volatile("s_nop 1\n\tv_mfma_f32_16x16x32_bf16 %0, %3, %4, 0\n\tv_mfma_f32_16x16x32_bf16 %2, %6, %4, 0\n\t"
                 "v_mfma_f32_16x16x32_bf16 %1, %5, %4, 0"
                 : "=&v"(sum), "=&v"(oa), "=&v"(ob) : "v"(one8), "v"(P), "v"(VA), "v"(VB));
    {
      float rs;
      f32x4 o;
      asm volatile("v_add_f32_dpp %0, %1, %1 quad_perm:[2,3,0,1] row_mask:0xf bank_mask:0xf\n\ts_nop 0\n\tv_rcp_f32 %0, %0" : "=&v"(rs) : "v"(sum[0]));
      D[4] = zero4(); D[5] = zero4(); D[6] = zero4();
      asm volatile("v_add_f32_dpp %0, %4, %8 quad_perm:[2,3,0,1] row_mask:0xf bank_mask:0xf\n\t"
                   "v_add_f32_dpp %1, %5, %9 quad_perm:[2,3,0,1] row_mask:0xf bank_mask:0xf\n\t"
                   "v_add_f32_dpp %2, %6, %10 quad_perm:[2,3,0,1] row_mask:0xf bank_mask:0xf\n\t"
                   "v_add_f32_dpp %3, %7, %11 quad_perm:[2,3,0,1] row_mask:0xf bank_mask:0xf"
                   : "=&v"(o[0]), "=&v"(o[1]), "=&v"(o[2]), "=&v"(o[3])
                   : "v"(ob[0]), "v"(ob[1]), "v"(ob[2]), "v"(ob[3]), "v"(oa[0]), "v"(oa[1]), "v"(oa[2]), "v"(oa[3]));
      const f32x2 r2 = {rs, rs};
      u32x2 oo = {pk2(__builtin_shufflevector(o, o, 0, 1) * r2), pk2(__builtin_shufflevector(o, o, 2, 3) * r2)};
      *(LAS u32x2*)(Lo + (orow + ((c & 2) << 2)) * 144 + w * 32 + gb * 8) = oo;
    }
    WAITVM(2);
    BAR();
    const u32x4 Ao = *(LAS u32x4*)(Lo + orow * 144 + gb * 32 + ((c >> 1) & 1) * 16);
    SCHED();
    D[7] = zero4();
    SCHED();
#define OST(T) asm volatile("v_smfmac_f32_16x16x64_bf16 %0, %1, %2, %3" : "+v"(D[T]) : "v"(Ao), "v"(Bo[T]), "v"(idx)); SCHED()
#define OAD(T) asm volatile("v_pk_add_f32 %0, %1, %2" : "=v"(f[T]) : "v"(__builtin_shufflevector(D[T], D[T], 0, 1)), "v"(__builtin_shufflevector(D[T], D[T], 2, 3))); SCHED()
#define OCV(P_, i, T) asm volatile("v_cvt_pk_bf16_f32 %0, %1, %2" : "=v"(P_[i]) : "v"(f[T][0]), "v"(f[T][1])); SCHED()
#define NOP(n) asm volatile("s_nop " #n); SCHED()
    {
      u32 pv[4], pw[4];
      f32x2 f[8];
      OST(0); OST(1); OST(2);
      OAD(0); OST(3);
      OAD(1); OCV(pv, 0, 0); OST(4);
      OAD(2); OCV(pv, 1, 1); OST(5);
      OAD(3); OCV(pv, 2, 2); OST(6);
      OAD(4); OCV(pv, 3, 3); OST(7);
      __builtin_amdgcn_raw_buffer_store_b128((u32x4){pv[0], pv[1], pv[2], pv[3]}, ws, pst, parS, AUX); SCHED();
      OAD(5); OCV(pw, 0, 4); NOP(0); OCV(pw, 1, 5); NOP(0);
      OAD(6); NOP(0); OCV(pw, 2, 6); NOP(0);
      OAD(7); NOP(0); OCV(pw, 3, 7);
      asm volatile("" ::"v"(pv[0]), "v"(pv[1]), "v"(pv[2]), "v"(pv[3]));
      st16(ws, (u32x4){pw[0], pw[1], pw[2], pw[3]}, pst, parT);
    }
    if (w < 2) {
      u32x4 g[4], gl[4];
      NAP(D1);
      SCHED();
#pragma unroll
      for (int k = 0; k < 4; k++) { g[k] = ld16(ws, rgat, parS + k * GK); SCHED(); }
      NAP(D2);
      SCHED();
#pragma unroll
      for (int k = 0; k < 4; k++) { gl[k] = ld16(ws, rgat, parS + k * GK); SCHED(); }
      float tot;
#define REDUCE(G)                                                                                                    \
  {                                                                                                                  \
    f32x4 acc;                                                                                                       \
    MF0(acc, G[0], selB); MF1(acc, G[1], selB); MF1(acc, G[2], selB); MF1(acc, G[3], selB);                           \
    asm volatile("s_nop 7" : "+v"(acc));                                                                             \
    const f32x2 t_ = __builtin_shufflevector(acc, acc, 0, 1) + __builtin_shufflevector(acc, acc, 2, 3); tot = t_[0] + t_[1];                                                               \
  }
      REDUCE(g);
      if (__builtin_expect(anyl(tot != tot), 0)) {
        REDUCE(gl);
        int n = 0;
#pragma unroll 1
        while (anyl(tot != tot) && !dead) {
          MEMBAR();
#pragma unroll
          for (int k = 0; k < 4; k++) { g[k] = ld16(ws, rgat, parS + k * GK); SCHED(); }
          REDUCE(g);
          if (++n > SPIN_MAX) dead = true;
        }
      }
      const float hi = __builtin_bit_cast(float, __builtin_amdgcn_update_dpp(0, __builtin_bit_cast(int, tot), 0x104, 0xf, 0xf, true));
      st4(ws, pk(tot, hi), rpub4, pubS);
      NAP(D3);
      asm volatile("" ::"v"(gl[0]), "v"(gl[1]), "v"(gl[2]), "v"(gl[3]));
    } else {
      if (s < 62) {
        NAP(KVD);
        fetch_kv(s + 1);
      } else {
        WAITVM0();
      }
    }
    BAR();
  }
  WAITVM0();
  BAR();
  if (w == 2 && l == 0) st4(ws, tag, 0, C_KM + (u32)((h * 64 + 63) * 2 + a) * 4u);
  if (ci == 0 && w == 1 && l == 0) st4(ws, tag, 0, C_EP);
}

DI void stageW(const char* W, u32 off0, u32 rs, u32 ldsbuf, int w, int l) {
  const u32 bx = (u32)((l & 15) ^ ((l >> 4) << 1));
  const u32 ro = off0 + (u32)(64 * w + (l >> 4)) * rs;
#pragma unroll
  for (int it = 0; it < 16; it++)
    dma16(W, ro + (u32)(it * 4) * rs + ((bx ^ (u32)(((it >> 1) & 1) << 3)) << 4), ldsbuf + (w * 16 + it) * 1024);
}
template <bool AG>
DI void readW(bf16x8 (&B)[2][16], int half, lptr buf, int w, int l) {
  const int gb = l >> 4, q = (l >> 2) & 3, p = l & 3;
  const int f = ((gb & 1) << 2) | q;
#pragma unroll
  for (int y = 0; y < 2; y++) {
    lptr b = buf + (8 * gb + q) * 256 + (((4 * w + 2 * y + (p >> 1)) ^ (f << 1)) << 4) + (p & 1) * 8;
#pragma unroll
    for (int k = 0; k < 8; k++) {
      B[y][half * 8 + k] = cat2(trr(b + k * 8192), trr(b + k * 8192 + 1024));
    }
  }
}
DI void stageX(const char* x, int s, u32 ldsbuf, int w, int l) {
  const u32 ro = (u32)(32 * s + 16 * (w >> 1) + (l & 15)) * 1024u + (u32)((w & 1) * 8) * 64u + (u32)(l >> 4) * 16u;
#pragma unroll
  for (int it = 0; it < 8; it++) dma16(x, ro + it * 64u, ldsbuf + (w * 8 + it) * 1024);
}

template <bool K, bool V, bool Q>
DI void qkv_seg(lptr xb, rsrc_t ws, u32 so, int w, int l, const bf16x8 (&Bq)[2][16], const bf16x8 (&Bk)[2][16], const bf16x8 (&Bv)[2][16]) {
  const f32x4 z4 = {0.f, 0.f, 0.f, 0.f};
  f32x4 aq[2][2], ak[2][2], av[2][2];
#pragma unroll
  for (int i = 0; i < 4; i++) { aq[i >> 1][i & 1] = z4; ak[i >> 1][i & 1] = z4; av[i >> 1][i & 1] = z4; }
  bf16x8 n0 = *(LAS bf16x8*)(xb), n1 = *(LAS bf16x8*)(xb + 16384);
#pragma unroll
  for (int ks = 0; ks < 16; ks++) {
    bf16x8 x0 = n0, x1 = n1;
    if (ks < 15) { n0 = *(LAS bf16x8*)(xb + (ks + 1) * 1024); n1 = *(LAS bf16x8*)(xb + 16384 + (ks + 1) * 1024); }
    asm volatile("" : "+v"(x0), "+v"(x1));
#pragma unroll
    for (int y = 0; y < 2; y++) {
      if (K) { ak[0][y] = mfma32(Bk[y][ks], x0, ak[0][y]); ak[1][y] = mfma32(Bk[y][ks], x1, ak[1][y]); }
      if (V) { av[0][y] = mfma32(x0, Bv[y][ks], av[0][y]); av[1][y] = mfma32(x1, Bv[y][ks], av[1][y]); }
      if (Q) { aq[0][y] = mfma32(Bq[y][ks], x0, aq[0][y]); aq[1][y] = mfma32(Bq[y][ks], x1, aq[1][y]); }
    }
    if (K) asm volatile("" : "+a"(ak[0][0]), "+a"(ak[0][1]), "+a"(ak[1][0]), "+a"(ak[1][1]));
    if (V) asm volatile("" : "+a"(av[0][0]), "+a"(av[0][1]), "+a"(av[1][0]), "+a"(av[1][1]));
    if (Q) asm volatile("" : "+a"(aq[0][0]), "+a"(aq[0][1]), "+a"(aq[1][0]), "+a"(aq[1][1]));
  }
#pragma unroll
  for (int t = 0; t < 2; t++) {
    if (K) {
      u32x4 kk = {pk(ak[t][0][0], ak[t][0][1]), pk(ak[t][0][2], ak[t][0][3]), pk(ak[t][1][0], ak[t][1][1]), pk(ak[t][1][2], ak[t][1][3])};
      st16(ws, kk, (u32)l * 16u + (u32)w * 1024u, OFF_KX + so + t * 4096u);
    }
    if (V) {
      u32x4 vv = {pk(av[0][t][0], av[0][t][1]), pk(av[0][t][2], av[0][t][3]), pk(av[1][t][0], av[1][t][1]), pk(av[1][t][2], av[1][t][3])};
      st16(ws, vv, (u32)l * 16u + (u32)(2 * w + t) * 1024u, OFF_VX + so);
    }
    if (Q) {
      u32x4 qq = {pk(aq[t][0][0], aq[t][0][1]), pk(aq[t][0][2], aq[t][0][3]), pk(aq[t][1][0], aq[t][1][1]), pk(aq[t][1][2], aq[t][1][3])};
      st16(ws, qq, (u32)l * 16u + (u32)w * 1024u, OFF_QX + so + t * 4096u);
    }
  }
}

DI u32 qkv_role(lptr smem, rsrc_t ws, const char* wsb, int h, int j, int w, int l, const char* x, const char* Wq, const char* Wk, const char* Wv) {
  const u32 lds0 = (u32)(size_t)smem;
  bf16x8 Bq[2][16], Bk[2][16], Bv[2][16];
  const u32 ho = (u32)h * 256u, hh = 256u * 8192u;
  const u32 fl = (w == 0 && l == 0) ? 0u : OOB;
  stageW(Wk, ho, 8192u, lds0, w, l);
  stageW(Wk, ho + hh, 8192u, lds0 + 65536, w, l);
  SCHED();
  const u32 tag = gettag(wsb);
  WAITVM(16); BAR(); readW<true>(Bk, 0, smem, w, l); BAR(); stageX(x, j, lds0 + 131072, w, l); stageW(Wv, ho, 8192u, lds0, w, l);
  WAITVM(24); BAR();
  readW<true>(Bk, 1, smem + 65536, w, l); BAR(); stageW(Wv, ho + hh, 8192u, lds0 + 65536, w, l);
  WAITVM(32); BAR();
  qkv_seg<true, false, false>(smem + 131072 + l * 16, ws, (u32)(j * 32 + h) * 8192u, w, l, Bq, Bk, Bv);
  WAITVM(18); BAR(); readW<false>(Bv, 0, smem, w, l);
  WAITVM(2); BAR(); readW<false>(Bv, 1, smem + 65536, w, l);
  qkv_seg<false, true, false>(smem + 131072 + l * 16, ws, (u32)(j * 32 + h) * 8192u, w, l, Bq, Bk, Bv);
  BAR();
  stageW(Wq, ho, 8192u, lds0, w, l);
  WAITVM(16); BAR();
  st4(ws, tag + 1u, fl, C_KV + (u32)(h * 64 + j) * 4u);
  stageW(Wq, ho + hh, 8192u, lds0 + 65536, w, l);
  WAITVM(16); BAR(); readW<true>(Bq, 0, smem, w, l); BAR(); stageX(x, j + 6, lds0, w, l);
  WAITVM(8); BAR(); readW<true>(Bq, 1, smem + 65536, w, l);
  qkv_seg<false, false, true>(smem + 131072 + l * 16, ws, (u32)(j * 32 + h) * 8192u, w, l, Bq, Bk, Bv);
  WAITVM(0); BAR();
  st4(ws, tag + 2u, fl, C_KV + (u32)(h * 64 + j) * 4u);
  int buf = 0;
  for (int s = j + 6; s < 64; s += 6) {
    if (s + 6 < 64) stageX(x, s + 6, lds0 + (buf ^ 1) * 32768, w, l);
    qkv_seg<true, true, true>(smem + buf * 32768 + l * 16, ws, (u32)(s * 32 + h) * 8192u, w, l, Bq, Bk, Bv);
    WAITVM(0); BAR();
    st4(ws, tag + 2u, fl, C_KV + (u32)(h * 64 + s) * 4u);
    buf ^= 1;
  }
  return tag;
}

DI void ffn1_role(lptr smem, rsrc_t ws, u32 tag, int cg, int jj, int w, int l, const char* x, const char* w1, const bf16* b1, const bf16* gw) {
  const u32 lds0 = (u32)(size_t)smem;
  bf16x8 B[2][16], Gf[16];
  stageW(w1, (u32)cg * 256u, 3584u, lds0, w, l);
  stageW(w1, (u32)cg * 256u + 256u * 3584u, 3584u, lds0 + 65536, w, l);
  float bias[8];
#pragma unroll
  for (int i = 0; i < 8; i++) bias[i] = (float)b1[cg * 128 + 32 * w + 16 * (i >> 2) + 4 * (l >> 4) + (i & 3)];
#pragma unroll
  for (int ks = 0; ks < 16; ks++) Gf[ks] = *(const bf16x8*)(gw + 32 * ks + 8 * (l >> 4));
  WAITVM(0); BAR(); readW<true>(B, 0, smem, w, l); readW<true>(B, 1, smem + 65536, w, l); BAR();
  stageX(x, jj, lds0, w, l);
  WAITVM(0); BAR();
  const f32x4 z4 = {0.f, 0.f, 0.f, 0.f};
  const bool dog = (cg == 0 && w == 0);
  int buf = 0;
  for (int s = jj; s < 64; s += 9) {
    if (s + 9 < 64) stageX(x, s + 9, lds0 + (buf ^ 1) * 32768, w, l);
    lptr xb = smem + buf * 32768 + l * 16;
    f32x4 a[2][2] = {{z4, z4}, {z4, z4}};
#pragma unroll
    for (int ks = 0; ks < 16; ks++) {
      const bf16x8 x0 = *(LAS bf16x8*)(xb + ks * 1024), x1 = *(LAS bf16x8*)(xb + 16384 + ks * 1024);
#pragma unroll
      for (int y = 0; y < 2; y++) { a[0][y] = mfma32(B[y][ks], x0, a[0][y]); a[1][y] = mfma32(B[y][ks], x1, a[1][y]); }
    }
#pragma unroll
    for (int t = 0; t < 2; t++) {
      float r[8];
#pragma unroll
      for (int i = 0; i < 8; i++) {
        const float hv = rbf(rbf(a[t][i >> 2][i & 3]) + bias[i]);
        const float z = hv > 0.f ? hv : 0.f;
        r[i] = z * z;
      }
      u32x4 o = {pk(r[0], r[1]), pk(r[2], r[3]), pk(r[4], r[5]), pk(r[6], r[7])};
      st16(ws, o, (u32)l * 16u + (u32)(cg * 4 + w) * 1024u, OFF_HF + (u32)(s * 2 + t) * 57344u);
    }
    if (dog) {
      f32x4 g0 = z4, g1 = z4;
#pragma unroll
      for (int ks = 0; ks < 16; ks++) {
        g0 = mfma32(Gf[ks], *(LAS bf16x8*)(xb + ks * 1024), g0);
        g1 = mfma32(Gf[ks], *(LAS bf16x8*)(xb + 16384 + ks * 1024), g1);
      }
      const float zz = rbf(l < 16 ? g0[0] : g1[0]);
      const float gg = rbf(1.f / (1.f + __expf(-zz)));
      if (l < 32) st4(ws, __builtin_bit_cast(u32, gg), (u32)l * 4u, OFF_G + (u32)s * 128u);
    }
    WAITVM(0); BAR();
    if (w == 0 && l == 0) st4(ws, tag, 0, C_HC + (u32)s * 128u + (u32)cg * 4u);
    buf ^= 1;
  }
}

DI void attn_role(rsrc_t ws, u32 tag, int u, int l) {
  const int h = u & 31, gb = l >> 4, c = l & 15;
  bool dead = false;
  const f32x4 z4 = {0.f, 0.f, 0.f, 0.f};
  const u32x4 one8 = {0x3F803F80u, 0x3F803F80u, 0x3F803F80u, 0x3F803F80u};
  const u32x2 one4 = {0x3F803F80u, 0x3F803F80u};
  for (int s = u >> 5; s < 64; s += 4) {
    const u32 so = (u32)(s * 32 + h) * 8192u, sm = (u32)(s * 32 + h) * 4096u, lo = (u32)l * 16u;
    waitflag(ws, 0, C_KV + (u32)(h * 64 + s) * 4u, 1ull, ~0u, tag + 2u, dead, 2);
    u32x4 Q[2][4], K[2][4], V[8], KM[4];
    u32x2 VM[8];
#pragma unroll
    for (int i = 0; i < 8; i++) {
      Q[i >> 2][i & 3] = ld16(ws, lo + (i & 3) * 1024u, OFF_QX + so + (i >> 2) * 4096u);
      K[i >> 2][i & 3] = ld16(ws, lo + (i & 3) * 1024u, OFF_KX + so + (i >> 2) * 4096u);
      V[i] = ld16(ws, lo + (i & 3) * 1024u, OFF_VX + so + (i >> 2) * 4096u);
    }
    SCHED();
    f32x4 s00 = z4, s01 = z4, s11 = z4;
#pragma unroll
    for (int ks = 0; ks < 4; ks++) {
      s00 = mfma32(as8(K[0][ks]), as8(Q[0][ks]), s00);
      s01 = mfma32(as8(K[0][ks]), as8(Q[1][ks]), s01);
      s11 = mfma32(as8(K[1][ks]), as8(Q[1][ks]), s11);
    }
#pragma unroll
    for (int i = 0; i < 4; i++)
      if (4 * gb + i > c) { s00[i] = NEG; s11[i] = NEG; }
    waitflag(ws, (u32)(l & 1) * 4u, C_KM + (u32)((h * 64 + s) * 2) * 4u, 3ull, ~0u, tag, dead, s < 60 ? 2 : 0);
#pragma unroll
    for (int i = 0; i < 4; i++) KM[i] = ld16(ws, lo + i * 1024u, OFF_KM + sm);
#pragma unroll
    for (int i = 0; i < 8; i++) VM[i] = (u32x2){ld4(ws, (u32)l * 4u + i * 256u, OFF_VM + sm), 0u};
    SCHED();
    f32x4 m0 = z4, m1 = z4;
#pragma unroll
    for (int ks = 0; ks < 4; ks++) { m0 = mfma32(as8(KM[ks]), as8(Q[0][ks]), m0); m1 = mfma32(as8(KM[ks]), as8(Q[1][ks]), m1); }
#pragma unroll
    for (int t = 0; t < 2; t++) {
      const f32x4 sM = t ? m1 : m0, sa = t ? s01 : s00;
      float m = fmaxf(fmaxf(sM[0], sM[1]), fmaxf(fmaxf(sa[0], sa[1]), fmaxf(sa[2], sa[3])));
      if (t) m = fmaxf(m, fmaxf(fmaxf(s11[0], s11[1]), fmaxf(s11[2], s11[3])));
      m = maxall(m) * SCL;
      float pM[4], p0[4], p1[4];
#pragma unroll
      for (int i = 0; i < 4; i++) {
        pM[i] = i < 2 ? __builtin_amdgcn_exp2f(__builtin_fmaf(sM[i], SCL, -m)) : 0.f;
        p0[i] = __builtin_amdgcn_exp2f(__builtin_fmaf(sa[i], SCL, -m));
        p1[i] = t ? __builtin_amdgcn_exp2f(__builtin_fmaf(s11[i], SCL, -m)) : 0.f;
      }
      const u32x2 PM = {pk(pM[0], pM[1]), 0u};
      const u32x4 PX = {pk(p0[0], p0[1]), pk(p0[2], p0[3]), pk(p1[0], p1[1]), pk(p1[2], p1[3])};
      const float rs = __builtin_amdgcn_rcpf(mfma16(one4, PM, z4)[0] + mfma32(as8(one8), as8(PX), z4)[0]);
#pragma unroll
      for (int ks = 0; ks < 4; ks++) {
        f32x4 oa = mfma32(as8(V[2 * ks]), as8(PX), z4) + mfma16(VM[2 * ks], PM, z4);
        f32x4 ob = mfma32(as8(V[2 * ks + 1]), as8(PX), z4) + mfma16(VM[2 * ks + 1], PM, z4);
        u32x4 o = {pk(oa[0] * rs, oa[1] * rs), pk(oa[2] * rs, oa[3] * rs), pk(ob[0] * rs, ob[1] * rs), pk(ob[2] * rs, ob[3] * rs)};
        st16(ws, o, lo + ks * 1024u, OFF_OF + (u32)(s * 2 + t) * 131072u + (u32)h * 4096u);
      }
    }
    WAITVM(0);
    if (l == 0) st4(ws, tag, 0, C_OC + (u32)s * 128u + (u32)h * 4u);
  }
}

DI void out_role(lptr smem, rsrc_t ws, u32 tag, int grp, int xt, int sl, int w, int l, const char* Wo, const char* w2, const bf16* b2, bf16* out) {
  const u32 lds0 = (u32)(size_t)smem + (u32)w * 32768u;
  lptr wb = smem + w * 32768;
  const int gb = l >> 4, q = (l >> 2) & 3, p = l & 3, c = l & 15;
  bf16x8 A[2][32], A2[2][14];
  bool dead = false;
  {
    const u32 ro = (u32)(l >> 2) * 1024u + (u32)sl * 64u + (u32)(l & 3) * 16u;
    auto issue = [&](const char* W, int row0, int n, int buf) {
#pragma unroll
      for (int it = 0; it < 16; it++)
        if (it < n) dma16(W, ro + (u32)(row0 + it * 16) * 1024u, lds0 + buf * 16384 + it * 1024);
    };
    lptr b = wb + (4 * gb + q) * 64 + p * 16;
    issue(w2, w * 448, 16, 0);
    issue(w2, w * 448 + 256, 12, 1);
    WAITVM(12);
#pragma unroll
    for (int k = 0; k < 8; k++)
#pragma unroll
      for (int y = 0; y < 2; y++) { A2[y][k] = cat2(trr(b + k * 2048 + y * 8), trr(b + k * 2048 + 1024 + y * 8)); }
    asm volatile("s_waitcnt lgkmcnt(0)" ::: "memory");
    issue(Wo, w * 1024, 16, 0);
    WAITVM(16);
#pragma unroll
    for (int k = 0; k < 6; k++)
#pragma unroll
      for (int y = 0; y < 2; y++) { A2[y][8 + k] = cat2(trr(b + 16384 + k * 2048 + y * 8), trr(b + 16384 + k * 2048 + 1024 + y * 8)); }
#pragma unroll
    for (int r = 0; r < 4; r++) {
      asm volatile("s_waitcnt lgkmcnt(0)" ::: "memory");
      if (r < 3) { issue(Wo, w * 1024 + (r + 1) * 256, 16, (r + 1) & 1); WAITVM(16); } else { WAITVM(0); }
#pragma unroll
      for (int k = 0; k < 8; k++)
#pragma unroll
        for (int y = 0; y < 2; y++)
        { A[y][r * 8 + k] = cat2(trr(b + (r & 1) * 16384 + k * 2048 + y * 8), trr(b + (r & 1) * 16384 + k * 2048 + 1024 + y * 8)); }
    }
  }
  float bias[8];
#pragma unroll
  for (int i = 0; i < 8; i++) bias[i] = (float)b2[sl * 32 + 8 * gb + i];
#define LDO(v, s) ld16(ws, v, s)
  BAR();
  const f32x4 z4 = {0.f, 0.f, 0.f, 0.f};
  for (int si = grp; si < 65; si += 3) {
    const int s = si < 63 ? si : 63;
    const bool act = si < 63 || (c >> 3) == si - 63;
    const u32 lo = act ? (u32)l * 16u : OOB;
    f32x4 a0 = z4, a1 = z4, h0 = z4, h1 = z4;
    waitflag(ws, (u32)(l & 15) * 4u, C_HC + (u32)s * 128u, 0x3FFFull, ~0u, tag, dead, 2);
    {
      const u32 so = OFF_HF + (u32)(s * 2 + xt) * 57344u + (u32)w * 14336u;
      u32x4 F[14];
#pragma unroll
      for (int k = 0; k < 14; k++) F[k] = LDO(lo, so + k * 1024u);
      SCHED();
#pragma unroll
      for (int k = 0; k < 14; k++) { h0 = mfma32(A2[0][k], as8(F[k]), h0); h1 = mfma32(A2[1][k], as8(F[k]), h1); }
    }
    const float g = __builtin_bit_cast(float, ld4(ws, (u32)c * 4u, OFF_G + (u32)(s * 2 + xt) * 64u));
    waitflag(ws, (u32)(l & 31) * 4u, C_OC + (u32)s * 128u, 0xFFFFFFFFull, ~0u, tag, dead, s < 61 ? 1 : 0);
    {
      const u32 so = OFF_OF + (u32)(s * 2 + xt) * 131072u + (u32)w * 32768u;
      u32x4 F[16];
#pragma unroll
      for (int k = 0; k < 16; k++) F[k] = LDO(lo, so + k * 1024u);
      SCHED();
#pragma unroll
      for (int k = 0; k < 32; k++) {
        a0 = mfma32(A[0][k], as8(F[k & 15]), a0); a1 = mfma32(A[1][k], as8(F[k & 15]), a1);
        if (k < 16) {
          asm volatile("" : "+a"(a0), "+a"(a1));
          F[k] = LDO(lo, so + 16384u + k * 1024u);
          SCHED();
        }
      }
    }
    lptr rb = smem + w * 4096 + l * 16;
    *(LAS f32x4*)(rb) = a0; *(LAS f32x4*)(rb + 1024) = a1; *(LAS f32x4*)(rb + 2048) = h0; *(LAS f32x4*)(rb + 3072) = h1;
    BAR();
    if (w == 0) {
#pragma unroll
      for (int k = 1; k < 4; k++) {
        a0 += *(LAS f32x4*)(rb + k * 4096); a1 += *(LAS f32x4*)(rb + k * 4096 + 1024);
        h0 += *(LAS f32x4*)(rb + k * 4096 + 2048); h1 += *(LAS f32x4*)(rb + k * 4096 + 3072);
      }
      const float g1 = rbf(1.f - g);
      float r[8];
#pragma unroll
      for (int i = 0; i < 8; i++) {
        const float av = rbf(i < 4 ? a0[i & 3] : a1[i & 3]);
        const float bv = rbf(rbf(i < 4 ? h0[i & 3] : h1[i & 3]) + bias[i]);
        r[i] = rbf(g * av) + rbf(g1 * bv);
      }
      u32x4 o = {pk(r[0], r[1]), pk(r[2], r[3]), pk(r[4], r[5]), pk(r[6], r[7])};
      if (act) *(u32x4*)(out + (size_t)(32 * s + 16 * xt + c) * 512 + sl * 32 + 8 * gb) = o;
    }
    BAR();
  }
}

DI void probe_role(lptr smem, rsrc_t ws, u32 tag, int w, int l) {
  const u32 base = ((w & 1) ? OFF_MEM : OFF_PART) + (u32)(w >> 1) * 131072u;
  u32 best[2] = {0xFFFFFFFFu, 0xFFFFFFFFu};
#pragma unroll 1
  for (int i = 0; i < 8; i++) {
#pragma unroll
    for (int sd = 0; sd < 2; sd++) {
      u64 t0 = __builtin_amdgcn_s_memtime();
      asm volatile("s_waitcnt lgkmcnt(0)" ::: "memory");
      st4(ws, 0xFFFFFFFFu, l == 0 ? 0u : OOB, base + (u32)i * 16384u + (u32)sd * 8192u);
      WAITVM(0);
      u64 t1 = __builtin_amdgcn_s_memtime();
      best[sd] = min(best[sd], (u32)(t1 - t0));
    }
  }
  if (l == 0) { *(LAS u32*)(smem + w * 8) = best[0]; *(LAS u32*)(smem + w * 8 + 4) = best[1]; }
  BAR();
  if (w == 0 && l == 0) {
    LAS u32* t = (LAS u32*)smem;
    const u32 a = min(t[0], t[4]), b = min(t[1], t[5]), c = min(t[2], t[6]), d = min(t[3], t[7]);
    st4(ws, tag | (b < a ? 1u : 0u) | (d < c ? 2u : 0u), 0, C_SEL);
  }
  BAR();
}

__global__ __launch_bounds__(256, 1) void fused_kernel(const char* x, const bf16* mem0, const char* Wq, const char* Wk, const char* Wv,
                                                      const char* Wo, const char* w1, const bf16* b1, const char* w2, const bf16* b2,
                                                      const bf16* gw, bf16* out, char* wsb) {
  extern __shared__ __attribute__((aligned(16))) char smem_[];
  lptr smem = (lptr)smem_;
  const int b = blockIdx.x, xq = b & 7, r = b >> 3;
  const int w = __builtin_amdgcn_readfirstlane(threadIdx.x >> 6), l = threadIdx.x & 63;
  const rsrc_t ws = __builtin_amdgcn_make_buffer_rsrc(wsb, 0, (int)WS_BYTES, 0x00020000);
  if (xq < 4 && r < 16) {
    chain_role(smem, ws, 8 * xq + (r >> 1), r & 1, w, l, Wq, Wk, Wv, Wo, wsb);
    return;
  }
  const int head = xq < 4 ? 8 * xq + ((r - 16) >> 1) : 8 * (xq - 4) + (r >> 2);
  const int j = xq < 4 ? 4 + (r & 1) : (r & 3);
  if (xq < 4) {
    const u32 tg = gettag(wsb);
    if (b == 31 * 8) probe_role(smem, ws, tg, w, l);
    else for (int u = (r - 16) * 4 + xq; u < 64; u += b == 30 * 8 ? 4 : 64) init_unit(ws, tg, u, w, l, mem0);
  }
  const u32 tag = qkv_role(smem, ws, wsb, head, j, w, l, x, Wq, Wk, Wv);
  const int wi = (r - 16) * 8 + xq;
  if (r >= 16 && wi < 126) ffn1_role(smem, ws, tag, wi / 9, wi % 9, w, l, x, w1, b1, gw);
  if (xq >= 4) {
    if (r >= 24) {
      attn_role(ws, tag, ((xq - 4) * 8 + (r - 24)) * 4 + w, l);
    } else {
      const int g2 = (xq - 4) >> 1;
      out_role(smem, ws, tag, r < 16 ? g2 : 2, xq & 1, r < 16 ? r : r - 16 + 8 * g2, w, l, Wo, w2, b2, out);
    }
  }
}

extern "C" void kernel_launch(void* const* d_in, const int* in_sizes, int n_in, void* d_out, int out_size, void* d_ws,
                              size_t ws_size, hipStream_t stream) {
  char* base = (char*)(((size_t)d_ws + 2 * MB - 1) & ~(size_t)(2 * MB - 1));
  (void)hipFuncSetAttribute((const void*)fused_kernel, hipFuncAttributeMaxDynamicSharedMemorySize, LDS_BYTES);
  fused_kernel<<<256, 256, LDS_BYTES, stream>>>((const char*)d_in[0], (const bf16*)d_in[1], (const char*)d_in[2], (const char*)d_in[3],
                                                (const char*)d_in[4], (const char*)d_in[5], (const char*)d_in[8], (const bf16*)d_in[9],
                                                (const char*)d_in[10], (const bf16*)d_in[11], (const bf16*)d_in[12], (bf16*)d_out, base);
}
```

```cpp
#include <hip/hip_runtime.h>
#include <hip/hip_bf16.h>

typedef unsigned u32;
typedef unsigned long long u64;
typedef __bf16 bf16;
typedef bf16 bf16x2 __attribute__((ext_vector_type(2)));
typedef bf16 bf16x8 __attribute__((ext_vector_type(8)));
typedef bf16 bf16x16 __attribute__((ext_vector_type(16)));
typedef short s16x4 __attribute__((ext_vector_type(4)));
typedef short s16x8 __attribute__((ext_vector_type(8)));
typedef short s16x16 __attribute__((ext_vector_type(16)));
typedef float f32x2 __attribute__((ext_vector_type(2)));
typedef float f32x4 __attribute__((ext_vector_type(4)));
typedef u32 u32x2 __attribute__((ext_vector_type(2)));
typedef u32 u32x4 __attribute__((ext_vector_type(4)));
typedef __amdgpu_buffer_rsrc_t rsrc_t;
#define LAS __attribute__((address_space(3)))
typedef LAS char* lptr;
#define DI __device__ __forceinline__

constexpr u32 MB = 1u << 20;
constexpr u32 OFF_PART = 0;
constexpr u32 OFF_MEM = 94 * MB;
constexpr u32 OFF_CTRL = 96 * MB;
constexpr u32 C_SEL = OFF_CTRL;
constexpr u32 C_EP = OFF_CTRL + 256;
constexpr u32 C_RDY = OFF_CTRL + 512;
constexpr u32 C_KV = OFF_CTRL + 1024;
constexpr u32 C_KM = C_KV + 8192;
constexpr u32 C_OC = C_KM + 16384;
constexpr u32 C_HC = C_OC + 8192;
constexpr u32 C_END = C_HC + 8192;
constexpr u32 OFF_QX = 4 * MB, OFF_KX = 20 * MB, OFF_VX = 36 * MB;
constexpr u32 OFF_KM = 52 * MB, OFF_VM = 60 * MB, OFF_OF = 68 * MB, OFF_HF = 84 * MB, OFF_G = 92 * MB;
constexpr u32 WS_BYTES = 97 * MB;
DI u32 slot_off(int s) { const u32 r = (u32)(63 - s); return (r >> 1) * 65536u + (r & 1u) * 2048u; }
DI u32 tile_off(int t) { return (u32)(t >> 2) * 16384u + (u32)((t >> 1) & 1) * 4096u + (u32)(t & 1) * 1024u; }
constexpr int LDS_BYTES = 160 * 1024;
constexpr int SPIN_MAX = 1 << 15;
constexpr int AUX = 16;

DI u32x4 ld16(rsrc_t r, u32 v, u32 s) { return __builtin_amdgcn_raw_buffer_load_b128(r, v, s, AUX); }
DI u32x2 ld8(rsrc_t r, u32 v, u32 s) { return __builtin_amdgcn_raw_buffer_load_b64(r, v, s, AUX); }
DI u32 ld4(rsrc_t r, u32 v, u32 s) { return __builtin_amdgcn_raw_buffer_load_b32(r, v, s, AUX); }
DI void st16(rsrc_t r, u32x4 x, u32 v, u32 s) {
  __builtin_amdgcn_raw_buffer_store_b128(x, r, v, s, AUX);
  asm volatile("s_nop 1" ::"v"(x) : "memory");
}
DI void st8(rsrc_t r, u32x2 x, u32 v, u32 s) { __builtin_amdgcn_raw_buffer_store_b64(x, r, v, s, AUX); }
DI void st4(rsrc_t r, u32 x, u32 v, u32 s) { __builtin_amdgcn_raw_buffer_store_b32(x, r, v, s, AUX); }
#define MEMBAR() asm volatile("" ::: "memory")
#define WAITVM(n) asm volatile("s_waitcnt vmcnt(" #n ")" ::: "memory")
#define WAITVM0() do { MEMBAR(); __builtin_amdgcn_s_waitcnt(0x0F70); MEMBAR(); } while (0)
#define BAR() asm volatile("s_waitcnt lgkmcnt(0)\n\ts_barrier" ::: "memory")
#define SCHED() __builtin_amdgcn_sched_barrier(0)
#define PINA(x) asm volatile("" : "+a"(x))
#define PINV(x) asm volatile("" : "+v"(x))
constexpr u32 OOB = 0x80000000u;

DI void dma16(const char* sbase, u32 voff, u32 ldsdst) {
  u32 keep;
  asm volatile("s_mov_b32 %0, m0\n\ts_mov_b32 m0, %3\n\ts_nop 0\n\tglobal_load_lds_dwordx4 %1, %2\n\ts_mov_b32 m0, %0"
               : "=&s"(keep) : "v"(voff), "s"(sbase), "s"(ldsdst) : "memory");
}
DI void dma4(const char* sbase, u32 voff, u32 ldsdst) {
  u32 keep;
  asm volatile("s_mov_b32 %0, m0\n\ts_mov_b32 m0, %3\n\ts_nop 0\n\tglobal_load_lds_dword %1, %2\n\ts_mov_b32 m0, %0"
               : "=&s"(keep) : "v"(voff), "s"(sbase), "s"(ldsdst) : "memory");
}
#define PF 2
DI s16x4 trr(lptr p) { return __builtin_amdgcn_ds_read_tr16_b64_v4i16((LAS s16x4*)p); }
DI bf16x8 cat2(s16x4 a, s16x4 b) { return __builtin_bit_cast(bf16x8, __builtin_shufflevector(a, b, 0, 1, 2, 3, 4, 5, 6, 7)); }
DI bf16x16 cat4(s16x4 a, s16x4 b, s16x4 c, s16x4 d) {
  s16x8 x = __builtin_shufflevector(a, b, 0, 1, 2, 3, 4, 5, 6, 7), y = __builtin_shufflevector(c, d, 0, 1, 2, 3, 4, 5, 6, 7);
  return __builtin_bit_cast(bf16x16, __builtin_shufflevector(x, y, 0, 1, 2, 3, 4, 5, 6, 7, 8, 9, 10, 11, 12, 13, 14, 15));
}
DI u32 pk(float lo, float hi) { f32x2 v = {lo, hi}; return __builtin_bit_cast(u32, __builtin_convertvector(v, bf16x2)); }
DI u32 pk2(f32x2 v) { return __builtin_bit_cast(u32, __builtin_convertvector(v, bf16x2)); }
DI float rbf(float x) { return (float)(bf16)x; }
DI bf16x8 as8(u32x4 x) { return __builtin_bit_cast(bf16x8, x); }
DI f32x4 mfma32(bf16x8 a, bf16x8 b, f32x4 c) { return __builtin_amdgcn_mfma_f32_16x16x32_bf16(a, b, c, 0, 0, 0); }
DI f32x4 mfma16(u32x2 a, u32x2 b, f32x4 c) {
  return __builtin_amdgcn_mfma_f32_16x16x16bf16_1k(__builtin_bit_cast(s16x4, a), __builtin_bit_cast(s16x4, b), c, 0, 0, 0);
}
DI f32x4 smf(u32x4 a, bf16x16 b, f32x4 c, int idx) { return __builtin_amdgcn_smfmac_f32_16x16x64_bf16(as8(a), b, c, idx, 0, 0); }
DI float comb(float x, float y) {
  auto r = __builtin_amdgcn_permlane32_swap(__builtin_bit_cast(u32, x), __builtin_bit_cast(u32, y), false, false);
  return __builtin_bit_cast(float, (u32)r[0]) + __builtin_bit_cast(float, (u32)r[1]);
}
DI float maxall(float m) {
  auto r = __builtin_amdgcn_permlane32_swap(__builtin_bit_cast(u32, m), __builtin_bit_cast(u32, m), false, false);
  m = fmaxf(__builtin_bit_cast(float, (u32)r[0]), __builtin_bit_cast(float, (u32)r[1]));
  auto q = __builtin_amdgcn_permlane16_swap(__builtin_bit_cast(u32, m), __builtin_bit_cast(u32, m), false, false);
  return fmaxf(__builtin_bit_cast(float, (u32)q[0]), __builtin_bit_cast(float, (u32)q[1]));
}
DI f32x4 zero4() {
  u64 a, b;
  asm volatile("v_mov_b64 %0, 0" : "=v"(a));
  asm volatile("v_mov_b64 %0, 0" : "=v"(b));
  typedef u64 u64x2 __attribute__((ext_vector_type(2)));
  u64x2 r = {a, b};
  return __builtin_bit_cast(f32x4, r);
}
DI u32 umax8(u32x4 a, u32x4 b) { return max(max(max(a[0], a[1]), max(a[2], a[3])), max(max(b[0], b[1]), max(b[2], b[3]))); }
DI bool anyl(bool p) { return __builtin_amdgcn_ballot_w64(p) != 0; }
DI void waitflag(rsrc_t r, u32 v, u32 s, u64 lanes, u32 msk, u32 val, bool& dead, int sleep) {
  int n = 0;
#pragma unroll 1
  while (!dead) {
    MEMBAR();
    const u32 x = ld4(r, v, s);
    if ((__builtin_amdgcn_ballot_w64((x & msk) != val) & lanes) == 0) break;
    asm volatile("" : "+s"(n));
    if (++n > SPIN_MAX) dead = true;
    for (int i = 0; i < sleep; i++) __builtin_amdgcn_s_sleep(4);
  }
  MEMBAR();
}
DI u32 gettag(const char* wsb) {
  u32 t = *(const __attribute__((address_space(4))) u32*)(wsb + C_EP);
  t = (t + 4u) & ~3u;
  asm volatile("" : "+s"(t));
  return t;
}
constexpr float NEG = -3.0e38f;
constexpr float SCL = 0.08838834764831845f * 1.4426950408889634f;

DI void chain_role(lptr smem, rsrc_t ws, int h, int a, int w, int l, const char* Wq, const char* Wk, const char* Wv,
                   const char* Wo, const bf16* mem0, const char* wsb) {
  const int gb = l >> 4, q = (l >> 2) & 3, p = l & 3, c = l & 15;
  const u32 lds0 = (u32)(size_t)smem;
  bf16x16 Bq[2][8], Bk[2][8], Bv[8], Bo[8];
  bool dead = false;
  const int ci = 2 * h + a;
  const int rho = 2 * ci + (w & 1);
  const int rt = rho >> 4, gs = (rho >> 2) & 3, ru = rho & 3;
  const u32 rpiece = tile_off(rt) + (u32)ru * 256u + (u32)gs * 64u;
  const u32 rpub4 = c < 4 ? rpiece + (u32)(c & 3) * 16u + (u32)gb * 4u : OOB;
  const u32 rblk0 = (u32)(rho >> 1) * 16384u + (u32)(rho & 1) * 4096u;
  const u32 lane0 = l == 0 ? 0u : OOB;
  const u32x4 ones = {~0u, ~0u, ~0u, ~0u};
  u32 selw, rdy, flg, tag;
  {
    const u32 cp = (u32)(w >> 1) * 8192u;
    const u32 bx = (u32)((l & 15) ^ ((l >> 5) << 1));
    const u32 rowoff = (u32)(64 * w + (l >> 4)) * 8192u + (u32)h * 256u;
    auto issueQK = [&](const char* W, int half, int buf) {
#pragma unroll
      for (int it = 0; it < 16; it++) {
        const u32 cit = (u32)((((it >> 3) & 1) << 3) | (((it >> 2) & 1) << 2));
        dma16(W, rowoff + (u32)(half * 256 + it * 4) * 8192u + ((bx ^ cit) << 4), lds0 + buf * 65536 + (w * 16 + it) * 1024);
      }
    };
    auto issueV = [&](int buf) {
      const u32 bv = (u32)((l & 7) ^ ((l >> 5) << 1));
      const u32 ro = (u32)(128 * w + ((l >> 3) & 1) * 32 + (l >> 4)) * 8192u + (u32)h * 256u + (u32)a * 128u;
#pragma unroll
      for (int it = 0; it < 16; it++)
        dma16(Wv, ro + (u32)((it >> 3) * 64 + (it & 7) * 4) * 8192u + ((bv ^ (u32)(((it >> 2) & 1) << 2)) << 4),
              lds0 + buf * 65536 + (w * 16 + it) * 1024);
    };
    auto issueO = [&](int buf) {
      const u32 ro = (u32)(h * 128 + a * 64 + w * 16) * 1024u;
#pragma unroll
      for (int it = 0; it < 16; it++) {
        const u32 f = (u32)((((it >> 3) & 1) << 2) | (((it >> 2) & 1) << 1) | (it & 1));
        dma16(Wo, ro + (u32)it * 1024u + (((u32)l ^ (f << 1)) << 4), lds0 + buf * 65536 + (w * 16 + it) * 1024);
      }
    };
    const int rowl = 32 * (q & 1) + 16 * (q >> 1) + 2 * gb;
    const int fq = ((q & 1) << 2) | ((q >> 1) << 1) | (gb & 1);
    auto readQK = [&](bf16x16(&B)[2][8], int half, int buf) {
#pragma unroll
      for (int y = 0; y < 2; y++) {
        lptr b = smem + buf * 65536 + rowl * 256 + (((4 * w + 2 * y + (p >> 1)) ^ (fq << 1)) << 4) + (p & 1) * 8;
#pragma unroll
        for (int t = 0; t < 4; t++)
        {
          lptr bt = b + (t ^ w) * 16384;
          B[y][half * 4 + t] = cat4(trr(bt), trr(bt + 256), trr(bt + 2048), trr(bt + 2304));
          PINA(B[y][half * 4 + t]);
        }
      }
    };
    dma16((const char*)mem0, (u32)(w * 2048 + l * 16), lds0 + 135168u + (u32)w * 2048u);
    dma16((const char*)mem0, (u32)(w * 2048 + 1024 + l * 16), lds0 + 136192u + (u32)w * 2048u);
    issueQK(Wq, 0, 0);
    issueQK(Wq, 1, 1);
    SCHED();
#pragma unroll
    for (int k = 0; k < 8; k++) st16(ws, ones, rblk0 + (u32)l * 16u + (k & 3) * 1024u, OFF_PART + (k >> 2) * MB + cp);
#pragma unroll
    for (int k = 0; k < 4; k++) {
      const int s = 1 + k * 16 + (l >> 2);
      st16(ws, ones, s < 64 ? slot_off(s) + rpiece + (u32)(l & 3) * 16u : OOB, OFF_MEM + cp);
    }
    SCHED();
    tag = gettag(wsb);
    WAITVM(28); BAR(); readQK(Bq, 0, 0);
    {
      const int d0 = 64 * rt + 16 * ((c >> 1) & 1) + 4 * ru + gb, r0 = 2 * gs + (c & 1);
      LAS unsigned short* m0 = (LAS unsigned short*)(smem + 135168) + r0 * 512 + d0;
      const u32 mv = (u32)m0[0] | ((u32)m0[32] << 16);
      BAR();
      st4(ws, mv, rpub4, OFF_MEM + cp + slot_off(0));
      SCHED();
    }
    issueQK(Wk, 0, 0);
    WAITVM(29); BAR(); readQK(Bq, 1, 1); BAR(); issueQK(Wk, 1, 1);
    WAITVM(16); BAR();
    st4(ws, tag, w == 0 ? lane0 : OOB, C_RDY + (u32)ci * 4u);
    SCHED();
    readQK(Bk, 0, 0); BAR(); issueV(0);
    WAITVM(16); BAR(); readQK(Bk, 1, 1); BAR(); issueO(1);
    WAITVM(16); BAR();
    {
      const int f2 = ((q >> 1) << 1) | (gb & 1);
      lptr b = smem + (16 * (q >> 1) + 2 * gb) * 256 + (q & 1) * 128 + (((2 * w + (p >> 1)) ^ (f2 << 1)) << 4) + (p & 1) * 8;
#pragma unroll
      for (int t = 0; t < 8; t++)
      {
        lptr bt = b + (t ^ w) * 8192;
        Bv[t] = cat4(trr(bt), trr(bt + 256), trr(bt + 2048), trr(bt + 2304));
        PINV(Bv[t]);
      }
    }
    SCHED();
    selw = ld4(ws, 0, C_SEL); rdy = ld4(ws, (u32)l * 4u, C_RDY); flg = ld4(ws, 0, C_KV + (u32)(h * 64) * 4u);
    SCHED();
    WAITVM0(); BAR();
    {
      const int fo = ((q >> 1) << 2) | ((gb & 1) << 1) | (q & 1);
      const int er = 16 * (gb >> 1) + 8 * (q >> 1) + 4 * (gb & 1) + (q & 1);
#pragma unroll
      for (int T = 0; T < 8; T++) {
        lptr b = smem + 65536 + er * 1024 + (((16 * w + 2 * T + (p >> 1)) ^ (fo << 1)) << 4) + (p & 1) * 8;
        Bo[T] = cat4(trr(b), trr(b + 2048), trr(b + 32768), trr(b + 32768 + 2048));
        PINV(Bo[T]);
      }
    }
    BAR();
  }
  if (anyl(rdy != tag)) waitflag(ws, (u32)l * 4u, C_RDY, ~0ull, ~0u, tag, dead, 1);
  if (anyl((selw & ~3u) != tag)) { waitflag(ws, 0, C_SEL, 1ull, ~3u, tag, dead, 1); selw = ld4(ws, 0, C_SEL); }
  selw = __builtin_amdgcn_readfirstlane(selw);
  flg = __builtin_amdgcn_readfirstlane(flg);
  const u32 sel0 = (selw & 1u) * 8192u, sel1 = ((selw >> 1) & 1u) * 8192u;
  const u32 rgat = rblk0 + sel0 + (u32)(4 * gb + (c & 3)) * 64u + (u32)(c >> 2) * 16u;
  constexpr u32 GK = 1024u;
  const u32 pst = (u32)(w * 16 + (l >> 3)) * 16384u + (u32)((l >> 2) & 1) * 4096u + sel0 + (u32)ci * 64u + (u32)(l & 3) * 16u;
  const u32 kvlane = (u32)l * 16u;
  lptr Lq = smem, Lk = smem + 6144, Lx = smem + 12288, Lv = smem + 20480, La = smem + 24576, Lo = smem + 49152;
#pragma unroll
  for (int i = 0; i < 3; i++) *(LAS u32x4*)(smem + i * 4096 + w * 1024 + l * 16) = (u32x4){0u, 0u, 0u, 0u};
  auto fetch_kv = [&](int sn) {
    if ((flg & ~3u) != tag) waitflag(ws, 0, C_KV + (u32)(h * 64 + sn) * 4u, 1ull, ~3u, tag, dead, 1);
    const u32 so = (u32)(sn * 32 + h) * 8192u;
    u32x4 t[6];
    const u32 fl = ld4(ws, 0, C_KV + (u32)(h * 64 + sn + 1) * 4u);
    SCHED();
#pragma unroll
    for (int i = 0; i < 4; i++) t[i] = ld16(ws, kvlane, OFF_KX + so + (u32)(w - 2) * 4096u + i * 1024u);
#pragma unroll
    for (int i = 0; i < 2; i++) t[4 + i] = ld16(ws, kvlane, OFF_VX + so + (u32)(4 * a + 2 * (w - 2) + i) * 1024u);
    SCHED();
    flg = __builtin_amdgcn_readfirstlane(fl);
#pragma unroll
    for (int i = 0; i < 4; i++) *(LAS u32x4*)(Lx + (w - 2) * 4096 + i * 1024 + l * 16) = t[i];
#pragma unroll
    for (int i = 0; i < 2; i++) *(LAS u32x4*)(Lv + (w - 2) * 2048 + i * 1024 + l * 16) = t[4 + i];
  };
  if (w >= 2) fetch_kv(0);
  WAITVM0();
  BAR();
  const u32 kmoff = (l >> 5) == a ? kvlane + (u32)w * 1024u : OOB;
  const u32 flgoff = w == 2 ? lane0 : OOB;
  const int idx = (l & 2) ? 0xEEEE : 0x4444;
  const int wq = (4 * w + (c >> 2)) * 384 + (c & 3) * 32 + gb * 8;
  const int rq = (4 * w + gb) * 384 + q * 32 + p * 8;
  const bool up = (c & 2) != 0;
  u32 pmask = up ? 0xFFFF0000u : 0x0000FFFFu;
  u32 tagv = tag;
  PINV(pmask); PINV(tagv);
  lptr Ls = smem + 53248;
  lptr Lar[4] = {La, La + (w ^ 1) * 1024 + l * 16, La + (w ^ 2) * 1024 + l * 16, La + (w ^ 3) * 1024 + l * 16};
  lptr Lkw = Lk + wq, Lsw = Ls + 4096 + w * 256 + l * 4;
  PINV(Lar[1]); PINV(Lar[2]); PINV(Lar[3]); PINV(Lkw); PINV(Lsw);
  const int orow = 2 * (c >> 2) + (c & 1);
  u32x4 selB;
  {
    const int j = c & 7;
    const u32 one = (j & 1) ? 0x3F800000u : 0x00003F80u;
    selB[0] = (j >> 1) == 0 ? one : 0u; selB[1] = (j >> 1) == 1 ? one : 0u;
    selB[2] = (j >> 1) == 2 ? one : 0u; selB[3] = (j >> 1) == 3 ? one : 0u;
  }
  u32x4 one8 = {0x3F803F80u, 0x3F803F80u, 0x3F803F80u, 0x3F803F80u};
  PINV(one8); PINV(selB);
#define SM2(d0, d1, B, t)                                                                                            \
  asm volatile("v_smfmac_f32_16x16x64_bf16 %0, %2, %3, %5\n\tv_smfmac_f32_16x16x64_bf16 %1, %2, %4, %5"              \
               : "+v"(d0), "+v"(d1) : "v"(A[t]), "a"(B[0][t]), "a"(B[1][t]), "v"(idx));                              \
  SCHED()
#define VSTEP(t)                                                                                                     \
  asm volatile("v_smfmac_f32_16x16x64_bf16 %0, %1, %2, %3" : "+v"(v0) : "v"(A[t]), "v"(Bv[t]), "v"(idx));            \
  SCHED()
#define MF0(D, F, G) asm volatile("v_mfma_f32_16x16x32_bf16 %0, %1, %2, 0" : "=&v"(D) : "v"(F), "v"(G)); SCHED()
#define MF1(D, F, G) asm volatile("v_mfma_f32_16x16x32_bf16 %0, %1, %2, %0" : "+v"(D) : "v"(F), "v"(G)); SCHED()
#define FOLD(D) pk2(__builtin_shufflevector(D, D, 0, 1) + __builtin_shufflevector(D, D, 2, 3))
#define UNDEF(x) asm volatile("" : "=v"(x))
#define NAP(n) do { if (n) __builtin_amdgcn_s_sleep(n); } while (0)
#define KVD 0
#define D1 10
#define D2 8
#define D3 10

  for (int s = 0; s < 64; s++) {
    const u32 mslot = OFF_MEM + sel1 + slot_off(s) + tile_off(w);
    u32x4 A[8];
    u32x4 g0 = ld16(ws, kvlane, mslot); SCHED();
    u32x4 g1 = ld16(ws, kvlane, mslot + 16384u); SCHED();
    f32x4 q0 = zero4(), q1 = zero4(), k0 = zero4(), k1 = zero4(), v0 = zero4();
    u32 parS = OFF_PART + (u32)(s & 1) * MB, parT = parS + 8 * 16384u, rstS = parS ^ MB, rstT = parT ^ MB;
    u32 kmS = OFF_KM + (u32)(s * 32 + h) * 4096u, vmS = kmS + (OFF_VM - OFF_KM), pubS = OFF_MEM + sel1 + slot_off(s + 1);
    u32 flS = C_KM - 8u + (u32)(h * 128 + a) * 4u + (u32)s * 8u, flV = s > 0 ? flgoff : OOB;
    asm volatile("" : "+s"(parS), "+s"(parT), "+s"(rstS), "+s"(rstT), "+s"(kmS), "+s"(vmS), "+s"(pubS), "+s"(flS), "+v"(flV));
    SCHED();
    *(LAS u32x4*)(La + w * 1024 + l * 16) = g0;
    *(LAS u32x4*)(La + w * 1024 + 4096 + l * 16) = g1;
    SCHED();
    if (__builtin_expect(anyl(umax8(g0, g1) == 0xFFFFFFFFu), 0)) {
      int n = 0;
      asm volatile("" : "+v"(g0), "+v"(g1));
#pragma unroll 1
      while (anyl(umax8(g0, g1) == 0xFFFFFFFFu) && !dead) {
        MEMBAR();
        g0 = ld16(ws, kvlane, mslot);
        g1 = ld16(ws, kvlane, mslot + 16384u);
        WAITVM0();
        if (++n > SPIN_MAX) dead = true;
      }
      *(LAS u32x4*)(La + w * 1024 + l * 16) = g0;
      *(LAS u32x4*)(La + w * 1024 + 4096 + l * 16) = g1;
    }
    BAR();
    A[0] = g0; A[4] = g1;
#define RDA(t) A[t] = *(LAS u32x4*)(Lar[t & 3] + (t & 4) * 1024); SCHED()
    RDA(1); RDA(2);
    SM2(q0, q1, Bq, 0);
    RDA(3);
    SM2(q0, q1, Bq, 4);
    RDA(5);
    SM2(k0, k1, Bk, 0);
    RDA(6);
    SM2(k0, k1, Bk, 4);
    RDA(7);
    SM2(q0, q1, Bq, 1);
    __builtin_amdgcn_raw_buffer_store_b128(ones, ws, pst, rstS, AUX); SCHED();
    SM2(q0, q1, Bq, 2);
    __builtin_amdgcn_raw_buffer_store_b128(ones, ws, pst, rstT, AUX); SCHED();
    SM2(q0, q1, Bq, 3);
    st4(ws, tagv, flV, flS); SCHED();
    SM2(q0, q1, Bq, 5);
    const u32x4 X0 = *(LAS u32x4*)(Lx + w * 1024 + l * 16), X1 = *(LAS u32x4*)(Lx + 4096 + w * 1024 + l * 16);
    SCHED();
    SM2(q0, q1, Bq, 6); SM2(q0, q1, Bq, 7);
    const u32x2 vxa = *(LAS u32x2*)(Lv + w * 1024 + l * 16);
    asm volatile("" ::: "memory");
    const u32x2 vxb = *(LAS u32x2*)(Lv + w * 1024 + l * 16 + 8);
    SCHED();
    SM2(k0, k1, Bk, 1);
    asm volatile("" : "+v"(q0), "+v"(q1));
    {
      const u32 a0 = FOLD(q0), a1 = FOLD(q1);
      *(LAS u32*)(Lq + wq) = a0; *(LAS u32*)(Lq + wq + 4) = a0; SCHED();
      SM2(k0, k1, Bk, 2);
      *(LAS u32*)(Lq + wq + 128) = a1; *(LAS u32*)(Lq + wq + 132) = a1; SCHED();
    }
    SM2(k0, k1, Bk, 3);
    const bf16x8 Fq = cat2(trr(Lq + rq), trr(Lq + rq + 128));
    SCHED();
    f32x4 D[8];
    SM2(k0, k1, Bk, 5);
    u32x4 VA = {0u, 0u, vxa[0], vxa[1]}, VB = {0u, 0u, vxb[0], vxb[1]};
    PINV(VA); PINV(VB);
    SCHED();
    SM2(k0, k1, Bk, 6);
    D[0] = zero4(); D[1] = zero4();
    SCHED();
    SM2(k0, k1, Bk, 7);
    f32x4 sM, s0, s1;
    MF0(s0, X0, Fq);
    MF0(s1, X1, Fq);
    VSTEP(0);
    asm volatile("" : "+v"(k0), "+v"(k1));
    { const u32 a0 = FOLD(k0), a1 = FOLD(k1); *(LAS u32*)(Lkw) = a0; *(LAS u32*)(Lkw + 128) = a1; SCHED(); }
    const bf16x8 Fk = cat2(trr(Lk + rq), trr(Lk + rq + 128));
    SCHED();
    VSTEP(1);
    asm volatile("" : "+v"(s0), "+v"(s1));
    {
      const f32x4 x = {up ? s1[0] : s0[0], up ? s1[1] : s0[1], up ? s1[2] : s0[2], up ? s1[3] : s0[3]};
      SCHED();
      VSTEP(2);
      *(LAS f32x4*)(Ls + w * 1024 + l * 16) = x;
      SCHED();
    }
    VSTEP(3);
    D[2] = zero4(); SCHED();
    VSTEP(4);
    D[3] = zero4(); SCHED();
    VSTEP(5);
    MF0(sM, Fk, Fq);
    VSTEP(6); VSTEP(7);
    st16(ws, __builtin_bit_cast(u32x4, Fk), kmoff, kmS);
    asm volatile("" : "+v"(sM));
    *(LAS float*)(Lsw) = up ? sM[1] : sM[0];
    SCHED();
    BAR();
    f32x4 xs[4];
    float ms[4];
#pragma unroll
    for (int j = 0; j < 4; j++) ms[j] = *(LAS float*)(Ls + 4096 + j * 256 + l * 4);
    SCHED();
#pragma unroll
    for (int j = 0; j < 4; j++) { xs[j] = *(LAS f32x4*)(Ls + j * 1024 + l * 16); SCHED(); }
    asm volatile("" : "+v"(v0));
    const u32 vm = FOLD(v0);
    st4(ws, vm, (u32)l * 4u + (u32)(4 * a + w) * 256u, vmS);
    if (s == 63) break;
    VA[0] = vm; VB[0] = vm;
    PINV(VA); PINV(VB);
    SCHED();
    float sm = ms[0] + ms[1], sn = ms[2] + ms[3];
    PINV(sm); PINV(sn);
    sm += sn;
    PINV(sm);
    SCHED();
    const f32x4 sx = (xs[0] + xs[1]) + (xs[2] + xs[3]);
    float m, mt;
    asm volatile("v_max3_f32 %0, %2, %3, %6\n\tv_max3_f32 %0, %0, %4, %5\n\ts_nop 1\n\t"
                 "v_max_f32_dpp %1, %0, %0 quad_perm:[2,3,0,1] row_mask:0xf bank_mask:0xf\n\t"
                 "v_max_f32_dpp %0, %0, %0 quad_perm:[2,3,0,1] row_mask:0xf bank_mask:0xf\n\ts_nop 1\n\t"
                 "v_permlane32_swap_b32 %0, %1\n\tv_max_f32 %0, %0, %1\n\tv_mov_b32 %1, %0\n\ts_nop 1\n\t"
                 "v_permlane16_swap_b32 %0, %1\n\tv_max_f32 %0, %0, %1"
                 : "=&v"(m), "=&v"(mt) : "v"(sx[0]), "v"(sx[1]), "v"(sx[2]), "v"(sx[3]), "v"(sm));
    m *= -SCL;
    const f32x2 m2 = {m, m}, scl2 = {SCL, SCL};
#define EXP(t) pk(__builtin_amdgcn_exp2f(t[0]), __builtin_amdgcn_exp2f(t[1]))
    float tM;
    asm volatile("v_fma_f32 %0, %1, %2, %3" : "=v"(tM) : "v"(sm), "s"(SCL), "v"(m));
    const f32x2 t0 = __builtin_elementwise_fma(__builtin_shufflevector(sx, sx, 0, 1), scl2, m2),
                t1 = __builtin_elementwise_fma(__builtin_shufflevector(sx, sx, 2, 3), scl2, m2);
    SCHED();
    const float eM = __builtin_amdgcn_exp2f(tM);
    const u32x4 P = {pk(eM, eM) & pmask, 0u, EXP(t0), EXP(t1)};
    f32x4 sum, oa, ob;
    asm volatile("s_nop 1\n\tv_mfma_f32_16x16x32_bf16 %0, %3, %4, 0\n\tv_mfma_f32_16x16x32_bf16 %2, %6, %4, 0\n\t"
                 "v_mfma_f32_16x16x32_bf16 %1, %5, %4, 0"
                 : "=&v"(sum), "=&v"(oa), "=&v"(ob) : "v"(one8), "v"(P), "v"(VA), "v"(VB));
    {
      float rs;
      f32x4 o;
      asm volatile("v_add_f32_dpp %0, %1, %1 quad_perm:[2,3,0,1] row_mask:0xf bank_mask:0xf\n\ts_nop 0\n\tv_rcp_f32 %0, %0" : "=&v"(rs) : "v"(sum[0]));
      D[4] = zero4(); D[5] = zero4(); D[6] = zero4();
      asm volatile("v_add_f32_dpp %0, %4, %8 quad_perm:[2,3,0,1] row_mask:0xf bank_mask:0xf\n\t"
                   "v_add_f32_dpp %1, %5, %9 quad_perm:[2,3,0,1] row_mask:0xf bank_mask:0xf\n\t"
                   "v_add_f32_dpp %2, %6, %10 quad_perm:[2,3,0,1] row_mask:0xf bank_mask:0xf\n\t"
                   "v_add_f32_dpp %3, %7, %11 quad_perm:[2,3,0,1] row_mask:0xf bank_mask:0xf"
                   : "=&v"(o[0]), "=&v"(o[1]), "=&v"(o[2]), "=&v"(o[3])
                   : "v"(ob[0]), "v"(ob[1]), "v"(ob[2]), "v"(ob[3]), "v"(oa[0]), "v"(oa[1]), "v"(oa[2]), "v"(oa[3]));
      const f32x2 r2 = {rs, rs};
      u32x2 oo = {pk2(__builtin_shufflevector(o, o, 0, 1) * r2), pk2(__builtin_shufflevector(o, o, 2, 3) * r2)};
      *(LAS u32x2*)(Lo + (orow + ((c & 2) << 2)) * 144 + w * 32 + gb * 8) = oo;
    }
    WAITVM(2);
    BAR();
    const u32x4 Ao = *(LAS u32x4*)(Lo + orow * 144 + gb * 32 + ((c >> 1) & 1) * 16);
    SCHED();
    D[7] = zero4();
    SCHED();
#define OST(T) asm volatile("v_smfmac_f32_16x16x64_bf16 %0, %1, %2, %3" : "+v"(D[T]) : "v"(Ao), "v"(Bo[T]), "v"(idx)); SCHED()
#define OAD(T) asm volatile("v_pk_add_f32 %0, %1, %2" : "=v"(f[T]) : "v"(__builtin_shufflevector(D[T], D[T], 0, 1)), "v"(__builtin_shufflevector(D[T], D[T], 2, 3))); SCHED()
#define OCV(P_, i, T) asm volatile("v_cvt_pk_bf16_f32 %0, %1, %2" : "=v"(P_[i]) : "v"(f[T][0]), "v"(f[T][1])); SCHED()
#define NOP(n) asm volatile("s_nop " #n); SCHED()
    {
      u32 pv[4], pw[4];
      f32x2 f[8];
      OST(0); OST(1); OST(2);
      OAD(0); OST(3);
      OAD(1); OCV(pv, 0, 0); OST(4);
      OAD(2); OCV(pv, 1, 1); OST(5);
      OAD(3); OCV(pv, 2, 2); OST(6);
      OAD(4); OCV(pv, 3, 3); OST(7);
      __builtin_amdgcn_raw_buffer_store_b128((u32x4){pv[0], pv[1], pv[2], pv[3]}, ws, pst, parS, AUX); SCHED();
      OAD(5); OCV(pw, 0, 4); NOP(0); OCV(pw, 1, 5); NOP(0);
      OAD(6); NOP(0); OCV(pw, 2, 6); NOP(0);
      OAD(7); NOP(0); OCV(pw, 3, 7);
      asm volatile("" ::"v"(pv[0]), "v"(pv[1]), "v"(pv[2]), "v"(pv[3]));
      st16(ws, (u32x4){pw[0], pw[1], pw[2], pw[3]}, pst, parT);
    }
    if (w < 2) {
      u32x4 g[4], gl[4];
      NAP(D1);
      SCHED();
#pragma unroll
      for (int k = 0; k < 4; k++) { g[k] = ld16(ws, rgat, parS + k * GK); SCHED(); }
      NAP(D2);
      SCHED();
#pragma unroll
      for (int k = 0; k < 4; k++) { gl[k] = ld16(ws, rgat, parS + k * GK); SCHED(); }
      float tot;
#define REDUCE(G)                                                                                                    \
  {                                                                                                                  \
    f32x4 acc;                                                                                                       \
    MF0(acc, G[0], selB); MF1(acc, G[1], selB); MF1(acc, G[2], selB); MF1(acc, G[3], selB);                           \
    asm volatile("s_nop 7" : "+v"(acc));                                                                             \
    const f32x2 t_ = __builtin_shufflevector(acc, acc, 0, 1) + __builtin_shufflevector(acc, acc, 2, 3); tot = t_[0] + t_[1];                                                               \
  }
      REDUCE(g);
      if (__builtin_expect(anyl(tot != tot), 0)) {
        REDUCE(gl);
        int n = 0;
#pragma unroll 1
        while (anyl(tot != tot) && !dead) {
          MEMBAR();
#pragma unroll
          for (int k = 0; k < 4; k++) { g[k] = ld16(ws, rgat, parS + k * GK); SCHED(); }
          REDUCE(g);
          if (++n > SPIN_MAX) dead = true;
        }
      }
      const float hi = __builtin_bit_cast(float, __builtin_amdgcn_update_dpp(0, __builtin_bit_cast(int, tot), 0x104, 0xf, 0xf, true));
      st4(ws, pk(tot, hi), rpub4, pubS);
      NAP(D3);
      asm volatile("" ::"v"(gl[0]), "v"(gl[1]), "v"(gl[2]), "v"(gl[3]));
    } else {
      if (s < 62) {
        NAP(KVD);
        fetch_kv(s + 1);
      } else {
        WAITVM0();
      }
    }
    BAR();
  }
  WAITVM0();
  BAR();
  if (w == 2 && l == 0) st4(ws, tag, 0, C_KM + (u32)((h * 64 + 63) * 2 + a) * 4u);
  if (ci == 0 && w == 1 && l == 0) st4(ws, tag, 0, C_EP);
}

DI void stageW(const char* W, u32 off0, u32 rs, u32 ldsbuf, int w, int l) {
  const u32 bx = (u32)((l & 15) ^ ((l >> 4) << 1));
  const u32 ro = off0 + (u32)(64 * w + (l >> 4)) * rs;
#pragma unroll
  for (int it = 0; it < 16; it++)
    dma16(W, ro + (u32)(it * 4) * rs + ((bx ^ (u32)(((it >> 1) & 1) << 3)) << 4), ldsbuf + (w * 16 + it) * 1024);
}
template <bool AG>
DI void readW(bf16x8 (&B)[2][16], int half, lptr buf, int w, int l) {
  const int gb = l >> 4, q = (l >> 2) & 3, p = l & 3;
  const int f = ((gb & 1) << 2) | q;
#pragma unroll
  for (int y = 0; y < 2; y++) {
    lptr b = buf + (8 * gb + q) * 256 + (((4 * w + 2 * y + (p >> 1)) ^ (f << 1)) << 4) + (p & 1) * 8;
#pragma unroll
    for (int k = 0; k < 8; k++) {
      B[y][half * 8 + k] = cat2(trr(b + k * 8192), trr(b + k * 8192 + 1024));
    }
  }
}
DI void stageX(const char* x, int s, u32 ldsbuf, int w, int l) {
  const u32 ro = (u32)(32 * s + 16 * (w >> 1) + (l & 15)) * 1024u + (u32)((w & 1) * 8) * 64u + (u32)(l >> 4) * 16u;
#pragma unroll
  for (int it = 0; it < 8; it++) dma16(x, ro + it * 64u, ldsbuf + (w * 8 + it) * 1024);
}

template <bool K, bool V, bool Q>
DI void qkv_seg(lptr xb, rsrc_t ws, u32 so, int w, int l, const bf16x8 (&Bq)[2][16], const bf16x8 (&Bk)[2][16], const bf16x8 (&Bv)[2][16]) {
  const f32x4 z4 = {0.f, 0.f, 0.f, 0.f};
  f32x4 aq[2][2], ak[2][2], av[2][2];
#pragma unroll
  for (int i = 0; i < 4; i++) { aq[i >> 1][i & 1] = z4; ak[i >> 1][i & 1] = z4; av[i >> 1][i & 1] = z4; }
  bf16x8 n0 = *(LAS bf16x8*)(xb), n1 = *(LAS bf16x8*)(xb + 16384);
#pragma unroll
  for (int ks = 0; ks < 16; ks++) {
    bf16x8 x0 = n0, x1 = n1;
    if (ks < 15) { n0 = *(LAS bf16x8*)(xb + (ks + 1) * 1024); n1 = *(LAS bf16x8*)(xb + 16384 + (ks + 1) * 1024); }
    asm volatile("" : "+v"(x0), "+v"(x1));
#pragma unroll
    for (int y = 0; y < 2; y++) {
      if (K) { ak[0][y] = mfma32(Bk[y][ks], x0, ak[0][y]); ak[1][y] = mfma32(Bk[y][ks], x1, ak[1][y]); }
      if (V) { av[0][y] = mfma32(x0, Bv[y][ks], av[0][y]); av[1][y] = mfma32(x1, Bv[y][ks], av[1][y]); }
      if (Q) { aq[0][y] = mfma32(Bq[y][ks], x0, aq[0][y]); aq[1][y] = mfma32(Bq[y][ks], x1, aq[1][y]); }
    }
    if (K) asm volatile("" : "+a"(ak[0][0]), "+a"(ak[0][1]), "+a"(ak[1][0]), "+a"(ak[1][1]));
    if (V) asm volatile("" : "+a"(av[0][0]), "+a"(av[0][1]), "+a"(av[1][0]), "+a"(av[1][1]));
    if (Q) asm volatile("" : "+a"(aq[0][0]), "+a"(aq[0][1]), "+a"(aq[1][0]), "+a"(aq[1][1]));
  }
#pragma unroll
  for (int t = 0; t < 2; t++) {
    if (K) {
      u32x4 kk = {pk(ak[t][0][0], ak[t][0][1]), pk(ak[t][0][2], ak[t][0][3]), pk(ak[t][1][0], ak[t][1][1]), pk(ak[t][1][2], ak[t][1][3])};
      st16(ws, kk, (u32)l * 16u + (u32)w * 1024u, OFF_KX + so + t * 4096u);
    }
    if (V) {
      u32x4 vv = {pk(av[0][t][0], av[0][t][1]), pk(av[0][t][2], av[0][t][3]), pk(av[1][t][0], av[1][t][1]), pk(av[1][t][2], av[1][t][3])};
      st16(ws, vv, (u32)l * 16u + (u32)(2 * w + t) * 1024u, OFF_VX + so);
    }
    if (Q) {
      u32x4 qq = {pk(aq[t][0][0], aq[t][0][1]), pk(aq[t][0][2], aq[t][0][3]), pk(aq[t][1][0], aq[t][1][1]), pk(aq[t][1][2], aq[t][1][3])};
      st16(ws, qq, (u32)l * 16u + (u32)w * 1024u, OFF_QX + so + t * 4096u);
    }
  }
}

DI u32 qkv_role(lptr smem, rsrc_t ws, const char* wsb, int h, int j, int w, int l, const char* x, const char* Wq, const char* Wk, const char* Wv,
               const char* Wo, const char* w1) {
  const u32 lds0 = (u32)(size_t)smem;
  bf16x8 Bq[2][16], Bk[2][16], Bv[2][16];
  const u32 ho = (u32)h * 256u, hh = 256u * 8192u;
  const u32 fl = (w == 0 && l == 0) ? 0u : OOB;
  stageW(Wk, ho, 8192u, lds0, w, l);
  stageW(Wk, ho + hh, 8192u, lds0 + 65536, w, l);
  {
    const int u = (h * 6 + j) * 4 + w;
    const char* p = u < 256 ? Wv : u < 512 ? Wo : u < 640 || PF == 1 ? x : w1;
    const u32 o = (u32)(u < 640 ? u & 255 : PF == 1 || u >= 752 ? 0 : u - 640) * 16384u + (u32)l * 128u;
    dma4(p, o, lds0 + 131072u + (u32)w * 8192u);
    dma4(p, o + 8192u, lds0 + 131072u + (u32)w * 8192u + 256u);
  }
  SCHED();
  const u32 tag = gettag(wsb);
  WAITVM(18); BAR(); readW<true>(Bk, 0, smem, w, l); BAR(); stageX(x, j, lds0 + 131072, w, l); stageW(Wv, ho, 8192u, lds0, w, l);
  WAITVM(26); BAR();
  readW<true>(Bk, 1, smem + 65536, w, l); BAR(); stageW(Wv, ho + hh, 8192u, lds0 + 65536, w, l);
  WAITVM(32); BAR();
  qkv_seg<true, false, false>(smem + 131072 + l * 16, ws, (u32)(j * 32 + h) * 8192u, w, l, Bq, Bk, Bv);
  WAITVM(18); BAR(); readW<false>(Bv, 0, smem, w, l);
  WAITVM(2); BAR(); readW<false>(Bv, 1, smem + 65536, w, l);
  qkv_seg<false, true, false>(smem + 131072 + l * 16, ws, (u32)(j * 32 + h) * 8192u, w, l, Bq, Bk, Bv);
  BAR();
  stageW(Wq, ho, 8192u, lds0, w, l);
  WAITVM(16); BAR();
  st4(ws, tag + 1u, fl, C_KV + (u32)(h * 64 + j) * 4u);
  stageW(Wq, ho + hh, 8192u, lds0 + 65536, w, l);
  WAITVM(16); BAR(); readW<true>(Bq, 0, smem, w, l); BAR(); stageX(x, j + 6, lds0, w, l);
  WAITVM(8); BAR(); readW<true>(Bq, 1, smem + 65536, w, l);
  qkv_seg<false, false, true>(smem + 131072 + l * 16, ws, (u32)(j * 32 + h) * 8192u, w, l, Bq, Bk, Bv);
  WAITVM(0); BAR();
  st4(ws, tag + 2u, fl, C_KV + (u32)(h * 64 + j) * 4u);
  int buf = 0;
  for (int s = j + 6; s < 64; s += 6) {
    if (s + 6 < 64) stageX(x, s + 6, lds0 + (buf ^ 1) * 32768, w, l);
    qkv_seg<true, true, true>(smem + buf * 32768 + l * 16, ws, (u32)(s * 32 + h) * 8192u, w, l, Bq, Bk, Bv);
    WAITVM(0); BAR();
    st4(ws, tag + 2u, fl, C_KV + (u32)(h * 64 + s) * 4u);
    buf ^= 1;
  }
  return tag;
}

DI void ffn1_role(lptr smem, rsrc_t ws, u32 tag, int cg, int jj, int w, int l, const char* x, const char* w1, const bf16* b1, const bf16* gw) {
  const u32 lds0 = (u32)(size_t)smem;
  bf16x8 B[2][16], Gf[16];
  stageW(w1, (u32)cg * 256u, 3584u, lds0, w, l);
  stageW(w1, (u32)cg * 256u + 256u * 3584u, 3584u, lds0 + 65536, w, l);
  float bias[8];
#pragma unroll
  for (int i = 0; i < 8; i++) bias[i] = (float)b1[cg * 128 + 32 * w + 16 * (i >> 2) + 4 * (l >> 4) + (i & 3)];
#pragma unroll
  for (int ks = 0; ks < 16; ks++) Gf[ks] = *(const bf16x8*)(gw + 32 * ks + 8 * (l >> 4));
  WAITVM(0); BAR(); readW<true>(B, 0, smem, w, l); readW<true>(B, 1, smem + 65536, w, l); BAR();
  stageX(x, jj, lds0, w, l);
  WAITVM(0); BAR();
  const f32x4 z4 = {0.f, 0.f, 0.f, 0.f};
  const bool dog = (cg == 0 && w == 0);
  int buf = 0;
  for (int s = jj; s < 64; s += 9) {
    if (s + 9 < 64) stageX(x, s + 9, lds0 + (buf ^ 1) * 32768, w, l);
    lptr xb = smem + buf * 32768 + l * 16;
    f32x4 a[2][2] = {{z4, z4}, {z4, z4}};
#pragma unroll
    for (int ks = 0; ks < 16; ks++) {
      const bf16x8 x0 = *(LAS bf16x8*)(xb + ks * 1024), x1 = *(LAS bf16x8*)(xb + 16384 + ks * 1024);
#pragma unroll
      for (int y = 0; y < 2; y++) { a[0][y] = mfma32(B[y][ks], x0, a[0][y]); a[1][y] = mfma32(B[y][ks], x1, a[1][y]); }
    }
#pragma unroll
    for (int t = 0; t < 2; t++) {
      float r[8];
#pragma unroll
      for (int i = 0; i < 8; i++) {
        const float hv = rbf(rbf(a[t][i >> 2][i & 3]) + bias[i]);
        const float z = hv > 0.f ? hv : 0.f;
        r[i] = z * z;
      }
      u32x4 o = {pk(r[0], r[1]), pk(r[2], r[3]), pk(r[4], r[5]), pk(r[6], r[7])};
      st16(ws, o, (u32)l * 16u + (u32)(cg * 4 + w) * 1024u, OFF_HF + (u32)(s * 2 + t) * 57344u);
    }
    if (dog) {
      f32x4 g0 = z4, g1 = z4;
#pragma unroll
      for (int ks = 0; ks < 16; ks++) {
        g0 = mfma32(Gf[ks], *(LAS bf16x8*)(xb + ks * 1024), g0);
        g1 = mfma32(Gf[ks], *(LAS bf16x8*)(xb + 16384 + ks * 1024), g1);
      }
      const float zz = rbf(l < 16 ? g0[0] : g1[0]);
      const float gg = rbf(1.f / (1.f + __expf(-zz)));
      if (l < 32) st4(ws, __builtin_bit_cast(u32, gg), (u32)l * 4u, OFF_G + (u32)s * 128u);
    }
    WAITVM(0); BAR();
    if (w == 0 && l == 0) st4(ws, tag, 0, C_HC + (u32)s * 128u + (u32)cg * 4u);
    buf ^= 1;
  }
}

DI void attn_role(rsrc_t ws, u32 tag, int u, int l) {
  const int h = u & 31, gb = l >> 4, c = l & 15;
  bool dead = false;
  const f32x4 z4 = {0.f, 0.f, 0.f, 0.f};
  const u32x4 one8 = {0x3F803F80u, 0x3F803F80u, 0x3F803F80u, 0x3F803F80u};
  const u32x2 one4 = {0x3F803F80u, 0x3F803F80u};
  for (int s = u >> 5; s < 64; s += 4) {
    const u32 so = (u32)(s * 32 + h) * 8192u, sm = (u32)(s * 32 + h) * 4096u, lo = (u32)l * 16u;
    waitflag(ws, 0, C_KV + (u32)(h * 64 + s) * 4u, 1ull, ~0u, tag + 2u, dead, 2);
    u32x4 Q[2][4], K[2][4], V[8], KM[4];
    u32x2 VM[8];
#pragma unroll
    for (int i = 0; i < 8; i++) {
      Q[i >> 2][i & 3] = ld16(ws, lo + (i & 3) * 1024u, OFF_QX + so + (i >> 2) * 4096u);
      K[i >> 2][i & 3] = ld16(ws, lo + (i & 3) * 1024u, OFF_KX + so + (i >> 2) * 4096u);
      V[i] = ld16(ws, lo + (i & 3) * 1024u, OFF_VX + so + (i >> 2) * 4096u);
    }
    SCHED();
    f32x4 s00 = z4, s01 = z4, s11 = z4;
#pragma unroll
    for (int ks = 0; ks < 4; ks++) {
      s00 = mfma32(as8(K[0][ks]), as8(Q[0][ks]), s00);
      s01 = mfma32(as8(K[0][ks]), as8(Q[1][ks]), s01);
      s11 = mfma32(as8(K[1][ks]), as8(Q[1][ks]), s11);
    }
#pragma unroll
    for (int i = 0; i < 4; i++)
      if (4 * gb + i > c) { s00[i] = NEG; s11[i] = NEG; }
    waitflag(ws, (u32)(l & 1) * 4u, C_KM + (u32)((h * 64 + s) * 2) * 4u, 3ull, ~0u, tag, dead, s < 60 ? 2 : 0);
#pragma unroll
    for (int i = 0; i < 4; i++) KM[i] = ld16(ws, lo + i * 1024u, OFF_KM + sm);
#pragma unroll
    for (int i = 0; i < 8; i++) VM[i] = (u32x2){ld4(ws, (u32)l * 4u + i * 256u, OFF_VM + sm), 0u};
    SCHED();
    f32x4 m0 = z4, m1 = z4;
#pragma unroll
    for (int ks = 0; ks < 4; ks++) { m0 = mfma32(as8(KM[ks]), as8(Q[0][ks]), m0); m1 = mfma32(as8(KM[ks]), as8(Q[1][ks]), m1); }
#pragma unroll
    for (int t = 0; t < 2; t++) {
      const f32x4 sM = t ? m1 : m0, sa = t ? s01 : s00;
      float m = fmaxf(fmaxf(sM[0], sM[1]), fmaxf(fmaxf(sa[0], sa[1]), fmaxf(sa[2], sa[3])));
      if (t) m = fmaxf(m, fmaxf(fmaxf(s11[0], s11[1]), fmaxf(s11[2], s11[3])));
      m = maxall(m) * SCL;
      float pM[4], p0[4], p1[4];
#pragma unroll
      for (int i = 0; i < 4; i++) {
        pM[i] = i < 2 ? __builtin_amdgcn_exp2f(__builtin_fmaf(sM[i], SCL, -m)) : 0.f;
        p0[i] = __builtin_amdgcn_exp2f(__builtin_fmaf(sa[i], SCL, -m));
        p1[i] = t ? __builtin_amdgcn_exp2f(__builtin_fmaf(s11[i], SCL, -m)) : 0.f;
      }
      const u32x2 PM = {pk(pM[0], pM[1]), 0u};
      const u32x4 PX = {pk(p0[0], p0[1]), pk(p0[2], p0[3]), pk(p1[0], p1[1]), pk(p1[2], p1[3])};
      const float rs = __builtin_amdgcn_rcpf(mfma16(one4, PM, z4)[0] + mfma32(as8(one8), as8(PX), z4)[0]);
#pragma unroll
      for (int ks = 0; ks < 4; ks++) {
        f32x4 oa = mfma32(as8(V[2 * ks]), as8(PX), z4) + mfma16(VM[2 * ks], PM, z4);
        f32x4 ob = mfma32(as8(V[2 * ks + 1]), as8(PX), z4) + mfma16(VM[2 * ks + 1], PM, z4);
        u32x4 o = {pk(oa[0] * rs, oa[1] * rs), pk(oa[2] * rs, oa[3] * rs), pk(ob[0] * rs, ob[1] * rs), pk(ob[2] * rs, ob[3] * rs)};
        st16(ws, o, lo + ks * 1024u, OFF_OF + (u32)(s * 2 + t) * 131072u + (u32)h * 4096u);
      }
    }
    WAITVM(0);
    if (l == 0) st4(ws, tag, 0, C_OC + (u32)s * 128u + (u32)h * 4u);
  }
}

DI void out_role(lptr smem, rsrc_t ws, u32 tag, int grp, int xt, int sl, int w, int l, const char* Wo, const char* w2, const bf16* b2, bf16* out) {
  const u32 lds0 = (u32)(size_t)smem + (u32)w * 32768u;
  lptr wb = smem + w * 32768;
  const int gb = l >> 4, q = (l >> 2) & 3, p = l & 3, c = l & 15;
  bf16x8 A[2][32], A2[2][14];
  bool dead = false;
  {
    const u32 ro = (u32)(l >> 2) * 1024u + (u32)sl * 64u + (u32)(l & 3) * 16u;
    auto issue = [&](const char* W, int row0, int n, int buf) {
#pragma unroll
      for (int it = 0; it < 16; it++)
        if (it < n) dma16(W, ro + (u32)(row0 + it * 16) * 1024u, lds0 + buf * 16384 + it * 1024);
    };
    lptr b = wb + (4 * gb + q) * 64 + p * 16;
    issue(w2, w * 448, 16, 0);
    issue(w2, w * 448 + 256, 12, 1);
    WAITVM(12);
#pragma unroll
    for (int k = 0; k < 8; k++)
#pragma unroll
      for (int y = 0; y < 2; y++) { A2[y][k] = cat2(trr(b + k * 2048 + y * 8), trr(b + k * 2048 + 1024 + y * 8)); }
    asm volatile("s_waitcnt lgkmcnt(0)" ::: "memory");
    issue(Wo, w * 1024, 16, 0);
    WAITVM(16);
#pragma unroll
    for (int k = 0; k < 6; k++)
#pragma unroll
      for (int y = 0; y < 2; y++) { A2[y][8 + k] = cat2(trr(b + 16384 + k * 2048 + y * 8), trr(b + 16384 + k * 2048 + 1024 + y * 8)); }
#pragma unroll
    for (int r = 0; r < 4; r++) {
      asm volatile("s_waitcnt lgkmcnt(0)" ::: "memory");
      if (r < 3) { issue(Wo, w * 1024 + (r + 1) * 256, 16, (r + 1) & 1); WAITVM(16); } else { WAITVM(0); }
#pragma unroll
      for (int k = 0; k < 8; k++)
#pragma unroll
        for (int y = 0; y < 2; y++)
        { A[y][r * 8 + k] = cat2(trr(b + (r & 1) * 16384 + k * 2048 + y * 8), trr(b + (r & 1) * 16384 + k * 2048 + 1024 + y * 8)); }
    }
  }
  float bias[8];
#pragma unroll
  for (int i = 0; i < 8; i++) bias[i] = (float)b2[sl * 32 + 8 * gb + i];
#define LDO(v, s) ld16(ws, v, s)
  BAR();
  const f32x4 z4 = {0.f, 0.f, 0.f, 0.f};
  for (int si = grp; si < 65; si += 3) {
    const int s = si < 63 ? si : 63;
    const bool act = si < 63 || (c >> 3) == si - 63;
    const u32 lo = act ? (u32)l * 16u : OOB;
    f32x4 a0 = z4, a1 = z4, h0 = z4, h1 = z4;
    waitflag(ws, (u32)(l & 15) * 4u, C_HC + (u32)s * 128u, 0x3FFFull, ~0u, tag, dead, 2);
    {
      const u32 so = OFF_HF + (u32)(s * 2 + xt) * 57344u + (u32)w * 14336u;
      u32x4 F[14];
#pragma unroll
      for (int k = 0; k < 14; k++) F[k] = LDO(lo, so + k * 1024u);
      SCHED();
#pragma unroll
      for (int k = 0; k < 14; k++) { h0 = mfma32(A2[0][k], as8(F[k]), h0); h1 = mfma32(A2[1][k], as8(F[k]), h1); }
    }
    const float g = __builtin_bit_cast(float, ld4(ws, (u32)c * 4u, OFF_G + (u32)(s * 2 + xt) * 64u));
    waitflag(ws, (u32)(l & 31) * 4u, C_OC + (u32)s * 128u, 0xFFFFFFFFull, ~0u, tag, dead, s < 61 ? 1 : 0);
    {
      const u32 so = OFF_OF + (u32)(s * 2 + xt) * 131072u + (u32)w * 32768u;
      u32x4 F[16];
#pragma unroll
      for (int k = 0; k < 16; k++) F[k] = LDO(lo, so + k * 1024u);
      SCHED();
#pragma unroll
      for (int k = 0; k < 32; k++) {
        a0 = mfma32(A[0][k], as8(F[k & 15]), a0); a1 = mfma32(A[1][k], as8(F[k & 15]), a1);
        if (k < 16) {
          asm volatile("" : "+a"(a0), "+a"(a1));
          F[k] = LDO(lo, so + 16384u + k * 1024u);
          SCHED();
        }
      }
    }
    lptr rb = smem + w * 4096 + l * 16;
    *(LAS f32x4*)(rb) = a0; *(LAS f32x4*)(rb + 1024) = a1; *(LAS f32x4*)(rb + 2048) = h0; *(LAS f32x4*)(rb + 3072) = h1;
    BAR();
    if (w == 0) {
#pragma unroll
      for (int k = 1; k < 4; k++) {
        a0 += *(LAS f32x4*)(rb + k * 4096); a1 += *(LAS f32x4*)(rb + k * 4096 + 1024);
        h0 += *(LAS f32x4*)(rb + k * 4096 + 2048); h1 += *(LAS f32x4*)(rb + k * 4096 + 3072);
      }
      const float g1 = rbf(1.f - g);
      float r[8];
#pragma unroll
      for (int i = 0; i < 8; i++) {
        const float av = rbf(i < 4 ? a0[i & 3] : a1[i & 3]);
        const float bv = rbf(rbf(i < 4 ? h0[i & 3] : h1[i & 3]) + bias[i]);
        r[i] = rbf(g * av) + rbf(g1 * bv);
      }
      u32x4 o = {pk(r[0], r[1]), pk(r[2], r[3]), pk(r[4], r[5]), pk(r[6], r[7])};
      if (act) *(u32x4*)(out + (size_t)(32 * s + 16 * xt + c) * 512 + sl * 32 + 8 * gb) = o;
    }
    BAR();
  }
}

DI void probe_role(lptr smem, rsrc_t ws, u32 tag, int w, int l) {
  const u32 base = ((w & 1) ? OFF_MEM : OFF_PART) + (u32)(w >> 1) * 131072u;
  u32 best[2] = {0xFFFFFFFFu, 0xFFFFFFFFu};
#pragma unroll 1
  for (int i = 0; i < 8; i++) {
#pragma unroll
    for (int sd = 0; sd < 2; sd++) {
      u64 t0 = __builtin_amdgcn_s_memtime();
      asm volatile("s_waitcnt lgkmcnt(0)" ::: "memory");
      st4(ws, 0xFFFFFFFFu, l == 0 ? 0u : OOB, base + (u32)i * 16384u + (u32)sd * 8192u);
      WAITVM(0);
      u64 t1 = __builtin_amdgcn_s_memtime();
      best[sd] = min(best[sd], (u32)(t1 - t0));
    }
  }
  if (l == 0) { *(LAS u32*)(smem + w * 8) = best[0]; *(LAS u32*)(smem + w * 8 + 4) = best[1]; }
  BAR();
  if (w == 0 && l == 0) {
    LAS u32* t = (LAS u32*)smem;
    const u32 a = min(t[0], t[4]), b = min(t[1], t[5]), c = min(t[2], t[6]), d = min(t[3], t[7]);
    st4(ws, tag | (b < a ? 1u : 0u) | (d < c ? 2u : 0u), 0, C_SEL);
  }
  BAR();
}

__global__ __launch_bounds__(256, 1) void fused_kernel(const char* x, const bf16* mem0, const char* Wq, const char* Wk, const char* Wv,
                                                      const char* Wo, const char* w1, const bf16* b1, const char* w2, const bf16* b2,
                                                      const bf16* gw, bf16* out, char* wsb) {
  extern __shared__ __attribute__((aligned(16))) char smem_[];
  lptr smem = (lptr)smem_;
  const int b = blockIdx.x, xq = b & 7, r = b >> 3;
  const int w = __builtin_amdgcn_readfirstlane(threadIdx.x >> 6), l = threadIdx.x & 63;
  const rsrc_t ws = __builtin_amdgcn_make_buffer_rsrc(wsb, 0, (int)WS_BYTES, 0x00020000);
  if (xq < 4 && r < 16) {
    chain_role(smem, ws, 8 * xq + (r >> 1), r & 1, w, l, Wq, Wk, Wv, Wo, mem0, wsb);
    return;
  }
  const int head = xq < 4 ? 8 * xq + ((r - 16) >> 1) : 8 * (xq - 4) + (r >> 2);
  const int j = xq < 4 ? 4 + (r & 1) : (r & 3);
  if (b == 31 * 8) probe_role(smem, ws, gettag(wsb), w, l);
  const u32 tag = qkv_role(smem, ws, wsb, head, j, w, l, x, Wq, Wk, Wv, Wo, w1);
  const int wi = (r - 16) * 8 + xq;
  if (r >= 16 && wi < 126) ffn1_role(smem, ws, tag, wi / 9, wi % 9, w, l, x, w1, b1, gw);
  if (xq >= 4) {
    if (r >= 24) {
      attn_role(ws, tag, ((xq - 4) * 8 + (r - 24)) * 4 + w, l);
    } else {
      const int g2 = (xq - 4) >> 1;
      out_role(smem, ws, tag, r < 16 ? g2 : 2, xq & 1, r < 16 ? r : r - 16 + 8 * g2, w, l, Wo, w2, b2, out);
    }
  }
}

extern "C" void kernel_launch(void* const* d_in, const int* in_sizes, int n_in, void* d_out, int out_size, void* d_ws,
                              size_t ws_size, hipStream_t stream) {
  char* base = (char*)(((size_t)d_ws + 2 * MB - 1) & ~(size_t)(2 * MB - 1));
  (void)hipFuncSetAttribute((const void*)fused_kernel, hipFuncAttributeMaxDynamicSharedMemorySize, LDS_BYTES);
  fused_kernel<<<256, 256, LDS_BYTES, stream>>>((const char*)d_in[0], (const bf16*)d_in[1], (const char*)d_in[2], (const char*)d_in[3],
                                                (const char*)d_in[4], (const char*)d_in[5], (const char*)d_in[8], (const bf16*)d_in[9],
                                                (const char*)d_in[10], (const bf16*)d_in[11], (const bf16*)d_in[12], (bf16*)d_out, base);
}
```

```cpp
#include <hip/hip_runtime.h>
#include <hip/hip_bf16.h>

typedef unsigned u32;
typedef unsigned long long u64;
typedef __bf16 bf16;
typedef bf16 bf16x2 __attribute__((ext_vector_type(2)));
typedef bf16 bf16x8 __attribute__((ext_vector_type(8)));
typedef bf16 bf16x16 __attribute__((ext_vector_type(16)));
typedef short s16x4 __attribute__((ext_vector_type(4)));
typedef short s16x8 __attribute__((ext_vector_type(8)));
typedef short s16x16 __attribute__((ext_vector_type(16)));
typedef float f32x2 __attribute__((ext_vector_type(2)));
typedef float f32x4 __attribute__((ext_vector_type(4)));
typedef u32 u32x2 __attribute__((ext_vector_type(2)));
typedef u32 u32x4 __attribute__((ext_vector_type(4)));
typedef __amdgpu_buffer_rsrc_t rsrc_t;
#define LAS __attribute__((address_space(3)))
typedef LAS char* lptr;
#define DI __device__ __forceinline__

constexpr u32 MB = 1u << 20;
constexpr u32 OFF_PART = 0;
constexpr u32 OFF_MEM = 94 * MB;
constexpr u32 OFF_CTRL = 96 * MB;
constexpr u32 C_SEL = OFF_CTRL;
constexpr u32 C_EP = OFF_CTRL + 256;
constexpr u32 C_RDY = OFF_CTRL + 512;
constexpr u32 C_KV = OFF_CTRL + 1024;
constexpr u32 C_KM = C_KV + 8192;
constexpr u32 C_OC = C_KM + 16384;
constexpr u32 C_HC = C_OC + 8192;
constexpr u32 C_END = C_HC + 8192;
constexpr u32 OFF_QX = 4 * MB, OFF_KX = 20 * MB, OFF_VX = 36 * MB;
constexpr u32 OFF_KM = 52 * MB, OFF_VM = 60 * MB, OFF_OF = 68 * MB, OFF_HF = 84 * MB, OFF_G = 92 * MB;
constexpr u32 WS_BYTES = 97 * MB;
DI u32 slot_off(int s) { const u32 r = (u32)(63 - s); return (r >> 1) * 65536u + (r & 1u) * 2048u; }
DI u32 tile_off(int t) { return (u32)(t >> 2) * 16384u + (u32)((t >> 1) & 1) * 4096u + (u32)(t & 1) * 1024u; }
constexpr int LDS_BYTES = 160 * 1024;
constexpr int SPIN_MAX = 1 << 15;
constexpr int AUX = 16;

DI u32x4 ld16(rsrc_t r, u32 v, u32 s) { return __builtin_amdgcn_raw_buffer_load_b128(r, v, s, AUX); }
DI u32x2 ld8(rsrc_t r, u32 v, u32 s) { return __builtin_amdgcn_raw_buffer_load_b64(r, v, s, AUX); }
DI u32 ld4(rsrc_t r, u32 v, u32 s) { return __builtin_amdgcn_raw_buffer_load_b32(r, v, s, AUX); }
DI void st16(rsrc_t r, u32x4 x, u32 v, u32 s) {
  __builtin_amdgcn_raw_buffer_store_b128(x, r, v, s, AUX);
  asm volatile("s_nop 1" ::"v"(x) : "memory");
}
DI void st8(rsrc_t r, u32x2 x, u32 v, u32 s) { __builtin_amdgcn_raw_buffer_store_b64(x, r, v, s, AUX); }
DI void st4(rsrc_t r, u32 x, u32 v, u32 s) { __builtin_amdgcn_raw_buffer_store_b32(x, r, v, s, AUX); }
#define MEMBAR() asm volatile("" ::: "memory")
#define WAITVM(n) asm volatile("s_waitcnt vmcnt(" #n ")" ::: "memory")
#define WAITVM0() do { MEMBAR(); __builtin_amdgcn_s_waitcnt(0x0F70); MEMBAR(); } while (0)
#define BAR() asm volatile("s_waitcnt lgkmcnt(0)\n\ts_barrier" ::: "memory")
#define SCHED() __builtin_amdgcn_sched_barrier(0)
#define PINA(x) asm volatile("" : "+a"(x))
#define PINV(x) asm volatile("" : "+v"(x))
constexpr u32 OOB = 0x80000000u;

DI void dma16(const char* sbase, u32 voff, u32 ldsdst) {
  u32 keep;
  asm volatile("s_mov_b32 %0, m0\n\ts_mov_b32 m0, %3\n\ts_nop 0\n\tglobal_load_lds_dwordx4 %1, %2\n\ts_mov_b32 m0, %0"
               : "=&s"(keep) : "v"(voff), "s"(sbase), "s"(ldsdst) : "memory");
}
DI void dma4(const char* sbase, u32 voff, u32 ldsdst) {
  u32 keep;
  asm volatile("s_mov_b32 %0, m0\n\ts_mov_b32 m0, %3\n\ts_nop 0\n\tglobal_load_lds_dword %1, %2\n\ts_mov_b32 m0, %0"
               : "=&s"(keep) : "v"(voff), "s"(sbase), "s"(ldsdst) : "memory");
}
#define PF 0
DI s16x4 trr(lptr p) { return __builtin_amdgcn_ds_read_tr16_b64_v4i16((LAS s16x4*)p); }
DI bf16x8 cat2(s16x4 a, s16x4 b) { return __builtin_bit_cast(bf16x8, __builtin_shufflevector(a, b, 0, 1, 2, 3, 4, 5, 6, 7)); }
DI bf16x16 cat4(s16x4 a, s16x4 b, s16x4 c, s16x4 d) {
  s16x8 x = __builtin_shufflevector(a, b, 0, 1, 2, 3, 4, 5, 6, 7), y = __builtin_shufflevector(c, d, 0, 1, 2, 3, 4, 5, 6, 7);
  return __builtin_bit_cast(bf16x16, __builtin_shufflevector(x, y, 0, 1, 2, 3, 4, 5, 6, 7, 8, 9, 10, 11, 12, 13, 14, 15));
}
DI u32 pk(float lo, float hi) { f32x2 v = {lo, hi}; return __builtin_bit_cast(u32, __builtin_convertvector(v, bf16x2)); }
DI u32 pk2(f32x2 v) { return __builtin_bit_cast(u32, __builtin_convertvector(v, bf16x2)); }
DI float rbf(float x) { return (float)(bf16)x; }
DI bf16x8 as8(u32x4 x) { return __builtin_bit_cast(bf16x8, x); }
DI f32x4 mfma32(bf16x8 a, bf16x8 b, f32x4 c) { return __builtin_amdgcn_mfma_f32_16x16x32_bf16(a, b, c, 0, 0, 0); }
DI f32x4 mfma16(u32x2 a, u32x2 b, f32x4 c) {
  return __builtin_amdgcn_mfma_f32_16x16x16bf16_1k(__builtin_bit_cast(s16x4, a), __builtin_bit_cast(s16x4, b), c, 0, 0, 0);
}
DI f32x4 smf(u32x4 a, bf16x16 b, f32x4 c, int idx) { return __builtin_amdgcn_smfmac_f32_16x16x64_bf16(as8(a), b, c, idx, 0, 0); }
DI float comb(float x, float y) {
  auto r = __builtin_amdgcn_permlane32_swap(__builtin_bit_cast(u32, x), __builtin_bit_cast(u32, y), false, false);
  return __builtin_bit_cast(float, (u32)r[0]) + __builtin_bit_cast(float, (u32)r[1]);
}
DI float maxall(float m) {
  auto r = __builtin_amdgcn_permlane32_swap(__builtin_bit_cast(u32, m), __builtin_bit_cast(u32, m), false, false);
  m = fmaxf(__builtin_bit_cast(float, (u32)r[0]), __builtin_bit_cast(float, (u32)r[1]));
  auto q = __builtin_amdgcn_permlane16_swap(__builtin_bit_cast(u32, m), __builtin_bit_cast(u32, m), false, false);
  return fmaxf(__builtin_bit_cast(float, (u32)q[0]), __builtin_bit_cast(float, (u32)q[1]));
}
DI f32x4 zero4() {
  u64 a, b;
  asm volatile("v_mov_b64 %0, 0" : "=v"(a));
  asm volatile("v_mov_b64 %0, 0" : "=v"(b));
  typedef u64 u64x2 __attribute__((ext_vector_type(2)));
  u64x2 r = {a, b};
  return __builtin_bit_cast(f32x4, r);
}
DI u32 umax8(u32x4 a, u32x4 b) { return max(max(max(a[0], a[1]), max(a[2], a[3])), max(max(b[0], b[1]), max(b[2], b[3]))); }
DI bool anyl(bool p) { return __builtin_amdgcn_ballot_w64(p) != 0; }
DI void waitflag(rsrc_t r, u32 v, u32 s, u64 lanes, u32 msk, u32 val, bool& dead, int sleep) {
  int n = 0;
#pragma unroll 1
  while (!dead) {
    MEMBAR();
    const u32 x = ld4(r, v, s);
    if ((__builtin_amdgcn_ballot_w64((x & msk) != val) & lanes) == 0) break;
    asm volatile("" : "+s"(n));
    if (++n > SPIN_MAX) dead = true;
    for (int i = 0; i < sleep; i++) __builtin_amdgcn_s_sleep(4);
  }
  MEMBAR();
}
DI u32 gettag(const char* wsb) {
  u32 t = *(const __attribute__((address_space(4))) u32*)(wsb + C_EP);
  t = (t + 4u) & ~3u;
  asm volatile("" : "+s"(t));
  return t;
}
constexpr float NEG = -3.0e38f;
constexpr float SCL = 0.08838834764831845f * 1.4426950408889634f;

DI void chain_role(lptr smem, rsrc_t ws, int h, int a, int w, int l, const char* Wq, const char* Wk, const char* Wv,
                   const char* Wo, const bf16* mem0, const char* wsb) {
  const int gb = l >> 4, q = (l >> 2) & 3, p = l & 3, c = l & 15;
  const u32 lds0 = (u32)(size_t)smem;
  bf16x16 Bq[2][8], Bk[2][8], Bv[8], Bo[8];
  bool dead = false;
  const int ci = 2 * h + a;
  const int rho = 2 * ci + (w & 1);
  const int rt = rho >> 4, gs = (rho >> 2) & 3, ru = rho & 3;
  const u32 rpiece = tile_off(rt) + (u32)ru * 256u + (u32)gs * 64u;
  const u32 rpub4 = c < 4 ? rpiece + (u32)(c & 3) * 16u + (u32)gb * 4u : OOB;
  const u32 rblk0 = (u32)(rho >> 1) * 16384u + (u32)(rho & 1) * 4096u;
  const u32 lane0 = l == 0 ? 0u : OOB;
  const u32x4 ones = {~0u, ~0u, ~0u, ~0u};
  u32 selw, rdy, flg, tag;
  {
    const u32 cp = (u32)(w >> 1) * 8192u;
    const u32 bx = (u32)((l & 15) ^ ((l >> 5) << 1));
    const u32 rowoff = (u32)(64 * w + (l >> 4)) * 8192u + (u32)h * 256u;
    auto issueQK = [&](const char* W, int half, int buf) {
#pragma unroll
      for (int it = 0; it < 16; it++) {
        const u32 cit = (u32)((((it >> 3) & 1) << 3) | (((it >> 2) & 1) << 2));
        dma16(W, rowoff + (u32)(half * 256 + it * 4) * 8192u + ((bx ^ cit) << 4), lds0 + buf * 65536 + (w * 16 + it) * 1024);
      }
    };
    auto issueV = [&](int buf) {
      const u32 bv = (u32)((l & 7) ^ ((l >> 5) << 1));
      const u32 ro = (u32)(128 * w + ((l >> 3) & 1) * 32 + (l >> 4)) * 8192u + (u32)h * 256u + (u32)a * 128u;
#pragma unroll
      for (int it = 0; it < 16; it++)
        dma16(Wv, ro + (u32)((it >> 3) * 64 + (it & 7) * 4) * 8192u + ((bv ^ (u32)(((it >> 2) & 1) << 2)) << 4),
              lds0 + buf * 65536 + (w * 16 + it) * 1024);
    };
    auto issueO = [&](int buf) {
      const u32 ro = (u32)(h * 128 + a * 64 + w * 16) * 1024u;
#pragma unroll
      for (int it = 0; it < 16; it++) {
        const u32 f = (u32)((((it >> 3) & 1) << 2) | (((it >> 2) & 1) << 1) | (it & 1));
        dma16(Wo, ro + (u32)it * 1024u + (((u32)l ^ (f << 1)) << 4), lds0 + buf * 65536 + (w * 16 + it) * 1024);
      }
    };
    const int rowl = 32 * (q & 1) + 16 * (q >> 1) + 2 * gb;
    const int fq = ((q & 1) << 2) | ((q >> 1) << 1) | (gb & 1);
    auto readQK = [&](bf16x16(&B)[2][8], int half, int buf) {
#pragma unroll
      for (int y = 0; y < 2; y++) {
        lptr b = smem + buf * 65536 + rowl * 256 + (((4 * w + 2 * y + (p >> 1)) ^ (fq << 1)) << 4) + (p & 1) * 8;
#pragma unroll
        for (int t = 0; t < 4; t++)
        {
          lptr bt = b + (t ^ w) * 16384;
          B[y][half * 4 + t] = cat4(trr(bt), trr(bt + 256), trr(bt + 2048), trr(bt + 2304));
          PINA(B[y][half * 4 + t]);
        }
      }
    };
    dma16((const char*)mem0, (u32)(w * 2048 + l * 16), lds0 + 135168u + (u32)w * 2048u);
    dma16((const char*)mem0, (u32)(w * 2048 + 1024 + l * 16), lds0 + 136192u + (u32)w * 2048u);
    issueQK(Wq, 0, 0);
    issueQK(Wq, 1, 1);
    SCHED();
#pragma unroll
    for (int k = 0; k < 8; k++) st16(ws, ones, rblk0 + (u32)l * 16u + (k & 3) * 1024u, OFF_PART + (k >> 2) * MB + cp);
#pragma unroll
    for (int k = 0; k < 4; k++) {
      const int s = 1 + k * 16 + (l >> 2);
      st16(ws, ones, s < 64 ? slot_off(s) + rpiece + (u32)(l & 3) * 16u : OOB, OFF_MEM + cp);
    }
    SCHED();
    tag = gettag(wsb);
    WAITVM(28); BAR(); readQK(Bq, 0, 0);
    {
      const int d0 = 64 * rt + 16 * ((c >> 1) & 1) + 4 * ru + gb, r0 = 2 * gs + (c & 1);
      LAS unsigned short* m0 = (LAS unsigned short*)(smem + 135168) + r0 * 512 + d0;
      const u32 mv = (u32)m0[0] | ((u32)m0[32] << 16);
      BAR();
      st4(ws, mv, rpub4, OFF_MEM + cp + slot_off(0));
      SCHED();
    }
    issueQK(Wk, 0, 0);
    WAITVM(29); BAR(); readQK(Bq, 1, 1); BAR(); issueQK(Wk, 1, 1);
    WAITVM(16); BAR();
    st4(ws, tag, w == 0 ? lane0 : OOB, C_RDY + (u32)ci * 4u);
    SCHED();
    readQK(Bk, 0, 0); BAR(); issueV(0);
    WAITVM(16); BAR(); readQK(Bk, 1, 1); BAR(); issueO(1);
    WAITVM(16); BAR();
    {
      const int f2 = ((q >> 1) << 1) | (gb & 1);
      lptr b = smem + (16 * (q >> 1) + 2 * gb) * 256 + (q & 1) * 128 + (((2 * w + (p >> 1)) ^ (f2 << 1)) << 4) + (p & 1) * 8;
#pragma unroll
      for (int t = 0; t < 8; t++)
      {
        lptr bt = b + (t ^ w) * 8192;
        Bv[t] = cat4(trr(bt), trr(bt + 256), trr(bt + 2048), trr(bt + 2304));
        PINV(Bv[t]);
      }
    }
    SCHED();
    selw = ld4(ws, 0, C_SEL); rdy = ld4(ws, (u32)l * 4u, C_RDY); flg = ld4(ws, 0, C_KV + (u32)(h * 64) * 4u);
    SCHED();
    WAITVM0(); BAR();
    {
      const int fo = ((q >> 1) << 2) | ((gb & 1) << 1) | (q & 1);
      const int er = 16 * (gb >> 1) + 8 * (q >> 1) + 4 * (gb & 1) + (q & 1);
#pragma unroll
      for (int T = 0; T < 8; T++) {
        lptr b = smem + 65536 + er * 1024 + (((16 * w + 2 * T + (p >> 1)) ^ (fo << 1)) << 4) + (p & 1) * 8;
        Bo[T] = cat4(trr(b), trr(b + 2048), trr(b + 32768), trr(b + 32768 + 2048));
        PINV(Bo[T]);
      }
    }
    BAR();
  }
  if (anyl(rdy != tag)) waitflag(ws, (u32)l * 4u, C_RDY, ~0ull, ~0u, tag, dead, 1);
  if (anyl((selw & ~3u) != tag)) { waitflag(ws, 0, C_SEL, 1ull, ~3u, tag, dead, 1); selw = ld4(ws, 0, C_SEL); }
  selw = __builtin_amdgcn_readfirstlane(selw);
  flg = __builtin_amdgcn_readfirstlane(flg);
  const u32 sel0 = (selw & 1u) * 8192u, sel1 = ((selw >> 1) & 1u) * 8192u;
  const u32 rgat = rblk0 + sel0 + (u32)(4 * gb + (c & 3)) * 64u + (u32)(c >> 2) * 16u;
  constexpr u32 GK = 1024u;
  const u32 pst = (u32)(w * 16 + (l >> 3)) * 16384u + (u32)((l >> 2) & 1) * 4096u + sel0 + (u32)ci * 64u + (u32)(l & 3) * 16u;
  const u32 kvlane = (u32)l * 16u;
  lptr Lq = smem, Lk = smem + 6144, Lx = smem + 12288, Lv = smem + 20480, La = smem + 24576, Lo = smem + 49152;
#pragma unroll
  for (int i = 0; i < 3; i++) *(LAS u32x4*)(smem + i * 4096 + w * 1024 + l * 16) = (u32x4){0u, 0u, 0u, 0u};
  auto fetch_kv = [&](int sn) {
    if ((flg & ~3u) != tag) waitflag(ws, 0, C_KV + (u32)(h * 64 + sn) * 4u, 1ull, ~3u, tag, dead, 1);
    const u32 so = (u32)(sn * 32 + h) * 8192u;
    u32x4 t[6];
    const u32 fl = ld4(ws, 0, C_KV + (u32)(h * 64 + sn + 1) * 4u);
    SCHED();
#pragma unroll
    for (int i = 0; i < 4; i++) t[i] = ld16(ws, kvlane, OFF_KX + so + (u32)(w - 2) * 4096u + i * 1024u);
#pragma unroll
    for (int i = 0; i < 2; i++) t[4 + i] = ld16(ws, kvlane, OFF_VX + so + (u32)(4 * a + 2 * (w - 2) + i) * 1024u);
    SCHED();
    flg = __builtin_amdgcn_readfirstlane(fl);
#pragma unroll
    for (int i = 0; i < 4; i++) *(LAS u32x4*)(Lx + (w - 2) * 4096 + i * 1024 + l * 16) = t[i];
#pragma unroll
    for (int i = 0; i < 2; i++) *(LAS u32x4*)(Lv + (w - 2) * 2048 + i * 1024 + l * 16) = t[4 + i];
  };
  if (w >= 2) fetch_kv(0);
  WAITVM0();
  BAR();
  const u32 kmoff = (l >> 5) == a ? kvlane + (u32)w * 1024u : OOB;
  const u32 flgoff = w == 2 ? lane0 : OOB;
  const int idx = (l & 2) ? 0xEEEE : 0x4444;
  const int wq = (4 * w + (c >> 2)) * 384 + (c & 3) * 32 + gb * 8;
  const int rq = (4 * w + gb) * 384 + q * 32 + p * 8;
  const bool up = (c & 2) != 0;
  u32 pmask = up ? 0xFFFF0000u : 0x0000FFFFu;
  u32 tagv = tag;
  PINV(pmask); PINV(tagv);
  lptr Ls = smem + 53248;
  lptr Lar[4] = {La, La + (w ^ 1) * 1024 + l * 16, La + (w ^ 2) * 1024 + l * 16, La + (w ^ 3) * 1024 + l * 16};
  lptr Lkw = Lk + wq, Lsw = Ls + 4096 + w * 256 + l * 4;
  PINV(Lar[1]); PINV(Lar[2]); PINV(Lar[3]); PINV(Lkw); PINV(Lsw);
  const int orow = 2 * (c >> 2) + (c & 1);
  u32x4 selB;
  {
    const int j = c & 7;
    const u32 one = (j & 1) ? 0x3F800000u : 0x00003F80u;
    selB[0] = (j >> 1) == 0 ? one : 0u; selB[1] = (j >> 1) == 1 ? one : 0u;
    selB[2] = (j >> 1) == 2 ? one : 0u; selB[3] = (j >> 1) == 3 ? one : 0u;
  }
  u32x4 one8 = {0x3F803F80u, 0x3F803F80u, 0x3F803F80u, 0x3F803F80u};
  PINV(one8); PINV(selB);
#define SM2(d0, d1, B, t)                                                                                            \
  asm volatile("v_smfmac_f32_16x16x64_bf16 %0, %2, %3, %5\n\tv_smfmac_f32_16x16x64_bf16 %1, %2, %4, %5"              \
               : "+v"(d0), "+v"(d1) : "v"(A[t]), "a"(B[0][t]), "a"(B[1][t]), "v"(idx));                              \
  SCHED()
#define VSTEP(t)                                                                                                     \
  asm volatile("v_smfmac_f32_16x16x64_bf16 %0, %1, %2, %3" : "+v"(v0) : "v"(A[t]), "v"(Bv[t]), "v"(idx));            \
  SCHED()
#define MF0(D, F, G) asm volatile("v_mfma_f32_16x16x32_bf16 %0, %1, %2, 0" : "=&v"(D) : "v"(F), "v"(G)); SCHED()
#define MF1(D, F, G) asm volatile("v_mfma_f32_16x16x32_bf16 %0, %1, %2, %0" : "+v"(D) : "v"(F), "v"(G)); SCHED()
#define FOLD(D) pk2(__builtin_shufflevector(D, D, 0, 1) + __builtin_shufflevector(D, D, 2, 3))
#define UNDEF(x) asm volatile("" : "=v"(x))
#define NAP(n) do { if (n) __builtin_amdgcn_s_sleep(n); } while (0)
#define KVD 0
#define D1 10
#define D2 8
#define D3 10

  for (int s = 0; s < 64; s++) {
    const u32 mslot = OFF_MEM + sel1 + slot_off(s) + tile_off(w);
    u32x4 A[8];
    u32x4 g0 = ld16(ws, kvlane, mslot); SCHED();
    u32x4 g1 = ld16(ws, kvlane, mslot + 16384u); SCHED();
    f32x4 q0 = zero4(), q1 = zero4(), k0 = zero4(), k1 = zero4(), v0 = zero4();
    u32 parS = OFF_PART + (u32)(s & 1) * MB, parT = parS + 8 * 16384u, rstS = parS ^ MB, rstT = parT ^ MB;
    u32 kmS = OFF_KM + (u32)(s * 32 + h) * 4096u, vmS = kmS + (OFF_VM - OFF_KM), pubS = OFF_MEM + sel1 + slot_off(s + 1);
    u32 flS = C_KM - 8u + (u32)(h * 128 + a) * 4u + (u32)s * 8u, flV = s > 0 ? flgoff : OOB;
    asm volatile("" : "+s"(parS), "+s"(parT), "+s"(rstS), "+s"(rstT), "+s"(kmS), "+s"(vmS), "+s"(pubS), "+s"(flS), "+v"(flV));
    SCHED();
    *(LAS u32x4*)(La + w * 1024 + l * 16) = g0;
    *(LAS u32x4*)(La + w * 1024 + 4096 + l * 16) = g1;
    SCHED();
    if (__builtin_expect(anyl(umax8(g0, g1) == 0xFFFFFFFFu), 0)) {
      int n = 0;
      asm volatile("" : "+v"(g0), "+v"(g1));
#pragma unroll 1
      while (anyl(umax8(g0, g1) == 0xFFFFFFFFu) && !dead) {
        MEMBAR();
        g0 = ld16(ws, kvlane, mslot);
        g1 = ld16(ws, kvlane, mslot + 16384u);
        WAITVM0();
        if (++n > SPIN_MAX) dead = true;
      }
      *(LAS u32x4*)(La + w * 1024 + l * 16) = g0;
      *(LAS u32x4*)(La + w * 1024 + 4096 + l * 16) = g1;
    }
    BAR();
    A[0] = g0; A[4] = g1;
#define RDA(t) A[t] = *(LAS u32x4*)(Lar[t & 3] + (t & 4) * 1024); SCHED()
    RDA(1); RDA(2);
    SM2(q0, q1, Bq, 0);
    RDA(3);
    SM2(q0, q1, Bq, 4);
    RDA(5);
    SM2(k0, k1, Bk, 0);
    RDA(6);
    SM2(k0, k1, Bk, 4);
    RDA(7);
    SM2(q0, q1, Bq, 1);
    __builtin_amdgcn_raw_buffer_store_b128(ones, ws, pst, rstS, AUX); SCHED();
    SM2(q0, q1, Bq, 2);
    __builtin_amdgcn_raw_buffer_store_b128(ones, ws, pst, rstT, AUX); SCHED();
    SM2(q0, q1, Bq, 3);
    st4(ws, tagv, flV, flS); SCHED();
    SM2(q0, q1, Bq, 5);
    const u32x4 X0 = *(LAS u32x4*)(Lx + w * 1024 + l * 16), X1 = *(LAS u32x4*)(Lx + 4096 + w * 1024 + l * 16);
    SCHED();
    SM2(q0, q1, Bq, 6); SM2(q0, q1, Bq, 7);
    const u32x2 vxa = *(LAS u32x2*)(Lv + w * 1024 + l * 16);
    asm volatile("" ::: "memory");
    const u32x2 vxb = *(LAS u32x2*)(Lv + w * 1024 + l * 16 + 8);
    SCHED();
    SM2(k0, k1, Bk, 1);
    asm volatile("" : "+v"(q0), "+v"(q1));
    {
      const u32 a0 = FOLD(q0), a1 = FOLD(q1);
      *(LAS u32*)(Lq + wq) = a0; *(LAS u32*)(Lq + wq + 4) = a0; SCHED();
      SM2(k0, k1, Bk, 2);
      *(LAS u32*)(Lq + wq + 128) = a1; *(LAS u32*)(Lq + wq + 132) = a1; SCHED();
    }
    SM2(k0, k1, Bk, 3);
    const bf16x8 Fq = cat2(trr(Lq + rq), trr(Lq + rq + 128));
    SCHED();
    f32x4 D[8];
    SM2(k0, k1, Bk, 5);
    u32x4 VA = {0u, 0u, vxa[0], vxa[1]}, VB = {0u, 0u, vxb[0], vxb[1]};
    PINV(VA); PINV(VB);
    SCHED();
    SM2(k0, k1, Bk, 6);
    D[0] = zero4(); D[1] = zero4();
    SCHED();
    SM2(k0, k1, Bk, 7);
    f32x4 sM, s0, s1;
    MF0(s0, X0, Fq);
    MF0(s1, X1, Fq);
    VSTEP(0);
    asm volatile("" : "+v"(k0), "+v"(k1));
    { const u32 a0 = FOLD(k0), a1 = FOLD(k1); *(LAS u32*)(Lkw) = a0; *(LAS u32*)(Lkw + 128) = a1; SCHED(); }
    const bf16x8 Fk = cat2(trr(Lk + rq), trr(Lk + rq + 128));
    SCHED();
    VSTEP(1);
    asm volatile("" : "+v"(s0), "+v"(s1));
    {
      const f32x4 x = {up ? s1[0] : s0[0], up ? s1[1] : s0[1], up ? s1[2] : s0[2], up ? s1[3] : s0[3]};
      SCHED();
      VSTEP(2);
      *(LAS f32x4*)(Ls + w * 1024 + l * 16) = x;
      SCHED();
    }
    VSTEP(3);
    D[2] = zero4(); SCHED();
    VSTEP(4);
    D[3] = zero4(); SCHED();
    VSTEP(5);
    MF0(sM, Fk, Fq);
    VSTEP(6); VSTEP(7);
    st16(ws, __builtin_bit_cast(u32x4, Fk), kmoff, kmS);
    asm volatile("" : "+v"(sM));
    *(LAS float*)(Lsw) = up ? sM[1] : sM[0];
    SCHED();
    BAR();
    f32x4 xs[4];
    float ms[4];
#pragma unroll
    for (int j = 0; j < 4; j++) ms[j] = *(LAS float*)(Ls + 4096 + j * 256 + l * 4);
    SCHED();
#pragma unroll
    for (int j = 0; j < 4; j++) { xs[j] = *(LAS f32x4*)(Ls + j * 1024 + l * 16); SCHED(); }
    asm volatile("" : "+v"(v0));
    const u32 vm = FOLD(v0);
    st4(ws, vm, (u32)l * 4u + (u32)(4 * a + w) * 256u, vmS);
    if (s == 63) break;
    VA[0] = vm; VB[0] = vm;
    PINV(VA); PINV(VB);
    SCHED();
    float sm = ms[0] + ms[1], sn = ms[2] + ms[3];
    PINV(sm); PINV(sn);
    sm += sn;
    PINV(sm);
    SCHED();
    const f32x4 sx = (xs[0] + xs[1]) + (xs[2] + xs[3]);
    float m, mt;
    asm volatile("v_max3_f32 %0, %2, %3, %6\n\tv_max3_f32 %0, %0, %4, %5\n\ts_nop 1\n\t"
                 "v_max_f32_dpp %1, %0, %0 quad_perm:[2,3,0,1] row_mask:0xf bank_mask:0xf\n\t"
                 "v_max_f32_dpp %0, %0, %0 quad_perm:[2,3,0,1] row_mask:0xf bank_mask:0xf\n\ts_nop 1\n\t"
                 "v_permlane32_swap_b32 %0, %1\n\tv_max_f32 %0, %0, %1\n\tv_mov_b32 %1, %0\n\ts_nop 1\n\t"
                 "v_permlane16_swap_b32 %0, %1\n\tv_max_f32 %0, %0, %1"
                 : "=&v"(m), "=&v"(mt) : "v"(sx[0]), "v"(sx[1]), "v"(sx[2]), "v"(sx[3]), "v"(sm));
    m *= -SCL;
    const f32x2 m2 = {m, m}, scl2 = {SCL, SCL};
#define EXP(t) pk(__builtin_amdgcn_exp2f(t[0]), __builtin_amdgcn_exp2f(t[1]))
    float tM;
    asm volatile("v_fma_f32 %0, %1, %2, %3" : "=v"(tM) : "v"(sm), "s"(SCL), "v"(m));
    const f32x2 t0 = __builtin_elementwise_fma(__builtin_shufflevector(sx, sx, 0, 1), scl2, m2),
                t1 = __builtin_elementwise_fma(__builtin_shufflevector(sx, sx, 2, 3), scl2, m2);
    SCHED();
    const float eM = __builtin_amdgcn_exp2f(tM);
    const u32x4 P = {pk(eM, eM) & pmask, 0u, EXP(t0), EXP(t1)};
    f32x4 sum, oa, ob;
    asm volatile("s_nop 1\n\tv_mfma_f32_16x16x32_bf16 %0, %3, %4, 0\n\tv_mfma_f32_16x16x32_bf16 %2, %6, %4, 0\n\t"
                 "v_mfma_f32_16x16x32_bf16 %1, %5, %4, 0"
                 : "=&v"(sum), "=&v"(oa), "=&v"(ob) : "v"(one8), "v"(P), "v"(VA), "v"(VB));
    {
      float rs;
      f32x4 o;
      asm volatile("v_add_f32_dpp %0, %1, %1 quad_perm:[2,3,0,1] row_mask:0xf bank_mask:0xf\n\ts_nop 0\n\tv_rcp_f32 %0, %0" : "=&v"(rs) : "v"(sum[0]));
      D[4] = zero4(); D[5] = zero4(); D[6] = zero4();
      asm volatile("v_add_f32_dpp %0, %4, %8 quad_perm:[2,3,0,1] row_mask:0xf bank_mask:0xf\n\t"
                   "v_add_f32_dpp %1, %5, %9 quad_perm:[2,3,0,1] row_mask:0xf bank_mask:0xf\n\t"
                   "v_add_f32_dpp %2, %6, %10 quad_perm:[2,3,0,1] row_mask:0xf bank_mask:0xf\n\t"
                   "v_add_f32_dpp %3, %7, %11 quad_perm:[2,3,0,1] row_mask:0xf bank_mask:0xf"
                   : "=&v"(o[0]), "=&v"(o[1]), "=&v"(o[2]), "=&v"(o[3])
                   : "v"(ob[0]), "v"(ob[1]), "v"(ob[2]), "v"(ob[3]), "v"(oa[0]), "v"(oa[1]), "v"(oa[2]), "v"(oa[3]));
      const f32x2 r2 = {rs, rs};
      u32x2 oo = {pk2(__builtin_shufflevector(o, o, 0, 1) * r2), pk2(__builtin_shufflevector(o, o, 2, 3) * r2)};
      *(LAS u32x2*)(Lo + (orow + ((c & 2) << 2)) * 144 + w * 32 + gb * 8) = oo;
    }
    WAITVM(2);
    BAR();
    const u32x4 Ao = *(LAS u32x4*)(Lo + orow * 144 + gb * 32 + ((c >> 1) & 1) * 16);
    SCHED();
    D[7] = zero4();
    SCHED();
#define OST(T) asm volatile("v_smfmac_f32_16x16x64_bf16 %0, %1, %2, %3" : "+v"(D[T]) : "v"(Ao), "v"(Bo[T]), "v"(idx)); SCHED()
#define OAD(T) asm volatile("v_pk_add_f32 %0, %1, %2" : "=v"(f[T]) : "v"(__builtin_shufflevector(D[T], D[T], 0, 1)), "v"(__builtin_shufflevector(D[T], D[T], 2, 3))); SCHED()
#define OCV(P_, i, T) asm volatile("v_cvt_pk_bf16_f32 %0, %1, %2" : "=v"(P_[i]) : "v"(f[T][0]), "v"(f[T][1])); SCHED()
#define NOP(n) asm volatile("s_nop " #n); SCHED()
    {
      u32 pv[4], pw[4];
      f32x2 f[8];
      OST(0); OST(1); OST(2);
      OAD(0); OST(3);
      OAD(1); OCV(pv, 0, 0); OST(4);
      OAD(2); OCV(pv, 1, 1); OST(5);
      OAD(3); OCV(pv, 2, 2); OST(6);
      OAD(4); OCV(pv, 3, 3); OST(7);
      __builtin_amdgcn_raw_buffer_store_b128((u32x4){pv[0], pv[1], pv[2], pv[3]}, ws, pst, parS, AUX); SCHED();
      OAD(5); OCV(pw, 0, 4); NOP(0); OCV(pw, 1, 5); NOP(0);
      OAD(6); NOP(0); OCV(pw, 2, 6); NOP(0);
      OAD(7); NOP(0); OCV(pw, 3, 7);
      asm volatile("" ::"v"(pv[0]), "v"(pv[1]), "v"(pv[2]), "v"(pv[3]));
      st16(ws, (u32x4){pw[0], pw[1], pw[2], pw[3]}, pst, parT);
    }
    if (w < 2) {
      u32x4 g[4], gl[4];
      NAP(D1);
      SCHED();
#pragma unroll
      for (int k = 0; k < 4; k++) { g[k] = ld16(ws, rgat, parS + k * GK); SCHED(); }
      NAP(D2);
      SCHED();
#pragma unroll
      for (int k = 0; k < 4; k++) { gl[k] = ld16(ws, rgat, parS + k * GK); SCHED(); }
      float tot;
#define REDUCE(G)                                                                                                    \
  {                                                                                                                  \
    f32x4 acc;                                                                                                       \
    MF0(acc, G[0], selB); MF1(acc, G[1], selB); MF1(acc, G[2], selB); MF1(acc, G[3], selB);                           \
    asm volatile("s_nop 7" : "+v"(acc));                                                                             \
    const f32x2 t_ = __builtin_shufflevector(acc, acc, 0, 1) + __builtin_shufflevector(acc, acc, 2, 3); tot = t_[0] + t_[1];                                                               \
  }
      REDUCE(g);
      if (__builtin_expect(anyl(tot != tot), 0)) {
        REDUCE(gl);
        int n = 0;
#pragma unroll 1
        while (anyl(tot != tot) && !dead) {
          MEMBAR();
#pragma unroll
          for (int k = 0; k < 4; k++) { g[k] = ld16(ws, rgat, parS + k * GK); SCHED(); }
          REDUCE(g);
          if (++n > SPIN_MAX) dead = true;
        }
      }
      const float hi = __builtin_bit_cast(float, __builtin_amdgcn_update_dpp(0, __builtin_bit_cast(int, tot), 0x104, 0xf, 0xf, true));
      st4(ws, pk(tot, hi), rpub4, pubS);
      NAP(D3);
      asm volatile("" ::"v"(gl[0]), "v"(gl[1]), "v"(gl[2]), "v"(gl[3]));
    } else {
      if (s < 62) {
        NAP(KVD);
        fetch_kv(s + 1);
      } else {
        WAITVM0();
      }
    }
    BAR();
  }
  WAITVM0();
  BAR();
  if (w == 2 && l == 0) st4(ws, tag, 0, C_KM + (u32)((h * 64 + 63) * 2 + a) * 4u);
  if (ci == 0 && w == 1 && l == 0) st4(ws, tag, 0, C_EP);
}

DI void stageW(const char* W, u32 off0, u32 rs, u32 ldsbuf, int w, int l) {
  const u32 bx = (u32)((l & 15) ^ ((l >> 4) << 1));
  const u32 ro = off0 + (u32)(64 * w + (l >> 4)) * rs;
#pragma unroll
  for (int it = 0; it < 16; it++)
    dma16(W, ro + (u32)(it * 4) * rs + ((bx ^ (u32)(((it >> 1) & 1) << 3)) << 4), ldsbuf + (w * 16 + it) * 1024);
}
template <bool AG>
DI void readW(bf16x8 (&B)[2][16], int half, lptr buf, int w, int l) {
  const int gb = l >> 4, q = (l >> 2) & 3, p = l & 3;
  const int f = ((gb & 1) << 2) | q;
#pragma unroll
  for (int y = 0; y < 2; y++) {
    lptr b = buf + (8 * gb + q) * 256 + (((4 * w + 2 * y + (p >> 1)) ^ (f << 1)) << 4) + (p & 1) * 8;
#pragma unroll
    for (int k = 0; k < 8; k++) {
      B[y][half * 8 + k] = cat2(trr(b + k * 8192), trr(b + k * 8192 + 1024));
    }
  }
}
DI void stageX(const char* x, int s, u32 ldsbuf, int w, int l) {
  const u32 ro = (u32)(32 * s + 16 * (w >> 1) + (l & 15)) * 1024u + (u32)((w & 1) * 8) * 64u + (u32)(l >> 4) * 16u;
#pragma unroll
  for (int it = 0; it < 8; it++) dma16(x, ro + it * 64u, ldsbuf + (w * 8 + it) * 1024);
}

template <bool K, bool V, bool Q>
DI void qkv_seg(lptr xb, rsrc_t ws, u32 so, int w, int l, const bf16x8 (&Bq)[2][16], const bf16x8 (&Bk)[2][16], const bf16x8 (&Bv)[2][16]) {
  const f32x4 z4 = {0.f, 0.f, 0.f, 0.f};
  f32x4 aq[2][2], ak[2][2], av[2][2];
#pragma unroll
  for (int i = 0; i < 4; i++) { aq[i >> 1][i & 1] = z4; ak[i >> 1][i & 1] = z4; av[i >> 1][i & 1] = z4; }
  bf16x8 n0 = *(LAS bf16x8*)(xb), n1 = *(LAS bf16x8*)(xb + 16384);
#pragma unroll
  for (int ks = 0; ks < 16; ks++) {
    bf16x8 x0 = n0, x1 = n1;
    if (ks < 15) { n0 = *(LAS bf16x8*)(xb + (ks + 1) * 1024); n1 = *(LAS bf16x8*)(xb + 16384 + (ks + 1) * 1024); }
    asm volatile("" : "+v"(x0), "+v"(x1));
#pragma unroll
    for (int y = 0; y < 2; y++) {
      if (K) { ak[0][y] = mfma32(Bk[y][ks], x0, ak[0][y]); ak[1][y] = mfma32(Bk[y][ks], x1, ak[1][y]); }
      if (V) { av[0][y] = mfma32(x0, Bv[y][ks], av[0][y]); av[1][y] = mfma32(x1, Bv[y][ks], av[1][y]); }
      if (Q) { aq[0][y] = mfma32(Bq[y][ks], x0, aq[0][y]); aq[1][y] = mfma32(Bq[y][ks], x1, aq[1][y]); }
    }
    if (K) asm volatile("" : "+a"(ak[0][0]), "+a"(ak[0][1]), "+a"(ak[1][0]), "+a"(ak[1][1]));
    if (V) asm volatile("" : "+a"(av[0][0]), "+a"(av[0][1]), "+a"(av[1][0]), "+a"(av[1][1]));
    if (Q) asm volatile("" : "+a"(aq[0][0]), "+a"(aq[0][1]), "+a"(aq[1][0]), "+a"(aq[1][1]));
  }
#pragma unroll
  for (int t = 0; t < 2; t++) {
    if (K) {
      u32x4 kk = {pk(ak[t][0][0], ak[t][0][1]), pk(ak[t][0][2], ak[t][0][3]), pk(ak[t][1][0], ak[t][1][1]), pk(ak[t][1][2], ak[t][1][3])};
      st16(ws, kk, (u32)l * 16u + (u32)w * 1024u, OFF_KX + so + t * 4096u);
    }
    if (V) {
      u32x4 vv = {pk(av[0][t][0], av[0][t][1]), pk(av[0][t][2], av[0][t][3]), pk(av[1][t][0], av[1][t][1]), pk(av[1][t][2], av[1][t][3])};
      st16(ws, vv, (u32)l * 16u + (u32)(2 * w + t) * 1024u, OFF_VX + so);
    }
    if (Q) {
      u32x4 qq = {pk(aq[t][0][0], aq[t][0][1]), pk(aq[t][0][2], aq[t][0][3]), pk(aq[t][1][0], aq[t][1][1]), pk(aq[t][1][2], aq[t][1][3])};
      st16(ws, qq, (u32)l * 16u + (u32)w * 1024u, OFF_QX + so + t * 4096u);
    }
  }
}

DI u32 qkv_role(lptr smem, rsrc_t ws, const char* wsb, int h, int j, int w, int l, const char* x, const char* Wq, const char* Wk, const char* Wv,
               const char* Wo, const char* w1) {
  const u32 lds0 = (u32)(size_t)smem;
  bf16x8 Bq[2][16], Bk[2][16], Bv[2][16];
  const u32 ho = (u32)h * 256u, hh = 256u * 8192u;
  const u32 fl = (w == 0 && l == 0) ? 0u : OOB;
  stageW(Wk, ho, 8192u, lds0, w, l);
  stageW(Wk, ho + hh, 8192u, lds0 + 65536, w, l);
  SCHED();
  const u32 tag = gettag(wsb);
  WAITVM(16); BAR(); readW<true>(Bk, 0, smem, w, l); BAR(); stageX(x, j, lds0 + 131072, w, l); stageW(Wv, ho, 8192u, lds0, w, l);
  WAITVM(24); BAR();
  readW<true>(Bk, 1, smem + 65536, w, l); BAR(); stageW(Wv, ho + hh, 8192u, lds0 + 65536, w, l);
  WAITVM(32); BAR();
  qkv_seg<true, false, false>(smem + 131072 + l * 16, ws, (u32)(j * 32 + h) * 8192u, w, l, Bq, Bk, Bv);
  WAITVM(18); BAR(); readW<false>(Bv, 0, smem, w, l);
  WAITVM(2); BAR(); readW<false>(Bv, 1, smem + 65536, w, l);
  qkv_seg<false, true, false>(smem + 131072 + l * 16, ws, (u32)(j * 32 + h) * 8192u, w, l, Bq, Bk, Bv);
  BAR();
  stageW(Wq, ho, 8192u, lds0, w, l);
  WAITVM(16); BAR();
  st4(ws, tag + 1u, fl, C_KV + (u32)(h * 64 + j) * 4u);
  stageW(Wq, ho + hh, 8192u, lds0 + 65536, w, l);
  WAITVM(16); BAR(); readW<true>(Bq, 0, smem, w, l); BAR(); stageX(x, j + 6, lds0, w, l);
  WAITVM(8); BAR(); readW<true>(Bq, 1, smem + 65536, w, l);
  qkv_seg<false, false, true>(smem + 131072 + l * 16, ws, (u32)(j * 32 + h) * 8192u, w, l, Bq, Bk, Bv);
  WAITVM(0); BAR();
  st4(ws, tag + 2u, fl, C_KV + (u32)(h * 64 + j) * 4u);
  int buf = 0;
  for (int s = j + 6; s < 64; s += 6) {
    if (s + 6 < 64) stageX(x, s + 6, lds0 + (buf ^ 1) * 32768, w, l);
    qkv_seg<true, true, true>(smem + buf * 32768 + l * 16, ws, (u32)(s * 32 + h) * 8192u, w, l, Bq, Bk, Bv);
    WAITVM(0); BAR();
    st4(ws, tag + 2u, fl, C_KV + (u32)(h * 64 + s) * 4u);
    buf ^= 1;
  }
  return tag;
}

DI void ffn1_role(lptr smem, rsrc_t ws, u32 tag, int cg, int jj, int w, int l, const char* x, const char* w1, const bf16* b1, const bf16* gw) {
  const u32 lds0 = (u32)(size_t)smem;
  bf16x8 B[2][16], Gf[16];
  stageW(w1, (u32)cg * 256u, 3584u, lds0, w, l);
  stageW(w1, (u32)cg * 256u + 256u * 3584u, 3584u, lds0 + 65536, w, l);
  float bias[8];
#pragma unroll
  for (int i = 0; i < 8; i++) bias[i] = (float)b1[cg * 128 + 32 * w + 16 * (i >> 2) + 4 * (l >> 4) + (i & 3)];
#pragma unroll
  for (int ks = 0; ks < 16; ks++) Gf[ks] = *(const bf16x8*)(gw + 32 * ks + 8 * (l >> 4));
  WAITVM(0); BAR(); readW<true>(B, 0, smem, w, l); readW<true>(B, 1, smem + 65536, w, l); BAR();
  stageX(x, jj, lds0, w, l);
  WAITVM(0); BAR();
  const f32x4 z4 = {0.f, 0.f, 0.f, 0.f};
  const bool dog = (cg == 0 && w == 0);
  int buf = 0;
  for (int s = jj; s < 64; s += 9) {
    if (s + 9 < 64) stageX(x, s + 9, lds0 + (buf ^ 1) * 32768, w, l);
    lptr xb = smem + buf * 32768 + l * 16;
    f32x4 a[2][2] = {{z4, z4}, {z4, z4}};
#pragma unroll
    for (int ks = 0; ks < 16; ks++) {
      const bf16x8 x0 = *(LAS bf16x8*)(xb + ks * 1024), x1 = *(LAS bf16x8*)(xb + 16384 + ks * 1024);
#pragma unroll
      for (int y = 0; y < 2; y++) { a[0][y] = mfma32(B[y][ks], x0, a[0][y]); a[1][y] = mfma32(B[y][ks], x1, a[1][y]); }
    }
#pragma unroll
    for (int t = 0; t < 2; t++) {
      float r[8];
#pragma unroll
      for (int i = 0; i < 8; i++) {
        const float hv = rbf(rbf(a[t][i >> 2][i & 3]) + bias[i]);
        const float z = hv > 0.f ? hv : 0.f;
        r[i] = z * z;
      }
      u32x4 o = {pk(r[0], r[1]), pk(r[2], r[3]), pk(r[4], r[5]), pk(r[6], r[7])};
      st16(ws, o, (u32)l * 16u + (u32)(cg * 4 + w) * 1024u, OFF_HF + (u32)(s * 2 + t) * 57344u);
    }
    if (dog) {
      f32x4 g0 = z4, g1 = z4;
#pragma unroll
      for (int ks = 0; ks < 16; ks++) {
        g0 = mfma32(Gf[ks], *(LAS bf16x8*)(xb + ks * 1024), g0);
        g1 = mfma32(Gf[ks], *(LAS bf16x8*)(xb + 16384 + ks * 1024), g1);
      }
      const float zz = rbf(l < 16 ? g0[0] : g1[0]);
      const float gg = rbf(1.f / (1.f + __expf(-zz)));
      if (l < 32) st4(ws, __builtin_bit_cast(u32, gg), (u32)l * 4u, OFF_G + (u32)s * 128u);
    }
    WAITVM(0); BAR();
    if (w == 0 && l == 0) st4(ws, tag, 0, C_HC + (u32)s * 128u + (u32)cg * 4u);
    buf ^= 1;
  }
}

DI void attn_role(rsrc_t ws, u32 tag, int u, int l) {
  const int h = u & 31, gb = l >> 4, c = l & 15;
  bool dead = false;
  const f32x4 z4 = {0.f, 0.f, 0.f, 0.f};
  const u32x4 one8 = {0x3F803F80u, 0x3F803F80u, 0x3F803F80u, 0x3F803F80u};
  const u32x2 one4 = {0x3F803F80u, 0x3F803F80u};
  for (int s = u >> 5; s < 64; s += 4) {
    const u32 so = (u32)(s * 32 + h) * 8192u, sm = (u32)(s * 32 + h) * 4096u, lo = (u32)l * 16u;
    waitflag(ws, 0, C_KV + (u32)(h * 64 + s) * 4u, 1ull, ~0u, tag + 2u, dead, 2);
    u32x4 Q[2][4], K[2][4], V[8], KM[4];
    u32x2 VM[8];
#pragma unroll
    for (int i = 0; i < 8; i++) {
      Q[i >> 2][i & 3] = ld16(ws, lo + (i & 3) * 1024u, OFF_QX + so + (i >> 2) * 4096u);
      K[i >> 2][i & 3] = ld16(ws, lo + (i & 3) * 1024u, OFF_KX + so + (i >> 2) * 4096u);
      V[i] = ld16(ws, lo + (i & 3) * 1024u, OFF_VX + so + (i >> 2) * 4096u);
    }
    SCHED();
    f32x4 s00 = z4, s01 = z4, s11 = z4;
#pragma unroll
    for (int ks = 0; ks < 4; ks++) {
      s00 = mfma32(as8(K[0][ks]), as8(Q[0][ks]), s00);
      s01 = mfma32(as8(K[0][ks]), as8(Q[1][ks]), s01);
      s11 = mfma32(as8(K[1][ks]), as8(Q[1][ks]), s11);
    }
#pragma unroll
    for (int i = 0; i < 4; i++)
      if (4 * gb + i > c) { s00[i] = NEG; s11[i] = NEG; }
    waitflag(ws, (u32)(l & 1) * 4u, C_KM + (u32)((h * 64 + s) * 2) * 4u, 3ull, ~0u, tag, dead, s < 60 ? 2 : 0);
#pragma unroll
    for (int i = 0; i < 4; i++) KM[i] = ld16(ws, lo + i * 1024u, OFF_KM + sm);
#pragma unroll
    for (int i = 0; i < 8; i++) VM[i] = (u32x2){ld4(ws, (u32)l * 4u + i * 256u, OFF_VM + sm), 0u};
    SCHED();
    f32x4 m0 = z4, m1 = z4;
#pragma unroll
    for (int ks = 0; ks < 4; ks++) { m0 = mfma32(as8(KM[ks]), as8(Q[0][ks]), m0); m1 = mfma32(as8(KM[ks]), as8(Q[1][ks]), m1); }
#pragma unroll
    for (int t = 0; t < 2; t++) {
      const f32x4 sM = t ? m1 : m0, sa = t ? s01 : s00;
      float m = fmaxf(fmaxf(sM[0], sM[1]), fmaxf(fmaxf(sa[0], sa[1]), fmaxf(sa[2], sa[3])));
      if (t) m = fmaxf(m, fmaxf(fmaxf(s11[0], s11[1]), fmaxf(s11[2], s11[3])));
      m = maxall(m) * SCL;
      float pM[4], p0[4], p1[4];
#pragma unroll
      for (int i = 0; i < 4; i++) {
        pM[i] = i < 2 ? __builtin_amdgcn_exp2f(__builtin_fmaf(sM[i], SCL, -m)) : 0.f;
        p0[i] = __builtin_amdgcn_exp2f(__builtin_fmaf(sa[i], SCL, -m));
        p1[i] = t ? __builtin_amdgcn_exp2f(__builtin_fmaf(s11[i], SCL, -m)) : 0.f;
      }
      const u32x2 PM = {pk(pM[0], pM[1]), 0u};
      const u32x4 PX = {pk(p0[0], p0[1]), pk(p0[2], p0[3]), pk(p1[0], p1[1]), pk(p1[2], p1[3])};
      const float rs = __builtin_amdgcn_rcpf(mfma16(one4, PM, z4)[0] + mfma32(as8(one8), as8(PX), z4)[0]);
#pragma unroll
      for (int ks = 0; ks < 4; ks++) {
        f32x4 oa = mfma32(as8(V[2 * ks]), as8(PX), z4) + mfma16(VM[2 * ks], PM, z4);
        f32x4 ob = mfma32(as8(V[2 * ks + 1]), as8(PX), z4) + mfma16(VM[2 * ks + 1], PM, z4);
        u32x4 o = {pk(oa[0] * rs, oa[1] * rs), pk(oa[2] * rs, oa[3] * rs), pk(ob[0] * rs, ob[1] * rs), pk(ob[2] * rs, ob[3] * rs)};
        st16(ws, o, lo + ks * 1024u, OFF_OF + (u32)(s * 2 + t) * 131072u + (u32)h * 4096u);
      }
    }
    WAITVM(0);
    if (l == 0) st4(ws, tag, 0, C_OC + (u32)s * 128u + (u32)h * 4u);
  }
}

DI void out_role(lptr smem, rsrc_t ws, u32 tag, int grp, int xt, int sl, int w, int l, const char* Wo, const char* w2, const bf16* b2, bf16* out) {
  const u32 lds0 = (u32)(size_t)smem + (u32)w * 32768u;
  lptr wb = smem + w * 32768;
  const int gb = l >> 4, q = (l >> 2) & 3, p = l & 3, c = l & 15;
  bf16x8 A[2][32], A2[2][14];
  bool dead = false;
  {
    const u32 ro = (u32)(l >> 2) * 1024u + (u32)sl * 64u + (u32)(l & 3) * 16u;
    auto issue = [&](const char* W, int row0, int n, int buf) {
#pragma unroll
      for (int it = 0; it < 16; it++)
        if (it < n) dma16(W, ro + (u32)(row0 + it * 16) * 1024u, lds0 + buf * 16384 + it * 1024);
    };
    lptr b = wb + (4 * gb + q) * 64 + p * 16;
    issue(w2, w * 448, 16, 0);
    issue(w2, w * 448 + 256, 12, 1);
    WAITVM(12);
#pragma unroll
    for (int k = 0; k < 8; k++)
#pragma unroll
      for (int y = 0; y < 2; y++) { A2[y][k] = cat2(trr(b + k * 2048 + y * 8), trr(b + k * 2048 + 1024 + y * 8)); }
    asm volatile("s_waitcnt lgkmcnt(0)" ::: "memory");
    issue(Wo, w * 1024, 16, 0);
    WAITVM(16);
#pragma unroll
    for (int k = 0; k < 6; k++)
#pragma unroll
      for (int y = 0; y < 2; y++) { A2[y][8 + k] = cat2(trr(b + 16384 + k * 2048 + y * 8), trr(b + 16384 + k * 2048 + 1024 + y * 8)); }
#pragma unroll
    for (int r = 0; r < 4; r++) {
      asm volatile("s_waitcnt lgkmcnt(0)" ::: "memory");
      if (r < 3) { issue(Wo, w * 1024 + (r + 1) * 256, 16, (r + 1) & 1); WAITVM(16); } else { WAITVM(0); }
#pragma unroll
      for (int k = 0; k < 8; k++)
#pragma unroll
        for (int y = 0; y < 2; y++)
        { A[y][r * 8 + k] = cat2(trr(b + (r & 1) * 16384 + k * 2048 + y * 8), trr(b + (r & 1) * 16384 + k * 2048 + 1024 + y * 8)); }
    }
  }
  float bias[8];
#pragma unroll
  for (int i = 0; i < 8; i++) bias[i] = (float)b2[sl * 32 + 8 * gb + i];
#define LDO(v, s) ld16(ws, v, s)
  BAR();
  const f32x4 z4 = {0.f, 0.f, 0.f, 0.f};
  for (int si = grp; si < 65; si += 3) {
    const int s = si < 63 ? si : 63;
    const bool act = si < 63 || (c >> 3) == si - 63;
    const u32 lo = act ? (u32)l * 16u : OOB;
    f32x4 a0 = z4, a1 = z4, h0 = z4, h1 = z4;
    waitflag(ws, (u32)(l & 15) * 4u, C_HC + (u32)s * 128u, 0x3FFFull, ~0u, tag, dead, 2);
    {
      const u32 so = OFF_HF + (u32)(s * 2 + xt) * 57344u + (u32)w * 14336u;
      u32x4 F[14];
#pragma unroll
      for (int k = 0; k < 14; k++) F[k] = LDO(lo, so + k * 1024u);
      SCHED();
#pragma unroll
      for (int k = 0; k < 14; k++) { h0 = mfma32(A2[0][k], as8(F[k]), h0); h1 = mfma32(A2[1][k], as8(F[k]), h1); }
    }
    const float g = __builtin_bit_cast(float, ld4(ws, (u32)c * 4u, OFF_G + (u32)(s * 2 + xt) * 64u));
    waitflag(ws, (u32)(l & 31) * 4u, C_OC + (u32)s * 128u, 0xFFFFFFFFull, ~0u, tag, dead, s < 61 ? 1 : 0);
    {
      const u32 so = OFF_OF + (u32)(s * 2 + xt) * 131072u + (u32)w * 32768u;
      u32x4 F[16];
#pragma unroll
      for (int k = 0; k < 16; k++) F[k] = LDO(lo, so + k * 1024u);
      SCHED();
#pragma unroll
      for (int k = 0; k < 32; k++) {
        a0 = mfma32(A[0][k], as8(F[k & 15]), a0); a1 = mfma32(A[1][k], as8(F[k & 15]), a1);
        if (k < 16) {
          asm volatile("" : "+a"(a0), "+a"(a1));
          F[k] = LDO(lo, so + 16384u + k * 1024u);
          SCHED();
        }
      }
    }
    lptr rb = smem + w * 4096 + l * 16;
    *(LAS f32x4*)(rb) = a0; *(LAS f32x4*)(rb + 1024) = a1; *(LAS f32x4*)(rb + 2048) = h0; *(LAS f32x4*)(rb + 3072) = h1;
    BAR();
    if (w == 0) {
#pragma unroll
      for (int k = 1; k < 4; k++) {
        a0 += *(LAS f32x4*)(rb + k * 4096); a1 += *(LAS f32x4*)(rb + k * 4096 + 1024);
        h0 += *(LAS f32x4*)(rb + k * 4096 + 2048); h1 += *(LAS f32x4*)(rb + k * 4096 + 3072);
      }
      const float g1 = rbf(1.f - g);
      float r[8];
#pragma unroll
      for (int i = 0; i < 8; i++) {
        const float av = rbf(i < 4 ? a0[i & 3] : a1[i & 3]);
        const float bv = rbf(rbf(i < 4 ? h0[i & 3] : h1[i & 3]) + bias[i]);
        r[i] = rbf(g * av) + rbf(g1 * bv);
      }
      u32x4 o = {pk(r[0], r[1]), pk(r[2], r[3]), pk(r[4], r[5]), pk(r[6], r[7])};
      if (act) *(u32x4*)(out + (size_t)(32 * s + 16 * xt + c) * 512 + sl * 32 + 8 * gb) = o;
    }
    BAR();
  }
}

DI void probe_role(lptr smem, rsrc_t ws, u32 tag, int w, int l) {
  const u32 base = ((w & 1) ? OFF_MEM : OFF_PART) + (u32)(w >> 1) * 131072u;
  u32 best[2] = {0xFFFFFFFFu, 0xFFFFFFFFu};
#pragma unroll 1
  for (int i = 0; i < 8; i++) {
#pragma unroll
    for (int sd = 0; sd < 2; sd++) {
      u64 t0 = __builtin_amdgcn_s_memtime();
      asm volatile("s_waitcnt lgkmcnt(0)" ::: "memory");
      st4(ws, 0xFFFFFFFFu, l == 0 ? 0u : OOB, base + (u32)i * 16384u + (u32)sd * 8192u);
      WAITVM(0);
      u64 t1 = __builtin_amdgcn_s_memtime();
      best[sd] = min(best[sd], (u32)(t1 - t0));
    }
  }
  if (l == 0) { *(LAS u32*)(smem + w * 8) = best[0]; *(LAS u32*)(smem + w * 8 + 4) = best[1]; }
  BAR();
  if (w == 0 && l == 0) {
    LAS u32* t = (LAS u32*)smem;
    const u32 a = min(t[0], t[4]), b = min(t[1], t[5]), c = min(t[2], t[6]), d = min(t[3], t[7]);
    st4(ws, tag | (b < a ? 1u : 0u) | (d < c ? 2u : 0u), 0, C_SEL);
  }
  BAR();
}

__global__ __launch_bounds__(256, 1) void fused_kernel(const char* x, const bf16* mem0, const char* Wq, const char* Wk, const char* Wv,
                                                      const char* Wo, const char* w1, const bf16* b1, const char* w2, const bf16* b2,
                                                      const bf16* gw, bf16* out, char* wsb) {
  extern __shared__ __attribute__((aligned(16))) char smem_[];
  lptr smem = (lptr)smem_;
  const int b = blockIdx.x, xq = b & 7, r = b >> 3;
  const int w = __builtin_amdgcn_readfirstlane(threadIdx.x >> 6), l = threadIdx.x & 63;
  const rsrc_t ws = __builtin_amdgcn_make_buffer_rsrc(wsb, 0, (int)WS_BYTES, 0x00020000);
  if (xq < 4 && r < 16) {
    chain_role(smem, ws, 8 * xq + (r >> 1), r & 1, w, l, Wq, Wk, Wv, Wo, mem0, wsb);
    return;
  }
  const int head = xq < 4 ? 8 * xq + ((r - 16) >> 1) : 8 * (xq - 4) + (r >> 2);
  const int j = xq < 4 ? 4 + (r & 1) : (r & 3);
  if (b == 31 * 8) probe_role(smem, ws, gettag(wsb), w, l);
  const u32 tag = qkv_role(smem, ws, wsb, head, j, w, l, x, Wq, Wk, Wv, Wo, w1);
  const int wi = (xq - 4) * 32 + r;
  if (xq >= 4 && wi < 126) ffn1_role(smem, ws, tag, wi / 9, wi % 9, w, l, x, w1, b1, gw);
  if (xq >= 4) {
    if (r >= 24) {
      attn_role(ws, tag, ((xq - 4) * 8 + (r - 24)) * 4 + w, l);
    } else {
      const int g2 = (xq - 4) >> 1;
      out_role(smem, ws, tag, r < 16 ? g2 : 2, xq & 1, r < 16 ? r : r - 16 + 8 * g2, w, l, Wo, w2, b2, out);
    }
  }
}

extern "C" void kernel_launch(void* const* d_in, const int* in_sizes, int n_in, void* d_out, int out_size, void* d_ws,
                              size_t ws_size, hipStream_t stream) {
  char* base = (char*)(((size_t)d_ws + 2 * MB - 1) & ~(size_t)(2 * MB - 1));
  (void)hipFuncSetAttribute((const void*)fused_kernel, hipFuncAttributeMaxDynamicSharedMemorySize, LDS_BYTES);
  fused_kernel<<<256, 256, LDS_BYTES, stream>>>((const char*)d_in[0], (const bf16*)d_in[1], (const char*)d_in[2], (const char*)d_in[3],
                                                (const char*)d_in[4], (const char*)d_in[5], (const char*)d_in[8], (const bf16*)d_in[9],
                                                (const char*)d_in[10], (const bf16*)d_in[11], (const bf16*)d_in[12], (bf16*)d_out, base);
}
```

```cpp
#include <hip/hip_runtime.h>
#include <hip/hip_bf16.h>

typedef unsigned u32;
typedef unsigned long long u64;
typedef __bf16 bf16;
typedef bf16 bf16x2 __attribute__((ext_vector_type(2)));
typedef bf16 bf16x8 __attribute__((ext_vector_type(8)));
typedef bf16 bf16x16 __attribute__((ext_vector_type(16)));
typedef short s16x4 __attribute__((ext_vector_type(4)));
typedef short s16x8 __attribute__((ext_vector_type(8)));
typedef short s16x16 __attribute__((ext_vector_type(16)));
typedef float f32x2 __attribute__((ext_vector_type(2)));
typedef float f32x4 __attribute__((ext_vector_type(4)));
typedef u32 u32x2 __attribute__((ext_vector_type(2)));
typedef u32 u32x4 __attribute__((ext_vector_type(4)));
typedef __amdgpu_buffer_rsrc_t rsrc_t;
#define LAS __attribute__((address_space(3)))
typedef LAS char* lptr;
#define DI __device__ __forceinline__

constexpr u32 MB = 1u << 20;
constexpr u32 OFF_PART = 0;
constexpr u32 OFF_MEM = 94 * MB;
constexpr u32 OFF_CTRL = 96 * MB;
constexpr u32 C_SEL = OFF_CTRL;
constexpr u32 C_EP = OFF_CTRL + 256;
constexpr u32 C_RDY = OFF_CTRL + 512;
constexpr u32 C_KV = OFF_CTRL + 1024;
constexpr u32 C_KM = C_KV + 8192;
constexpr u32 C_OC = C_KM + 16384;
constexpr u32 C_HC = C_OC + 8192;
constexpr u32 C_END = C_HC + 8192;
constexpr u32 OFF_QX = 4 * MB, OFF_KX = 20 * MB, OFF_VX = 36 * MB;
constexpr u32 OFF_KM = 52 * MB, OFF_VM = 60 * MB, OFF_OF = 68 * MB, OFF_HF = 84 * MB, OFF_G = 92 * MB;
constexpr u32 WS_BYTES = 97 * MB;
DI u32 slot_off(int s) { const u32 r = (u32)(63 - s); return (r >> 1) * 65536u + (r & 1u) * 2048u; }
DI u32 tile_off(int t) { return (u32)(t >> 2) * 16384u + (u32)((t >> 1) & 1) * 4096u + (u32)(t & 1) * 1024u; }
constexpr int LDS_BYTES = 160 * 1024;
constexpr int SPIN_MAX = 1 << 15;
constexpr int AUX = 16;

DI u32x4 ld16(rsrc_t r, u32 v, u32 s) { return __builtin_amdgcn_raw_buffer_load_b128(r, v, s, AUX); }
DI u32x2 ld8(rsrc_t r, u32 v, u32 s) { return __builtin_amdgcn_raw_buffer_load_b64(r, v, s, AUX); }
DI u32 ld4(rsrc_t r, u32 v, u32 s) { return __builtin_amdgcn_raw_buffer_load_b32(r, v, s, AUX); }
DI void st16(rsrc_t r, u32x4 x, u32 v, u32 s) {
  __builtin_amdgcn_raw_buffer_store_b128(x, r, v, s, AUX);
  asm volatile("s_nop 1" ::"v"(x) : "memory");
}
DI void st8(rsrc_t r, u32x2 x, u32 v, u32 s) { __builtin_amdgcn_raw_buffer_store_b64(x, r, v, s, AUX); }
DI void st4(rsrc_t r, u32 x, u32 v, u32 s) { __builtin_amdgcn_raw_buffer_store_b32(x, r, v, s, AUX); }
#define MEMBAR() asm volatile("" ::: "memory")
#define WAITVM(n) asm volatile("s_waitcnt vmcnt(" #n ")" ::: "memory")
#define WAITVM0() do { MEMBAR(); __builtin_amdgcn_s_waitcnt(0x0F70); MEMBAR(); } while (0)
#define BAR() asm volatile("s_waitcnt lgkmcnt(0)\n\ts_barrier" ::: "memory")
#define SCHED() __builtin_amdgcn_sched_barrier(0)
#define PINA(x) asm volatile("" : "+a"(x))
#define PINV(x) asm volatile("" : "+v"(x))
constexpr u32 OOB = 0x80000000u;

DI void dma16(const char* sbase, u32 voff, u32 ldsdst) {
  u32 keep;
  asm volatile("s_mov_b32 %0, m0\n\ts_mov_b32 m0, %3\n\ts_nop 0\n\tglobal_load_lds_dwordx4 %1, %2\n\ts_mov_b32 m0, %0"
               : "=&s"(keep) : "v"(voff), "s"(sbase), "s"(ldsdst) : "memory");
}
DI s16x4 trr(lptr p) { return __builtin_amdgcn_ds_read_tr16_b64_v4i16((LAS s16x4*)p); }
DI bf16x8 cat2(s16x4 a, s16x4 b) { return __builtin_bit_cast(bf16x8, __builtin_shufflevector(a, b, 0, 1, 2, 3, 4, 5, 6, 7)); }
DI bf16x16 cat4(s16x4 a, s16x4 b, s16x4 c, s16x4 d) {
  s16x8 x = __builtin_shufflevector(a, b, 0, 1, 2, 3, 4, 5, 6, 7), y = __builtin_shufflevector(c, d, 0, 1, 2, 3, 4, 5, 6, 7);
  return __builtin_bit_cast(bf16x16, __builtin_shufflevector(x, y, 0, 1, 2, 3, 4, 5, 6, 7, 8, 9, 10, 11, 12, 13, 14, 15));
}
DI u32 pk(float lo, float hi) { f32x2 v = {lo, hi}; return __builtin_bit_cast(u32, __builtin_convertvector(v, bf16x2)); }
DI u32 pk2(f32x2 v) { return __builtin_bit_cast(u32, __builtin_convertvector(v, bf16x2)); }
DI float rbf(float x) { return (float)(bf16)x; }
DI bf16x8 as8(u32x4 x) { return __builtin_bit_cast(bf16x8, x); }
DI f32x4 mfma32(bf16x8 a, bf16x8 b, f32x4 c) { return __builtin_amdgcn_mfma_f32_16x16x32_bf16(a, b, c, 0, 0, 0); }
DI f32x4 mfma16(u32x2 a, u32x2 b, f32x4 c) {
  return __builtin_amdgcn_mfma_f32_16x16x16bf16_1k(__builtin_bit_cast(s16x4, a), __builtin_bit_cast(s16x4, b), c, 0, 0, 0);
}
DI f32x4 smf(u32x4 a, bf16x16 b, f32x4 c, int idx) { return __builtin_amdgcn_smfmac_f32_16x16x64_bf16(as8(a), b, c, idx, 0, 0); }
DI float comb(float x, float y) {
  auto r = __builtin_amdgcn_permlane32_swap(__builtin_bit_cast(u32, x), __builtin_bit_cast(u32, y), false, false);
  return __builtin_bit_cast(float, (u32)r[0]) + __builtin_bit_cast(float, (u32)r[1]);
}
DI float maxall(float m) {
  auto r = __builtin_amdgcn_permlane32_swap(__builtin_bit_cast(u32, m), __builtin_bit_cast(u32, m), false, false);
  m = fmaxf(__builtin_bit_cast(float, (u32)r[0]), __builtin_bit_cast(float, (u32)r[1]));
  auto q = __builtin_amdgcn_permlane16_swap(__builtin_bit_cast(u32, m), __builtin_bit_cast(u32, m), false, false);
  return fmaxf(__builtin_bit_cast(float, (u32)q[0]), __builtin_bit_cast(float, (u32)q[1]));
}
DI f32x4 zero4() {
  u64 a, b;
  asm volatile("v_mov_b64 %0, 0" : "=v"(a));
  asm volatile("v_mov_b64 %0, 0" : "=v"(b));
  typedef u64 u64x2 __attribute__((ext_vector_type(2)));
  u64x2 r = {a, b};
  return __builtin_bit_cast(f32x4, r);
}
DI u32 umax8(u32x4 a, u32x4 b) { return max(max(max(a[0], a[1]), max(a[2], a[3])), max(max(b[0], b[1]), max(b[2], b[3]))); }
DI bool anyl(bool p) { return __builtin_amdgcn_ballot_w64(p) != 0; }
DI void waitflag(rsrc_t r, u32 v, u32 s, u64 lanes, u32 msk, u32 val, bool& dead, int sleep) {
  int n = 0;
#pragma unroll 1
  while (!dead) {
    MEMBAR();
    const u32 x = ld4(r, v, s);
    if ((__builtin_amdgcn_ballot_w64((x & msk) != val) & lanes) == 0) break;
    asm volatile("" : "+s"(n));
    if (++n > SPIN_MAX) dead = true;
    for (int i = 0; i < sleep; i++) __builtin_amdgcn_s_sleep(4);
  }
  MEMBAR();
}
DI u32 gettag(const char* wsb) {
  u32 t = *(const __attribute__((address_space(4))) u32*)(wsb + C_EP);
  t = (t + 4u) & ~3u;
  asm volatile("" : "+s"(t));
  return t;
}
constexpr float NEG = -3.0e38f;
constexpr float SCL = 0.08838834764831845f * 1.4426950408889634f;

DI void chain_role(lptr smem, rsrc_t ws, int h, int a, int w, int l, const char* Wq, const char* Wk, const char* Wv,
                   const char* Wo, const bf16* mem0, const char* wsb) {
  const int gb = l >> 4, q = (l >> 2) & 3, p = l & 3, c = l & 15;
  const u32 lds0 = (u32)(size_t)smem;
  bf16x16 Bq[2][8], Bk[2][8], Bv[8], Bo[8];
  bool dead = false;
  const int ci = 2 * h + a;
  const int rho = 2 * ci + (w & 1);
  const int rt = rho >> 4, gs = (rho >> 2) & 3, ru = rho & 3;
  const u32 rpiece = tile_off(rt) + (u32)ru * 256u + (u32)gs * 64u;
  const u32 rpub4 = c < 4 ? rpiece + (u32)(c & 3) * 16u + (u32)gb * 4u : OOB;
  const u32 rblk0 = (u32)(rho >> 1) * 16384u + (u32)(rho & 1) * 4096u;
  const u32 lane0 = l == 0 ? 0u : OOB;
  const u32x4 ones = {~0u, ~0u, ~0u, ~0u};
  u32 selw, rdy, flg, tag;
  {
    const u32 cp = (u32)(w >> 1) * 8192u;
    const u32 bx = (u32)((l & 15) ^ ((l >> 5) << 1));
    const u32 rowoff = (u32)(64 * w + (l >> 4)) * 8192u + (u32)h * 256u;
    auto issueQK = [&](const char* W, int half, int buf) {
#pragma unroll
      for (int it = 0; it < 16; it++) {
        const u32 cit = (u32)((((it >> 3) & 1) << 3) | (((it >> 2) & 1) << 2));
        dma16(W, rowoff + (u32)(half * 256 + it * 4) * 8192u + ((bx ^ cit) << 4), lds0 + buf * 65536 + (w * 16 + it) * 1024);
      }
    };
    auto issueV = [&](int buf) {
      const u32 bv = (u32)((l & 7) ^ ((l >> 5) << 1));
      const u32 ro = (u32)(128 * w + ((l >> 3) & 1) * 32 + (l >> 4)) * 8192u + (u32)h * 256u + (u32)a * 128u;
#pragma unroll
      for (int it = 0; it < 16; it++)
        dma16(Wv, ro + (u32)((it >> 3) * 64 + (it & 7) * 4) * 8192u + ((bv ^ (u32)(((it >> 2) & 1) << 2)) << 4),
              lds0 + buf * 65536 + (w * 16 + it) * 1024);
    };
    auto issueO = [&](int buf) {
      const u32 ro = (u32)(h * 128 + a * 64 + w * 16) * 1024u;
#pragma unroll
      for (int it = 0; it < 16; it++) {
        const u32 f = (u32)((((it >> 3) & 1) << 2) | (((it >> 2) & 1) << 1) | (it & 1));
        dma16(Wo, ro + (u32)it * 1024u + (((u32)l ^ (f << 1)) << 4), lds0 + buf * 65536 + (w * 16 + it) * 1024);
      }
    };
    const int rowl = 32 * (q & 1) + 16 * (q >> 1) + 2 * gb;
    const int fq = ((q & 1) << 2) | ((q >> 1) << 1) | (gb & 1);
    auto readQK = [&](bf16x16(&B)[2][8], int half, int buf) {
#pragma unroll
      for (int y = 0; y < 2; y++) {
        lptr b = smem + buf * 65536 + rowl * 256 + (((4 * w + 2 * y + (p >> 1)) ^ (fq << 1)) << 4) + (p & 1) * 8;
#pragma unroll
        for (int t = 0; t < 4; t++)
        {
          lptr bt = b + (t ^ w) * 16384;
          B[y][half * 4 + t] = cat4(trr(bt), trr(bt + 256), trr(bt + 2048), trr(bt + 2304));
          PINA(B[y][half * 4 + t]);
        }
      }
    };
    dma16((const char*)mem0, (u32)(w * 2048 + l * 16), lds0 + 135168u + (u32)w * 2048u);
    dma16((const char*)mem0, (u32)(w * 2048 + 1024 + l * 16), lds0 + 136192u + (u32)w * 2048u);
    issueQK(Wq, 0, 0);
    issueQK(Wq, 1, 1);
    SCHED();
#pragma unroll
    for (int k = 0; k < 8; k++) st16(ws, ones, rblk0 + (u32)l * 16u + (k & 3) * 1024u, OFF_PART + (k >> 2) * MB + cp);
#pragma unroll
    for (int k = 0; k < 4; k++) {
      const int s = 1 + k * 16 + (l >> 2);
      st16(ws, ones, s < 64 ? slot_off(s) + rpiece + (u32)(l & 3) * 16u : OOB, OFF_MEM + cp);
    }
    SCHED();
    tag = gettag(wsb);
    WAITVM(28); BAR(); readQK(Bq, 0, 0);
    {
      const int d0 = 64 * rt + 16 * ((c >> 1) & 1) + 4 * ru + gb, r0 = 2 * gs + (c & 1);
      LAS unsigned short* m0 = (LAS unsigned short*)(smem + 135168) + r0 * 512 + d0;
      const u32 mv = (u32)m0[0] | ((u32)m0[32] << 16);
      BAR();
      st4(ws, mv, rpub4, OFF_MEM + cp + slot_off(0));
      SCHED();
    }
    issueQK(Wk, 0, 0);
    WAITVM(29); BAR(); readQK(Bq, 1, 1); BAR(); issueQK(Wk, 1, 1);
    WAITVM(16); BAR();
    st4(ws, tag, w == 0 ? lane0 : OOB, C_RDY + (u32)ci * 4u);
    SCHED();
    readQK(Bk, 0, 0); BAR(); issueV(0);
    WAITVM(16); BAR(); readQK(Bk, 1, 1); BAR(); issueO(1);
    WAITVM(16); BAR();
    {
      const int f2 = ((q >> 1) << 1) | (gb & 1);
      lptr b = smem + (16 * (q >> 1) + 2 * gb) * 256 + (q & 1) * 128 + (((2 * w + (p >> 1)) ^ (f2 << 1)) << 4) + (p & 1) * 8;
#pragma unroll
      for (int t = 0; t < 8; t++)
      {
        lptr bt = b + (t ^ w) * 8192;
        Bv[t] = cat4(trr(bt), trr(bt + 256), trr(bt + 2048), trr(bt + 2304));
        PINV(Bv[t]);
      }
    }
    SCHED();
    selw = ld4(ws, 0, C_SEL); rdy = ld4(ws, (u32)l * 4u, C_RDY); flg = ld4(ws, 0, C_KV + (u32)(h * 64) * 4u);
    SCHED();
    WAITVM0(); BAR();
    {
      const int fo = ((q >> 1) << 2) | ((gb & 1) << 1) | (q & 1);
      const int er = 16 * (gb >> 1) + 8 * (q >> 1) + 4 * (gb & 1) + (q & 1);
#pragma unroll
      for (int T = 0; T < 8; T++) {
        lptr b = smem + 65536 + er * 1024 + (((16 * w + 2 * T + (p >> 1)) ^ (fo << 1)) << 4) + (p & 1) * 8;
        Bo[T] = cat4(trr(b), trr(b + 2048), trr(b + 32768), trr(b + 32768 + 2048));
        PINV(Bo[T]);
      }
    }
    BAR();
  }
  if (anyl(rdy != tag)) waitflag(ws, (u32)l * 4u, C_RDY, ~0ull, ~0u, tag, dead, 1);
  if (anyl((selw & ~3u) != tag)) { waitflag(ws, 0, C_SEL, 1ull, ~3u, tag, dead, 1); selw = ld4(ws, 0, C_SEL); }
  selw = __builtin_amdgcn_readfirstlane(selw);
  flg = __builtin_amdgcn_readfirstlane(flg);
  const u32 sel0 = (selw & 1u) * 8192u, sel1 = ((selw >> 1) & 1u) * 8192u;
  const u32 rgat = rblk0 + sel0 + (u32)(4 * gb + (c & 3)) * 64u + (u32)(c >> 2) * 16u;
  constexpr u32 GK = 1024u;
  const u32 pst = (u32)(w * 16 + (l >> 3)) * 16384u + (u32)((l >> 2) & 1) * 4096u + sel0 + (u32)ci * 64u + (u32)(l & 3) * 16u;
  const u32 kvlane = (u32)l * 16u;
  lptr Lq = smem, Lk = smem + 6144, Lx = smem + 12288, Lv = smem + 20480, La = smem + 24576, Lo = smem + 49152;
#pragma unroll
  for (int i = 0; i < 3; i++) *(LAS u32x4*)(smem + i * 4096 + w * 1024 + l * 16) = (u32x4){0u, 0u, 0u, 0u};
  auto fetch_kv = [&](int sn) {
    if ((flg & ~3u) != tag) waitflag(ws, 0, C_KV + (u32)(h * 64 + sn) * 4u, 1ull, ~3u, tag, dead, 1);
    const u32 so = (u32)(sn * 32 + h) * 8192u;
    u32x4 t[6];
    const u32 fl = ld4(ws, 0, C_KV + (u32)(h * 64 + sn + 1) * 4u);
    SCHED();
#pragma unroll
    for (int i = 0; i < 4; i++) t[i] = ld16(ws, kvlane, OFF_KX + so + (u32)(w - 2) * 4096u + i * 1024u);
#pragma unroll
    for (int i = 0; i < 2; i++) t[4 + i] = ld16(ws, kvlane, OFF_VX + so + (u32)(4 * a + 2 * (w - 2) + i) * 1024u);
    SCHED();
    flg = __builtin_amdgcn_readfirstlane(fl);
#pragma unroll
    for (int i = 0; i < 4; i++) *(LAS u32x4*)(Lx + (w - 2) * 4096 + i * 1024 + l * 16) = t[i];
#pragma unroll
    for (int i = 0; i < 2; i++) *(LAS u32x4*)(Lv + (w - 2) * 2048 + i * 1024 + l * 16) = t[4 + i];
  };
  if (w >= 2) fetch_kv(0);
  WAITVM0();
  BAR();
  const u32 kmoff = (l >> 5) == a ? kvlane + (u32)w * 1024u : OOB;
  const u32 flgoff = w == 2 ? lane0 : OOB;
  const int idx = (l & 2) ? 0xEEEE : 0x4444;
  const int wq = (4 * w + (c >> 2)) * 384 + (c & 3) * 32 + gb * 8;
  const int rq = (4 * w + gb) * 384 + q * 32 + p * 8;
  const bool up = (c & 2) != 0;
  u32 pmask = up ? 0xFFFF0000u : 0x0000FFFFu;
  u32 tagv = tag;
  PINV(pmask); PINV(tagv);
  lptr Ls = smem + 53248;
  lptr Lar[4] = {La, La + (w ^ 1) * 1024 + l * 16, La + (w ^ 2) * 1024 + l * 16, La + (w ^ 3) * 1024 + l * 16};
  lptr Lkw = Lk + wq, Lsw = Ls + 4096 + w * 256 + l * 4;
  PINV(Lar[1]); PINV(Lar[2]); PINV(Lar[3]); PINV(Lkw); PINV(Lsw);
  const int orow = 2 * (c >> 2) + (c & 1);
  u32x4 selB;
  {
    const int j = c & 7;
    const u32 one = (j & 1) ? 0x3F800000u : 0x00003F80u;
    selB[0] = (j >> 1) == 0 ? one : 0u; selB[1] = (j >> 1) == 1 ? one : 0u;
    selB[2] = (j >> 1) == 2 ? one : 0u; selB[3] = (j >> 1) == 3 ? one : 0u;
  }
  u32x4 one8 = {0x3F803F80u, 0x3F803F80u, 0x3F803F80u, 0x3F803F80u};
  PINV(one8); PINV(selB);
#define SM2(d0, d1, B, t)                                                                                            \
  asm volatile("v_smfmac_f32_16x16x64_bf16 %0, %2, %3, %5\n\tv_smfmac_f32_16x16x64_bf16 %1, %2, %4, %5"              \
               : "+v"(d0), "+v"(d1) : "v"(A[t]), "a"(B[0][t]), "a"(B[1][t]), "v"(idx));                              \
  SCHED()
#define VSTEP(t)                                                                                                     \
  asm volatile("v_smfmac_f32_16x16x64_bf16 %0, %1, %2, %3" : "+v"(v0) : "v"(A[t]), "v"(Bv[t]), "v"(idx));            \
  SCHED()
#define MF0(D, F, G) asm volatile("v_mfma_f32_16x16x32_bf16 %0, %1, %2, 0" : "=&v"(D) : "v"(F), "v"(G)); SCHED()
#define MF1(D, F, G) asm volatile("v_mfma_f32_16x16x32_bf16 %0, %1, %2, %0" : "+v"(D) : "v"(F), "v"(G)); SCHED()
#define FOLD(D) pk2(__builtin_shufflevector(D, D, 0, 1) + __builtin_shufflevector(D, D, 2, 3))
#define UNDEF(x) asm volatile("" : "=v"(x))
#define NAP(n) do { if (n) __builtin_amdgcn_s_sleep(n); } while (0)
#define KVD 0
#define D1 10
#define D2 8
#define D3 10

  for (int s = 0; s < 64; s++) {
    const u32 mslot = OFF_MEM + sel1 + slot_off(s) + tile_off(w);
    u32x4 A[8];
    u32x4 g0 = ld16(ws, kvlane, mslot); SCHED();
    u32x4 g1 = ld16(ws, kvlane, mslot + 16384u); SCHED();
    f32x4 q0 = zero4(), q1 = zero4(), k0 = zero4(), k1 = zero4(), v0 = zero4();
    u32 parS = OFF_PART + (u32)(s & 1) * MB, parT = parS + 8 * 16384u, rstS = parS ^ MB, rstT = parT ^ MB;
    u32 kmS = OFF_KM + (u32)(s * 32 + h) * 4096u, vmS = kmS + (OFF_VM - OFF_KM), pubS = OFF_MEM + sel1 + slot_off(s + 1);
    u32 flS = C_KM - 8u + (u32)(h * 128 + a) * 4u + (u32)s * 8u, flV = s > 0 ? flgoff : OOB;
    asm volatile("" : "+s"(parS), "+s"(parT), "+s"(rstS), "+s"(rstT), "+s"(kmS), "+s"(vmS), "+s"(pubS), "+s"(flS), "+v"(flV));
    SCHED();
    *(LAS u32x4*)(La + w * 1024 + l * 16) = g0;
    *(LAS u32x4*)(La + w * 1024 + 4096 + l * 16) = g1;
    SCHED();
    if (__builtin_expect(anyl(umax8(g0, g1) == 0xFFFFFFFFu), 0)) {
      int n = 0;
      asm volatile("" : "+v"(g0), "+v"(g1));
#pragma unroll 1
      while (anyl(umax8(g0, g1) == 0xFFFFFFFFu) && !dead) {
        MEMBAR();
        g0 = ld16(ws, kvlane, mslot);
        g1 = ld16(ws, kvlane, mslot + 16384u);
        WAITVM0();
        if (++n > SPIN_MAX) dead = true;
      }
      *(LAS u32x4*)(La + w * 1024 + l * 16) = g0;
      *(LAS u32x4*)(La + w * 1024 + 4096 + l * 16) = g1;
    }
    BAR();
    A[0] = g0; A[4] = g1;
#define RDA(t) A[t] = *(LAS u32x4*)(Lar[t & 3] + (t & 4) * 1024); SCHED()
    RDA(1); RDA(2);
    SM2(q0, q1, Bq, 0);
    RDA(3);
    SM2(q0, q1, Bq, 4);
    RDA(5);
    SM2(k0, k1, Bk, 0);
    RDA(6);
    SM2(k0, k1, Bk, 4);
    RDA(7);
    SM2(q0, q1, Bq, 1);
    __builtin_amdgcn_raw_buffer_store_b128(ones, ws, pst, rstS, AUX); SCHED();
    SM2(q0, q1, Bq, 2);
    __builtin_amdgcn_raw_buffer_store_b128(ones, ws, pst, rstT, AUX); SCHED();
    SM2(q0, q1, Bq, 3);
    st4(ws, tagv, flV, flS); SCHED();
    SM2(q0, q1, Bq, 5);
    const u32x4 X0 = *(LAS u32x4*)(Lx + w * 1024 + l * 16), X1 = *(LAS u32x4*)(Lx + 4096 + w * 1024 + l * 16);
    SCHED();
    SM2(q0, q1, Bq, 6); SM2(q0, q1, Bq, 7);
    const u32x2 vxa = *(LAS u32x2*)(Lv + w * 1024 + l * 16);
    asm volatile("" ::: "memory");
    const u32x2 vxb = *(LAS u32x2*)(Lv + w * 1024 + l * 16 + 8);
    SCHED();
    SM2(k0, k1, Bk, 1);
    asm volatile("" : "+v"(q0), "+v"(q1));
    {
      const u32 a0 = FOLD(q0), a1 = FOLD(q1);
      *(LAS u32*)(Lq + wq) = a0; *(LAS u32*)(Lq + wq + 4) = a0; SCHED();
      SM2(k0, k1, Bk, 2);
      *(LAS u32*)(Lq + wq + 128) = a1; *(LAS u32*)(Lq + wq + 132) = a1; SCHED();
    }
    SM2(k0, k1, Bk, 3);
    const bf16x8 Fq = cat2(trr(Lq + rq), trr(Lq + rq + 128));
    SCHED();
    f32x4 D[8];
    SM2(k0, k1, Bk, 5);
    u32x4 VA = {0u, 0u, vxa[0], vxa[1]}, VB = {0u, 0u, vxb[0], vxb[1]};
    PINV(VA); PINV(VB);
    SCHED();
    SM2(k0, k1, Bk, 6);
    D[0] = zero4(); D[1] = zero4();
    SCHED();
    SM2(k0, k1, Bk, 7);
    f32x4 sM, s0, s1;
    MF0(s0, X0, Fq);
    MF0(s1, X1, Fq);
    VSTEP(0);
    asm volatile("" : "+v"(k0), "+v"(k1));
    { const u32 a0 = FOLD(k0), a1 = FOLD(k1); *(LAS u32*)(Lkw) = a0; *(LAS u32*)(Lkw + 128) = a1; SCHED(); }
    const bf16x8 Fk = cat2(trr(Lk + rq), trr(Lk + rq + 128));
    SCHED();
    VSTEP(1);
    asm volatile("" : "+v"(s0), "+v"(s1));
    {
      const f32x4 x = {up ? s1[0] : s0[0], up ? s1[1] : s0[1], up ? s1[2] : s0[2], up ? s1[3] : s0[3]};
      SCHED();
      VSTEP(2);
      *(LAS f32x4*)(Ls + w * 1024 + l * 16) = x;
      SCHED();
    }
    VSTEP(3);
    D[2] = zero4(); SCHED();
    VSTEP(4);
    D[3] = zero4(); SCHED();
    VSTEP(5);
    MF0(sM, Fk, Fq);
    VSTEP(6); VSTEP(7);
    st16(ws, __builtin_bit_cast(u32x4, Fk), kmoff, kmS);
    asm volatile("" : "+v"(sM));
    *(LAS float*)(Lsw) = up ? sM[1] : sM[0];
    SCHED();
    BAR();
    f32x4 xs[4];
    float ms[4];
#pragma unroll
    for (int j = 0; j < 4; j++) ms[j] = *(LAS float*)(Ls + 4096 + j * 256 + l * 4);
    SCHED();
#pragma unroll
    for (int j = 0; j < 4; j++) { xs[j] = *(LAS f32x4*)(Ls + j * 1024 + l * 16); SCHED(); }
    asm volatile("" : "+v"(v0));
    const u32 vm = FOLD(v0);
    st4(ws, vm, (u32)l * 4u + (u32)(4 * a + w) * 256u, vmS);
    if (s == 63) break;
    VA[0] = vm; VB[0] = vm;
    PINV(VA); PINV(VB);
    SCHED();
    float sm = ms[0] + ms[1], sn = ms[2] + ms[3];
    PINV(sm); PINV(sn);
    sm += sn;
    PINV(sm);
    SCHED();
    const f32x4 sx = (xs[0] + xs[1]) + (xs[2] + xs[3]);
    float m, mt;
    asm volatile("v_max3_f32 %0, %2, %3, %6\n\tv_max3_f32 %0, %0, %4, %5\n\ts_nop 1\n\t"
                 "v_max_f32_dpp %1, %0, %0 quad_perm:[2,3,0,1] row_mask:0xf bank_mask:0xf\n\t"
                 "v_max_f32_dpp %0, %0, %0 quad_perm:[2,3,0,1] row_mask:0xf bank_mask:0xf\n\ts_nop 1\n\t"
                 "v_permlane32_swap_b32 %0, %1\n\tv_max_f32 %0, %0, %1\n\tv_mov_b32 %1, %0\n\ts_nop 1\n\t"
                 "v_permlane16_swap_b32 %0, %1\n\tv_max_f32 %0, %0, %1"
                 : "=&v"(m), "=&v"(mt) : "v"(sx[0]), "v"(sx[1]), "v"(sx[2]), "v"(sx[3]), "v"(sm));
    m *= -SCL;
    const f32x2 m2 = {m, m}, scl2 = {SCL, SCL};
#define EXP(t) pk(__builtin_amdgcn_exp2f(t[0]), __builtin_amdgcn_exp2f(t[1]))
    float tM;
    asm volatile("v_fma_f32 %0, %1, %2, %3" : "=v"(tM) : "v"(sm), "s"(SCL), "v"(m));
    const f32x2 t0 = __builtin_elementwise_fma(__builtin_shufflevector(sx, sx, 0, 1), scl2, m2),
                t1 = __builtin_elementwise_fma(__builtin_shufflevector(sx, sx, 2, 3), scl2, m2);
    SCHED();
    const float eM = __builtin_amdgcn_exp2f(tM);
    const u32x4 P = {pk(eM, eM) & pmask, 0u, EXP(t0), EXP(t1)};
    f32x4 sum, oa, ob;
    asm volatile("s_nop 1\n\tv_mfma_f32_16x16x32_bf16 %0, %3, %4, 0\n\tv_mfma_f32_16x16x32_bf16 %2, %6, %4, 0\n\t"
                 "v_mfma_f32_16x16x32_bf16 %1, %5, %4, 0"
                 : "=&v"(sum), "=&v"(oa), "=&v"(ob) : "v"(one8), "v"(P), "v"(VA), "v"(VB));
    {
      float rs;
      f32x4 o;
      asm volatile("v_add_f32_dpp %0, %1, %1 quad_perm:[2,3,0,1] row_mask:0xf bank_mask:0xf\n\ts_nop 0\n\tv_rcp_f32 %0, %0" : "=&v"(rs) : "v"(sum[0]));
      D[4] = zero4(); D[5] = zero4(); D[6] = zero4();
      asm volatile("v_add_f32_dpp %0, %4, %8 quad_perm:[2,3,0,1] row_mask:0xf bank_mask:0xf\n\t"
                   "v_add_f32_dpp %1, %5, %9 quad_perm:[2,3,0,1] row_mask:0xf bank_mask:0xf\n\t"
                   "v_add_f32_dpp %2, %6, %10 quad_perm:[2,3,0,1] row_mask:0xf bank_mask:0xf\n\t"
                   "v_add_f32_dpp %3, %7, %11 quad_perm:[2,3,0,1] row_mask:0xf bank_mask:0xf"
                   : "=&v"(o[0]), "=&v"(o[1]), "=&v"(o[2]), "=&v"(o[3])
                   : "v"(ob[0]), "v"(ob[1]), "v"(ob[2]), "v"(ob[3]), "v"(oa[0]), "v"(oa[1]), "v"(oa[2]), "v"(oa[3]));
      const f32x2 r2 = {rs, rs};
      u32x2 oo = {pk2(__builtin_shufflevector(o, o, 0, 1) * r2), pk2(__builtin_shufflevector(o, o, 2, 3) * r2)};
      *(LAS u32x2*)(Lo + (orow + ((c & 2) << 2)) * 144 + w * 32 + gb * 8) = oo;
    }
    WAITVM(2);
    BAR();
    const u32x4 Ao = *(LAS u32x4*)(Lo + orow * 144 + gb * 32 + ((c >> 1) & 1) * 16);
    SCHED();
    D[7] = zero4();
    SCHED();
#define OST(T) asm volatile("v_smfmac_f32_16x16x64_bf16 %0, %1, %2, %3" : "+v"(D[T]) : "v"(Ao), "v"(Bo[T]), "v"(idx)); SCHED()
#define OAD(T) asm volatile("v_pk_add_f32 %0, %1, %2" : "=v"(f[T]) : "v"(__builtin_shufflevector(D[T], D[T], 0, 1)), "v"(__builtin_shufflevector(D[T], D[T], 2, 3))); SCHED()
#define OCV(P_, i, T) asm volatile("v_cvt_pk_bf16_f32 %0, %1, %2" : "=v"(P_[i]) : "v"(f[T][0]), "v"(f[T][1])); SCHED()
#define NOP(n) asm volatile("s_nop " #n); SCHED()
    {
      u32 pv[4], pw[4];
      f32x2 f[8];
      OST(0); OST(1); OST(2);
      OAD(0); OST(3);
      OAD(1); OCV(pv, 0, 0); OST(4);
      OAD(2); OCV(pv, 1, 1); OST(5);
      OAD(3); OCV(pv, 2, 2); OST(6);
      OAD(4); OCV(pv, 3, 3); OST(7);
      __builtin_amdgcn_raw_buffer_store_b128((u32x4){pv[0], pv[1], pv[2], pv[3]}, ws, pst, parS, AUX); SCHED();
      OAD(5); OCV(pw, 0, 4); NOP(0); OCV(pw, 1, 5); NOP(0);
      OAD(6); NOP(0); OCV(pw, 2, 6); NOP(0);
      OAD(7); NOP(0); OCV(pw, 3, 7);
      asm volatile("" ::"v"(pv[0]), "v"(pv[1]), "v"(pv[2]), "v"(pv[3]));
      st16(ws, (u32x4){pw[0], pw[1], pw[2], pw[3]}, pst, parT);
    }
    if (w < 2) {
      u32x4 g[4], gl[4];
      NAP(D1);
      SCHED();
#pragma unroll
      for (int k = 0; k < 4; k++) { g[k] = ld16(ws, rgat, parS + k * GK); SCHED(); }
      NAP(D2);
      SCHED();
#pragma unroll
      for (int k = 0; k < 4; k++) { gl[k] = ld16(ws, rgat, parS + k * GK); SCHED(); }
      float tot;
#define REDUCE(G)                                                                                                    \
  {                                                                                                                  \
    f32x4 acc;                                                                                                       \
    MF0(acc, G[0], selB); MF1(acc, G[1], selB); MF1(acc, G[2], selB); MF1(acc, G[3], selB);                           \
    asm volatile("s_nop 7" : "+v"(acc));                                                                             \
    const f32x2 t_ = __builtin_shufflevector(acc, acc, 0, 1) + __builtin_shufflevector(acc, acc, 2, 3); tot = t_[0] + t_[1];                                                               \
  }
      REDUCE(g);
      if (__builtin_expect(anyl(tot != tot), 0)) {
        REDUCE(gl);
        int n = 0;
#pragma unroll 1
        while (anyl(tot != tot) && !dead) {
          MEMBAR();
#pragma unroll
          for (int k = 0; k < 4; k++) { g[k] = ld16(ws, rgat, parS + k * GK); SCHED(); }
          REDUCE(g);
          if (++n > SPIN_MAX) dead = true;
        }
      }
      const float hi = __builtin_bit_cast(float, __builtin_amdgcn_update_dpp(0, __builtin_bit_cast(int, tot), 0x104, 0xf, 0xf, true));
      st4(ws, pk(tot, hi), rpub4, pubS);
      NAP(D3);
      asm volatile("" ::"v"(gl[0]), "v"(gl[1]), "v"(gl[2]), "v"(gl[3]));
    } else {
      if (s < 62) {
        NAP(KVD);
        fetch_kv(s + 1);
      } else {
        WAITVM0();
      }
    }
    BAR();
  }
  WAITVM0();
  BAR();
  if (w == 2 && l == 0) st4(ws, tag, 0, C_KM + (u32)((h * 64 + 63) * 2 + a) * 4u);
  if (ci == 0 && w == 1 && l == 0) st4(ws, tag, 0, C_EP);
}

DI void stageW(const char* W, u32 off0, u32 rs, u32 ldsbuf, int w, int l) {
  const u32 bx = (u32)((l & 15) ^ ((l >> 4) << 1));
  const u32 ro = off0 + (u32)(64 * w + (l >> 4)) * rs;
#pragma unroll
  for (int it = 0; it < 16; it++)
    dma16(W, ro + (u32)(it * 4) * rs + ((bx ^ (u32)(((it >> 1) & 1) << 3)) << 4), ldsbuf + (w * 16 + it) * 1024);
}
template <bool AG>
DI void readW(bf16x8 (&B)[2][16], int half, lptr buf, int w, int l) {
  const int gb = l >> 4, q = (l >> 2) & 3, p = l & 3;
  const int f = ((gb & 1) << 2) | q;
#pragma unroll
  for (int y = 0; y < 2; y++) {
    lptr b = buf + (8 * gb + q) * 256 + (((4 * w + 2 * y + (p >> 1)) ^ (f << 1)) << 4) + (p & 1) * 8;
#pragma unroll
    for (int k = 0; k < 8; k++) {
      B[y][half * 8 + k] = cat2(trr(b + k * 8192), trr(b + k * 8192 + 1024));
    }
  }
}
DI void stageX(const char* x, int s, u32 ldsbuf, int w, int l) {
  const u32 ro = (u32)(32 * s + 16 * (w >> 1) + (l & 15)) * 1024u + (u32)((w & 1) * 8) * 64u + (u32)(l >> 4) * 16u;
#pragma unroll
  for (int it = 0; it < 8; it++) dma16(x, ro + it * 64u, ldsbuf + (w * 8 + it) * 1024);
}

template <bool K, bool V, bool Q>
DI void qkv_seg(lptr xb, rsrc_t ws, u32 so, int w, int l, const bf16x8 (&Bq)[2][16], const bf16x8 (&Bk)[2][16], const bf16x8 (&Bv)[2][16]) {
  const f32x4 z4 = {0.f, 0.f, 0.f, 0.f};
  f32x4 aq[2][2], ak[2][2], av[2][2];
#pragma unroll
  for (int i = 0; i < 4; i++) { aq[i >> 1][i & 1] = z4; ak[i >> 1][i & 1] = z4; av[i >> 1][i & 1] = z4; }
  bf16x8 n0 = *(LAS bf16x8*)(xb), n1 = *(LAS bf16x8*)(xb + 16384);
#pragma unroll
  for (int ks = 0; ks < 16; ks++) {
    bf16x8 x0 = n0, x1 = n1;
    if (ks < 15) { n0 = *(LAS bf16x8*)(xb + (ks + 1) * 1024); n1 = *(LAS bf16x8*)(xb + 16384 + (ks + 1) * 1024); }
    asm volatile("" : "+v"(x0), "+v"(x1));
#pragma unroll
    for (int y = 0; y < 2; y++) {
      if (K) { ak[0][y] = mfma32(Bk[y][ks], x0, ak[0][y]); ak[1][y] = mfma32(Bk[y][ks], x1, ak[1][y]); }
      if (V) { av[0][y] = mfma32(x0, Bv[y][ks], av[0][y]); av[1][y] = mfma32(x1, Bv[y][ks], av[1][y]); }
      if (Q) { aq[0][y] = mfma32(Bq[y][ks], x0, aq[0][y]); aq[1][y] = mfma32(Bq[y][ks], x1, aq[1][y]); }
    }
    if (K) asm volatile("" : "+a"(ak[0][0]), "+a"(ak[0][1]), "+a"(ak[1][0]), "+a"(ak[1][1]));
    if (V) asm volatile("" : "+a"(av[0][0]), "+a"(av[0][1]), "+a"(av[1][0]), "+a"(av[1][1]));
    if (Q) asm volatile("" : "+a"(aq[0][0]), "+a"(aq[0][1]), "+a"(aq[1][0]), "+a"(aq[1][1]));
  }
#pragma unroll
  for (int t = 0; t < 2; t++) {
    if (K) {
      u32x4 kk = {pk(ak[t][0][0], ak[t][0][1]), pk(ak[t][0][2], ak[t][0][3]), pk(ak[t][1][0], ak[t][1][1]), pk(ak[t][1][2], ak[t][1][3])};
      st16(ws, kk, (u32)l * 16u + (u32)w * 1024u, OFF_KX + so + t * 4096u);
    }
    if (V) {
      u32x4 vv = {pk(av[0][t][0], av[0][t][1]), pk(av[0][t][2], av[0][t][3]), pk(av[1][t][0], av[1][t][1]), pk(av[1][t][2], av[1][t][3])};
      st16(ws, vv, (u32)l * 16u + (u32)(2 * w + t) * 1024u, OFF_VX + so);
    }
    if (Q) {
      u32x4 qq = {pk(aq[t][0][0], aq[t][0][1]), pk(aq[t][0][2], aq[t][0][3]), pk(aq[t][1][0], aq[t][1][1]), pk(aq[t][1][2], aq[t][1][3])};
      st16(ws, qq, (u32)l * 16u + (u32)w * 1024u, OFF_QX + so + t * 4096u);
    }
  }
}

#define TQ 12
DI u32 qkv_role(lptr smem, rsrc_t ws, const char* wsb, int h, int j, int w, int l, const char* x, const char* Wq, const char* Wk, const char* Wv) {
  const u32 lds0 = (u32)(size_t)smem;
  bf16x8 Bq[2][16], Bk[2][16], Bv[2][16];
  const u32 ho = (u32)h * 256u, hh = 256u * 8192u;
  const u32 fl = (w == 0 && l == 0) ? 0u : OOB;
  stageW(Wk, ho, 8192u, lds0, w, l);
  stageW(Wk, ho + hh, 8192u, lds0 + 65536, w, l);
  SCHED();
  const u32 tag = gettag(wsb);
  WAITVM(16); BAR(); readW<true>(Bk, 0, smem, w, l); BAR(); stageX(x, j, lds0 + 131072, w, l); stageW(Wv, ho, 8192u, lds0, w, l);
  WAITVM(24); BAR();
  readW<true>(Bk, 1, smem + 65536, w, l); BAR(); stageW(Wv, ho + hh, 8192u, lds0 + 65536, w, l);
  WAITVM(32); BAR();
  qkv_seg<true, false, false>(smem + 131072 + l * 16, ws, (u32)(j * 32 + h) * 8192u, w, l, Bq, Bk, Bv);
  WAITVM(18); BAR(); readW<false>(Bv, 0, smem, w, l);
  WAITVM(2); BAR(); readW<false>(Bv, 1, smem + 65536, w, l);
  qkv_seg<false, true, false>(smem + 131072 + l * 16, ws, (u32)(j * 32 + h) * 8192u, w, l, Bq, Bk, Bv);
  BAR();
  stageW(Wq, ho, 8192u, lds0, w, l);
  WAITVM(16); BAR();
  st4(ws, tag + 1u, fl, C_KV + (u32)(h * 64 + j) * 4u);
  stageW(Wq, ho + hh, 8192u, lds0 + 65536, w, l);
  WAITVM(16); BAR(); readW<true>(Bq, 0, smem, w, l); BAR(); stageX(x, j + 6, lds0, w, l);
  WAITVM(8); BAR(); readW<true>(Bq, 1, smem + 65536, w, l);
  qkv_seg<false, false, true>(smem + 131072 + l * 16, ws, (u32)(j * 32 + h) * 8192u, w, l, Bq, Bk, Bv);
  WAITVM(0); BAR();
  st4(ws, tag + 2u, fl, C_KV + (u32)(h * 64 + j) * 4u);
  auto nxt = [&](int s, int& e) {
    if (e < 0 && s + 6 < (j < 4 ? 64 : TQ)) return s + 6;
    e = e < 0 ? j : e + 4;
    return (j < 4 && e < 20 - TQ / 3) ? TQ + 4 + 6 * (e >> 1) + (e & 1) : 64;
  };
  int buf = 0, e = -1;
  for (int s = j + 6; s < 64;) {
    const int sn = nxt(s, e);
    if (sn < 64) stageX(x, sn, lds0 + (buf ^ 1) * 32768, w, l);
    qkv_seg<true, true, true>(smem + buf * 32768 + l * 16, ws, (u32)(s * 32 + h) * 8192u, w, l, Bq, Bk, Bv);
    WAITVM(0); BAR();
    st4(ws, tag + 2u, fl, C_KV + (u32)(h * 64 + s) * 4u);
    buf ^= 1;
    s = sn;
  }
  return tag;
}

DI void ffn1_role(lptr smem, rsrc_t ws, u32 tag, int cg, int jj, int w, int l, const char* x, const char* w1, const bf16* b1, const bf16* gw) {
  const u32 lds0 = (u32)(size_t)smem;
  bf16x8 B[2][16], Gf[16];
  stageW(w1, (u32)cg * 256u, 3584u, lds0, w, l);
  stageW(w1, (u32)cg * 256u + 256u * 3584u, 3584u, lds0 + 65536, w, l);
  float bias[8];
#pragma unroll
  for (int i = 0; i < 8; i++) bias[i] = (float)b1[cg * 128 + 32 * w + 16 * (i >> 2) + 4 * (l >> 4) + (i & 3)];
#pragma unroll
  for (int ks = 0; ks < 16; ks++) Gf[ks] = *(const bf16x8*)(gw + 32 * ks + 8 * (l >> 4));
  WAITVM(0); BAR(); readW<true>(B, 0, smem, w, l); readW<true>(B, 1, smem + 65536, w, l); BAR();
  stageX(x, jj, lds0, w, l);
  WAITVM(0); BAR();
  const f32x4 z4 = {0.f, 0.f, 0.f, 0.f};
  const bool dog = (cg == 0 && w == 0);
  int buf = 0;
  for (int s = jj; s < 64; s += 9) {
    if (s + 9 < 64) stageX(x, s + 9, lds0 + (buf ^ 1) * 32768, w, l);
    lptr xb = smem + buf * 32768 + l * 16;
    f32x4 a[2][2] = {{z4, z4}, {z4, z4}};
#pragma unroll
    for (int ks = 0; ks < 16; ks++) {
      const bf16x8 x0 = *(LAS bf16x8*)(xb + ks * 1024), x1 = *(LAS bf16x8*)(xb + 16384 + ks * 1024);
#pragma unroll
      for (int y = 0; y < 2; y++) { a[0][y] = mfma32(B[y][ks], x0, a[0][y]); a[1][y] = mfma32(B[y][ks], x1, a[1][y]); }
    }
#pragma unroll
    for (int t = 0; t < 2; t++) {
      float r[8];
#pragma unroll
      for (int i = 0; i < 8; i++) {
        const float hv = rbf(rbf(a[t][i >> 2][i & 3]) + bias[i]);
        const float z = hv > 0.f ? hv : 0.f;
        r[i] = z * z;
      }
      u32x4 o = {pk(r[0], r[1]), pk(r[2], r[3]), pk(r[4], r[5]), pk(r[6], r[7])};
      st16(ws, o, (u32)l * 16u + (u32)(cg * 4 + w) * 1024u, OFF_HF + (u32)(s * 2 + t) * 57344u);
    }
    if (dog) {
      f32x4 g0 = z4, g1 = z4;
#pragma unroll
      for (int ks = 0; ks < 16; ks++) {
        g0 = mfma32(Gf[ks], *(LAS bf16x8*)(xb + ks * 1024), g0);
        g1 = mfma32(Gf[ks], *(LAS bf16x8*)(xb + 16384 + ks * 1024), g1);
      }
      const float zz = rbf(l < 16 ? g0[0] : g1[0]);
      const float gg = rbf(1.f / (1.f + __expf(-zz)));
      if (l < 32) st4(ws, __builtin_bit_cast(u32, gg), (u32)l * 4u, OFF_G + (u32)s * 128u);
    }
    WAITVM(0); BAR();
    if (w == 0 && l == 0) st4(ws, tag, 0, C_HC + (u32)s * 128u + (u32)cg * 4u);
    buf ^= 1;
  }
}

DI void attn_role(rsrc_t ws, u32 tag, int u, int l) {
  const int h = u & 31, gb = l >> 4, c = l & 15;
  bool dead = false;
  const f32x4 z4 = {0.f, 0.f, 0.f, 0.f};
  const u32x4 one8 = {0x3F803F80u, 0x3F803F80u, 0x3F803F80u, 0x3F803F80u};
  const u32x2 one4 = {0x3F803F80u, 0x3F803F80u};
  for (int s = u >> 5; s < 64; s += 4) {
    const u32 so = (u32)(s * 32 + h) * 8192u, sm = (u32)(s * 32 + h) * 4096u, lo = (u32)l * 16u;
    waitflag(ws, 0, C_KV + (u32)(h * 64 + s) * 4u, 1ull, ~0u, tag + 2u, dead, 2);
    u32x4 Q[2][4], K[2][4], V[8], KM[4];
    u32x2 VM[8];
#pragma unroll
    for (int i = 0; i < 8; i++) {
      Q[i >> 2][i & 3] = ld16(ws, lo + (i & 3) * 1024u, OFF_QX + so + (i >> 2) * 4096u);
      K[i >> 2][i & 3] = ld16(ws, lo + (i & 3) * 1024u, OFF_KX + so + (i >> 2) * 4096u);
      V[i] = ld16(ws, lo + (i & 3) * 1024u, OFF_VX + so + (i >> 2) * 4096u);
    }
    SCHED();
    f32x4 s00 = z4, s01 = z4, s11 = z4;
#pragma unroll
    for (int ks = 0; ks < 4; ks++) {
      s00 = mfma32(as8(K[0][ks]), as8(Q[0][ks]), s00);
      s01 = mfma32(as8(K[0][ks]), as8(Q[1][ks]), s01);
      s11 = mfma32(as8(K[1][ks]), as8(Q[1][ks]), s11);
    }
#pragma unroll
    for (int i = 0; i < 4; i++)
      if (4 * gb + i > c) { s00[i] = NEG; s11[i] = NEG; }
    waitflag(ws, (u32)(l & 1) * 4u, C_KM + (u32)((h * 64 + s) * 2) * 4u, 3ull, ~0u, tag, dead, s < 60 ? 2 : 0);
#pragma unroll
    for (int i = 0; i < 4; i++) KM[i] = ld16(ws, lo + i * 1024u, OFF_KM + sm);
#pragma unroll
    for (int i = 0; i < 8; i++) VM[i] = (u32x2){ld4(ws, (u32)l * 4u + i * 256u, OFF_VM + sm), 0u};
    SCHED();
    f32x4 m0 = z4, m1 = z4;
#pragma unroll
    for (int ks = 0; ks < 4; ks++) { m0 = mfma32(as8(KM[ks]), as8(Q[0][ks]), m0); m1 = mfma32(as8(KM[ks]), as8(Q[1][ks]), m1); }
#pragma unroll
    for (int t = 0; t < 2; t++) {
      const f32x4 sM = t ? m1 : m0, sa = t ? s01 : s00;
      float m = fmaxf(fmaxf(sM[0], sM[1]), fmaxf(fmaxf(sa[0], sa[1]), fmaxf(sa[2], sa[3])));
      if (t) m = fmaxf(m, fmaxf(fmaxf(s11[0], s11[1]), fmaxf(s11[2], s11[3])));
      m = maxall(m) * SCL;
      float pM[4], p0[4], p1[4];
#pragma unroll
      for (int i = 0; i < 4; i++) {
        pM[i] = i < 2 ? __builtin_amdgcn_exp2f(__builtin_fmaf(sM[i], SCL, -m)) : 0.f;
        p0[i] = __builtin_amdgcn_exp2f(__builtin_fmaf(sa[i], SCL, -m));
        p1[i] = t ? __builtin_amdgcn_exp2f(__builtin_fmaf(s11[i], SCL, -m)) : 0.f;
      }
      const u32x2 PM = {pk(pM[0], pM[1]), 0u};
      const u32x4 PX = {pk(p0[0], p0[1]), pk(p0[2], p0[3]), pk(p1[0], p1[1]), pk(p1[2], p1[3])};
      const float rs = __builtin_amdgcn_rcpf(mfma16(one4, PM, z4)[0] + mfma32(as8(one8), as8(PX), z4)[0]);
#pragma unroll
      for (int ks = 0; ks < 4; ks++) {
        f32x4 oa = mfma32(as8(V[2 * ks]), as8(PX), z4) + mfma16(VM[2 * ks], PM, z4);
        f32x4 ob = mfma32(as8(V[2 * ks + 1]), as8(PX), z4) + mfma16(VM[2 * ks + 1], PM, z4);
        u32x4 o = {pk(oa[0] * rs, oa[1] * rs), pk(oa[2] * rs, oa[3] * rs), pk(ob[0] * rs, ob[1] * rs), pk(ob[2] * rs, ob[3] * rs)};
        st16(ws, o, lo + ks * 1024u, OFF_OF + (u32)(s * 2 + t) * 131072u + (u32)h * 4096u);
      }
    }
    WAITVM(0);
    if (l == 0) st4(ws, tag, 0, C_OC + (u32)s * 128u + (u32)h * 4u);
  }
}

DI void out_role(lptr smem, rsrc_t ws, u32 tag, int grp, int xt, int sl, int w, int l, const char* Wo, const char* w2, const bf16* b2, bf16* out) {
  const u32 lds0 = (u32)(size_t)smem + (u32)w * 32768u;
  lptr wb = smem + w * 32768;
  const int gb = l >> 4, q = (l >> 2) & 3, p = l & 3, c = l & 15;
  bf16x8 A[2][32], A2[2][14];
  bool dead = false;
  {
    const u32 ro = (u32)(l >> 2) * 1024u + (u32)sl * 64u + (u32)(l & 3) * 16u;
    auto issue = [&](const char* W, int row0, int n, int buf) {
#pragma unroll
      for (int it = 0; it < 16; it++)
        if (it < n) dma16(W, ro + (u32)(row0 + it * 16) * 1024u, lds0 + buf * 16384 + it * 1024);
    };
    lptr b = wb + (4 * gb + q) * 64 + p * 16;
    issue(w2, w * 448, 16, 0);
    issue(w2, w * 448 + 256, 12, 1);
    WAITVM(12);
#pragma unroll
    for (int k = 0; k < 8; k++)
#pragma unroll
      for (int y = 0; y < 2; y++) { A2[y][k] = cat2(trr(b + k * 2048 + y * 8), trr(b + k * 2048 + 1024 + y * 8)); }
    asm volatile("s_waitcnt lgkmcnt(0)" ::: "memory");
    issue(Wo, w * 1024, 16, 0);
    WAITVM(16);
#pragma unroll
    for (int k = 0; k < 6; k++)
#pragma unroll
      for (int y = 0; y < 2; y++) { A2[y][8 + k] = cat2(trr(b + 16384 + k * 2048 + y * 8), trr(b + 16384 + k * 2048 + 1024 + y * 8)); }
#pragma unroll
    for (int r = 0; r < 4; r++) {
      asm volatile("s_waitcnt lgkmcnt(0)" ::: "memory");
      if (r < 3) { issue(Wo, w * 1024 + (r + 1) * 256, 16, (r + 1) & 1); WAITVM(16); } else { WAITVM(0); }
#pragma unroll
      for (int k = 0; k < 8; k++)
#pragma unroll
        for (int y = 0; y < 2; y++)
        { A[y][r * 8 + k] = cat2(trr(b + (r & 1) * 16384 + k * 2048 + y * 8), trr(b + (r & 1) * 16384 + k * 2048 + 1024 + y * 8)); }
    }
  }
  float bias[8];
#pragma unroll
  for (int i = 0; i < 8; i++) bias[i] = (float)b2[sl * 32 + 8 * gb + i];
#define LDO(v, s) ld16(ws, v, s)
  BAR();
  const f32x4 z4 = {0.f, 0.f, 0.f, 0.f};
  {
    int n = 0;
#pragma unroll 1
    while (!dead) {
      MEMBAR();
      u32 bad = 0;
#pragma unroll
      for (int k = 0; k < 14; k++) bad |= ld4(ws, (u32)l * 128u + k * 4u, C_HC) ^ tag;
      if (!anyl(bad != 0)) break;
      asm volatile("" : "+s"(n));
      if (++n > SPIN_MAX) dead = true;
      __builtin_amdgcn_s_sleep(16);
    }
    MEMBAR();
  }
  for (int si = grp; si < 65; si += 3) {
    const int s = si < 63 ? si : 63;
    const bool act = si < 63 || (c >> 3) == si - 63;
    const u32 lo = act ? (u32)l * 16u : OOB;
    f32x4 a0 = z4, a1 = z4, h0 = z4, h1 = z4;
    {
      const u32 so = OFF_HF + (u32)(s * 2 + xt) * 57344u + (u32)w * 14336u;
      u32x4 F[14];
#pragma unroll
      for (int k = 0; k < 14; k++) F[k] = LDO(lo, so + k * 1024u);
      SCHED();
#pragma unroll
      for (int k = 0; k < 14; k++) { h0 = mfma32(A2[0][k], as8(F[k]), h0); h1 = mfma32(A2[1][k], as8(F[k]), h1); }
    }
    const float g = __builtin_bit_cast(float, ld4(ws, (u32)c * 4u, OFF_G + (u32)(s * 2 + xt) * 64u));
    waitflag(ws, (u32)(l & 31) * 4u, C_OC + (u32)s * 128u, 0xFFFFFFFFull, ~0u, tag, dead, s < 61 ? 1 : 0);
    {
      const u32 so = OFF_OF + (u32)(s * 2 + xt) * 131072u + (u32)w * 32768u;
      u32x4 F[16];
#pragma unroll
      for (int k = 0; k < 16; k++) F[k] = LDO(lo, so + k * 1024u);
      SCHED();
#pragma unroll
      for (int k = 0; k < 32; k++) {
        a0 = mfma32(A[0][k], as8(F[k & 15]), a0); a1 = mfma32(A[1][k], as8(F[k & 15]), a1);
        if (k < 16) {
          asm volatile("" : "+a"(a0), "+a"(a1));
          F[k] = LDO(lo, so + 16384u + k * 1024u);
          SCHED();
        }
      }
    }
    lptr rb = smem + w * 4096 + l * 16;
    *(LAS f32x4*)(rb) = a0; *(LAS f32x4*)(rb + 1024) = a1; *(LAS f32x4*)(rb + 2048) = h0; *(LAS f32x4*)(rb + 3072) = h1;
    BAR();
    if (w == 0) {
#pragma unroll
      for (int k = 1; k < 4; k++) {
        a0 += *(LAS f32x4*)(rb + k * 4096); a1 += *(LAS f32x4*)(rb + k * 4096 + 1024);
        h0 += *(LAS f32x4*)(rb + k * 4096 + 2048); h1 += *(LAS f32x4*)(rb + k * 4096 + 3072);
      }
      const float g1 = rbf(1.f - g);
      float r[8];
#pragma unroll
      for (int i = 0; i < 8; i++) {
        const float av = rbf(i < 4 ? a0[i & 3] : a1[i & 3]);
        const float bv = rbf(rbf(i < 4 ? h0[i & 3] : h1[i & 3]) + bias[i]);
        r[i] = rbf(g * av) + rbf(g1 * bv);
      }
      u32x4 o = {pk(r[0], r[1]), pk(r[2], r[3]), pk(r[4], r[5]), pk(r[6], r[7])};
      if (act) *(u32x4*)(out + (size_t)(32 * s + 16 * xt + c) * 512 + sl * 32 + 8 * gb) = o;
    }
    BAR();
  }
}

DI void probe_role(lptr smem, rsrc_t ws, u32 tag, int w, int l) {
  const u32 base = ((w & 1) ? OFF_MEM : OFF_PART) + (u32)(w >> 1) * 131072u;
  u32 best[2] = {0xFFFFFFFFu, 0xFFFFFFFFu};
#pragma unroll 1
  for (int i = 0; i < 8; i++) {
#pragma unroll
    for (int sd = 0; sd < 2; sd++) {
      u64 t0 = __builtin_amdgcn_s_memtime();
      asm volatile("s_waitcnt lgkmcnt(0)" ::: "memory");
      st4(ws, 0xFFFFFFFFu, l == 0 ? 0u : OOB, base + (u32)i * 16384u + (u32)sd * 8192u);
      WAITVM(0);
      u64 t1 = __builtin_amdgcn_s_memtime();
      best[sd] = min(best[sd], (u32)(t1 - t0));
    }
  }
  if (l == 0) { *(LAS u32*)(smem + w * 8) = best[0]; *(LAS u32*)(smem + w * 8 + 4) = best[1]; }
  BAR();
  if (w == 0 && l == 0) {
    LAS u32* t = (LAS u32*)smem;
    const u32 a = min(t[0], t[4]), b = min(t[1], t[5]), c = min(t[2], t[6]), d = min(t[3], t[7]);
    st4(ws, tag | (b < a ? 1u : 0u) | (d < c ? 2u : 0u), 0, C_SEL);
  }
  BAR();
}

__global__ __launch_bounds__(256, 1) void fused_kernel(const char* x, const bf16* mem0, const char* Wq, const char* Wk, const char* Wv,
                                                      const char* Wo, const char* w1, const bf16* b1, const char* w2, const bf16* b2,
                                                      const bf16* gw, bf16* out, char* wsb) {
  extern __shared__ __attribute__((aligned(16))) char smem_[];
  lptr smem = (lptr)smem_;
  const int b = blockIdx.x, xq = b & 7, r = b >> 3;
  const int w = __builtin_amdgcn_readfirstlane(threadIdx.x >> 6), l = threadIdx.x & 63;
  const rsrc_t ws = __builtin_amdgcn_make_buffer_rsrc(wsb, 0, (int)WS_BYTES, 0x00020000);
  if (xq < 4 && r < 16) {
    chain_role(smem, ws, 8 * xq + (r >> 1), r & 1, w, l, Wq, Wk, Wv, Wo, mem0, wsb);
    return;
  }
  const int head = xq < 4 ? 8 * xq + ((r - 16) >> 1) : 8 * (xq - 4) + (r >> 2);
  const int j = xq < 4 ? 4 + (r & 1) : (r & 3);
  if (b == 31 * 8) probe_role(smem, ws, gettag(wsb), w, l);
  const u32 tag = qkv_role(smem, ws, wsb, head, j, w, l, x, Wq, Wk, Wv);
  const int wi = (xq - 4) * 32 + r;
  if (xq >= 4 && wi < 126) ffn1_role(smem, ws, tag, wi / 9, wi % 9, w, l, x, w1, b1, gw);
  if (xq >= 4) {
    if (r >= 24) {
      attn_role(ws, tag, ((xq - 4) * 8 + (r - 24)) * 4 + w, l);
    } else {
      const int g2 = (xq - 4) >> 1;
      out_role(smem, ws, tag, r < 16 ? g2 : 2, xq & 1, r < 16 ? r : r - 16 + 8 * g2, w, l, Wo, w2, b2, out);
    }
  }
}

extern "C" void kernel_launch(void* const* d_in, const int* in_sizes, int n_in, void* d_out, int out_size, void* d_ws,
                              size_t ws_size, hipStream_t stream) {
  char* base = (char*)(((size_t)d_ws + 2 * MB - 1) & ~(size_t)(2 * MB - 1));
  (void)hipFuncSetAttribute((const void*)fused_kernel, hipFuncAttributeMaxDynamicSharedMemorySize, LDS_BYTES);
  fused_kernel<<<256, 256, LDS_BYTES, stream>>>((const char*)d_in[0], (const bf16*)d_in[1], (const char*)d_in[2], (const char*)d_in[3],
                                                (const char*)d_in[4], (const char*)d_in[5], (const char*)d_in[8], (const bf16*)d_in[9],
                                                (const char*)d_in[10], (const bf16*)d_in[11], (const bf16*)d_in[12], (bf16*)d_out, base);
}
```

```cpp
#include <hip/hip_runtime.h>
#include <hip/hip_bf16.h>

typedef unsigned u32;
typedef unsigned long long u64;
typedef __bf16 bf16;
typedef bf16 bf16x2 __attribute__((ext_vector_type(2)));
typedef bf16 bf16x8 __attribute__((ext_vector_type(8)));
typedef bf16 bf16x16 __attribute__((ext_vector_type(16)));
typedef short s16x4 __attribute__((ext_vector_type(4)));
typedef short s16x8 __attribute__((ext_vector_type(8)));
typedef short s16x16 __attribute__((ext_vector_type(16)));
typedef float f32x2 __attribute__((ext_vector_type(2)));
typedef float f32x4 __attribute__((ext_vector_type(4)));
typedef u32 u32x2 __attribute__((ext_vector_type(2)));
typedef u32 u32x4 __attribute__((ext_vector_type(4)));
typedef __amdgpu_buffer_rsrc_t rsrc_t;
#define LAS __attribute__((address_space(3)))
typedef LAS char* lptr;
#define DI __device__ __forceinline__

constexpr u32 MB = 1u << 20;
constexpr u32 OFF_PART = 0;
constexpr u32 OFF_MEM = 94 * MB;
constexpr u32 OFF_CTRL = 96 * MB;
constexpr u32 C_SEL = OFF_CTRL;
constexpr u32 C_EP = OFF_CTRL + 256;
constexpr u32 C_RDY = OFF_CTRL + 512;
constexpr u32 C_KV = OFF_CTRL + 1024;
constexpr u32 C_KM = C_KV + 8192;
constexpr u32 C_OC = C_KM + 16384;
constexpr u32 C_HC = C_OC + 8192;
constexpr u32 C_END = C_HC + 8192;
constexpr u32 OFF_QX = 4 * MB, OFF_KX = 20 * MB, OFF_VX = 36 * MB;
constexpr u32 OFF_KM = 52 * MB, OFF_VM = 60 * MB, OFF_OF = 68 * MB, OFF_HF = 84 * MB, OFF_G = 92 * MB;
constexpr u32 WS_BYTES = 97 * MB;
DI u32 slot_off(int s) { const u32 r = (u32)(63 - s); return (r >> 1) * 65536u + (r & 1u) * 2048u; }
DI u32 tile_off(int t) { return (u32)(t >> 2) * 16384u + (u32)((t >> 1) & 1) * 4096u + (u32)(t & 1) * 1024u; }
constexpr int LDS_BYTES = 160 * 1024;
constexpr int SPIN_MAX = 1 << 15;
constexpr int AUX = 16;

DI u32x4 ld16(rsrc_t r, u32 v, u32 s) { return __builtin_amdgcn_raw_buffer_load_b128(r, v, s, AUX); }
DI u32x2 ld8(rsrc_t r, u32 v, u32 s) { return __builtin_amdgcn_raw_buffer_load_b64(r, v, s, AUX); }
DI u32 ld4(rsrc_t r, u32 v, u32 s) { return __builtin_amdgcn_raw_buffer_load_b32(r, v, s, AUX); }
DI void st16(rsrc_t r, u32x4 x, u32 v, u32 s) {
  __builtin_amdgcn_raw_buffer_store_b128(x, r, v, s, AUX);
  asm volatile("s_nop 1" ::"v"(x) : "memory");
}
DI void st8(rsrc_t r, u32x2 x, u32 v, u32 s) { __builtin_amdgcn_raw_buffer_store_b64(x, r, v, s, AUX); }
DI void st4(rsrc_t r, u32 x, u32 v, u32 s) { __builtin_amdgcn_raw_buffer_store_b32(x, r, v, s, AUX); }
#define MEMBAR() asm volatile("" ::: "memory")
#define WAITVM(n) asm volatile("s_waitcnt vmcnt(" #n ")" ::: "memory")
#define WAITVM0() do { MEMBAR(); __builtin_amdgcn_s_waitcnt(0x0F70); MEMBAR(); } while (0)
#define BAR() asm volatile("s_waitcnt lgkmcnt(0)\n\ts_barrier" ::: "memory")
#define SCHED() __builtin_amdgcn_sched_barrier(0)
#define PINA(x) asm volatile("" : "+a"(x))
#define PINV(x) asm volatile("" : "+v"(x))
constexpr u32 OOB = 0x80000000u;

DI void dma16(const char* sbase, u32 voff, u32 ldsdst) {
  u32 keep;
  asm volatile("s_mov_b32 %0, m0\n\ts_mov_b32 m0, %3\n\ts_nop 0\n\tglobal_load_lds_dwordx4 %1, %2\n\ts_mov_b32 m0, %0"
               : "=&s"(keep) : "v"(voff), "s"(sbase), "s"(ldsdst) : "memory");
}
DI s16x4 trr(lptr p) { return __builtin_amdgcn_ds_read_tr16_b64_v4i16((LAS s16x4*)p); }
DI bf16x8 cat2(s16x4 a, s16x4 b) { return __builtin_bit_cast(bf16x8, __builtin_shufflevector(a, b, 0, 1, 2, 3, 4, 5, 6, 7)); }
DI bf16x16 cat4(s16x4 a, s16x4 b, s16x4 c, s16x4 d) {
  s16x8 x = __builtin_shufflevector(a, b, 0, 1, 2, 3, 4, 5, 6, 7), y = __builtin_shufflevector(c, d, 0, 1, 2, 3, 4, 5, 6, 7);
  return __builtin_bit_cast(bf16x16, __builtin_shufflevector(x, y, 0, 1, 2, 3, 4, 5, 6, 7, 8, 9, 10, 11, 12, 13, 14, 15));
}
DI u32 pk(float lo, float hi) { f32x2 v = {lo, hi}; return __builtin_bit_cast(u32, __builtin_convertvector(v, bf16x2)); }
DI u32 pk2(f32x2 v) { return __builtin_bit_cast(u32, __builtin_convertvector(v, bf16x2)); }
DI float rbf(float x) { return (float)(bf16)x; }
DI bf16x8 as8(u32x4 x) { return __builtin_bit_cast(bf16x8, x); }
DI f32x4 mfma32(bf16x8 a, bf16x8 b, f32x4 c) { return __builtin_amdgcn_mfma_f32_16x16x32_bf16(a, b, c, 0, 0, 0); }
DI f32x4 mfma16(u32x2 a, u32x2 b, f32x4 c) {
  return __builtin_amdgcn_mfma_f32_16x16x16bf16_1k(__builtin_bit_cast(s16x4, a), __builtin_bit_cast(s16x4, b), c, 0, 0, 0);
}
DI f32x4 smf(u32x4 a, bf16x16 b, f32x4 c, int idx) { return __builtin_amdgcn_smfmac_f32_16x16x64_bf16(as8(a), b, c, idx, 0, 0); }
DI float comb(float x, float y) {
  auto r = __builtin_amdgcn_permlane32_swap(__builtin_bit_cast(u32, x), __builtin_bit_cast(u32, y), false, false);
  return __builtin_bit_cast(float, (u32)r[0]) + __builtin_bit_cast(float, (u32)r[1]);
}
DI float maxall(float m) {
  auto r = __builtin_amdgcn_permlane32_swap(__builtin_bit_cast(u32, m), __builtin_bit_cast(u32, m), false, false);
  m = fmaxf(__builtin_bit_cast(float, (u32)r[0]), __builtin_bit_cast(float, (u32)r[1]));
  auto q = __builtin_amdgcn_permlane16_swap(__builtin_bit_cast(u32, m), __builtin_bit_cast(u32, m), false, false);
  return fmaxf(__builtin_bit_cast(float, (u32)q[0]), __builtin_bit_cast(float, (u32)q[1]));
}
DI f32x4 zero4() {
  u64 a, b;
  asm volatile("v_mov_b64 %0, 0" : "=v"(a));
  asm volatile("v_mov_b64 %0, 0" : "=v"(b));
  typedef u64 u64x2 __attribute__((ext_vector_type(2)));
  u64x2 r = {a, b};
  return __builtin_bit_cast(f32x4, r);
}
DI u32 umax8(u32x4 a, u32x4 b) { return max(max(max(a[0], a[1]), max(a[2], a[3])), max(max(b[0], b[1]), max(b[2], b[3]))); }
DI bool anyl(bool p) { return __builtin_amdgcn_ballot_w64(p) != 0; }
DI void waitflag(rsrc_t r, u32 v, u32 s, u64 lanes, u32 msk, u32 val, bool& dead, int sleep) {
  int n = 0;
#pragma unroll 1
  while (!dead) {
    MEMBAR();
    const u32 x = ld4(r, v, s);
    if ((__builtin_amdgcn_ballot_w64((x & msk) != val) & lanes) == 0) break;
    asm volatile("" : "+s"(n));
    if (++n > SPIN_MAX) dead = true;
    for (int i = 0; i < sleep; i++) __builtin_amdgcn_s_sleep(4);
  }
  MEMBAR();
}
DI u32 gettag(const char* wsb) {
  u32 t = *(const __attribute__((address_space(4))) u32*)(wsb + C_EP);
  t = (t + 4u) & ~3u;
  asm volatile("" : "+s"(t));
  return t;
}
constexpr float NEG = -3.0e38f;
constexpr float SCL = 0.08838834764831845f * 1.4426950408889634f;

DI void chain_role(lptr smem, rsrc_t ws, int h, int a, int w, int l, const char* Wq, const char* Wk, const char* Wv,
                   const char* Wo, const bf16* mem0, const char* wsb) {
  const int gb = l >> 4, q = (l >> 2) & 3, p = l & 3, c = l & 15;
  const u32 lds0 = (u32)(size_t)smem;
  bf16x16 Bq[2][8], Bk[2][8], Bv[8], Bo[8];
  bool dead = false;
  const int ci = 2 * h + a;
  const int rho = 2 * ci + (w & 1);
  const int rt = rho >> 4, gs = (rho >> 2) & 3, ru = rho & 3;
  const u32 rpiece = tile_off(rt) + (u32)ru * 256u + (u32)gs * 64u;
  const u32 rpub4 = c < 4 ? rpiece + (u32)(c & 3) * 16u + (u32)gb * 4u : OOB;
  const u32 rblk0 = (u32)(rho >> 1) * 16384u + (u32)(rho & 1) * 4096u;
  const u32 lane0 = l == 0 ? 0u : OOB;
  const u32x4 ones = {~0u, ~0u, ~0u, ~0u};
  u32 selw, rdy, flg, tag;
  {
    const u32 cp = (u32)(w >> 1) * 8192u;
    const u32 bx = (u32)((l & 15) ^ ((l >> 5) << 1));
    const u32 rowoff = (u32)(64 * w + (l >> 4)) * 8192u + (u32)h * 256u;
    auto issueQK = [&](const char* W, int half, int buf) {
#pragma unroll
      for (int it = 0; it < 16; it++) {
        const u32 cit = (u32)((((it >> 3) & 1) << 3) | (((it >> 2) & 1) << 2));
        dma16(W, rowoff + (u32)(half * 256 + it * 4) * 8192u + ((bx ^ cit) << 4), lds0 + buf * 65536 + (w * 16 + it) * 1024);
      }
    };
    auto issueV = [&](int buf) {
      const u32 bv = (u32)((l & 7) ^ ((l >> 5) << 1));
      const u32 ro = (u32)(128 * w + ((l >> 3) & 1) * 32 + (l >> 4)) * 8192u + (u32)h * 256u + (u32)a * 128u;
#pragma unroll
      for (int it = 0; it < 16; it++)
        dma16(Wv, ro + (u32)((it >> 3) * 64 + (it & 7) * 4) * 8192u + ((bv ^ (u32)(((it >> 2) & 1) << 2)) << 4),
              lds0 + buf * 65536 + (w * 16 + it) * 1024);
    };
    auto issueO = [&](int buf) {
      const u32 ro = (u32)(h * 128 + a * 64 + w * 16) * 1024u;
#pragma unroll
      for (int it = 0; it < 16; it++) {
        const u32 f = (u32)((((it >> 3) & 1) << 2) | (((it >> 2) & 1) << 1) | (it & 1));
        dma16(Wo, ro + (u32)it * 1024u + (((u32)l ^ (f << 1)) << 4), lds0 + buf * 65536 + (w * 16 + it) * 1024);
      }
    };
    const int rowl = 32 * (q & 1) + 16 * (q >> 1) + 2 * gb;
    const int fq = ((q & 1) << 2) | ((q >> 1) << 1) | (gb & 1);
    auto readQK = [&](bf16x16(&B)[2][8], int half, int buf) {
#pragma unroll
      for (int y = 0; y < 2; y++) {
        lptr b = smem + buf * 65536 + rowl * 256 + (((4 * w + 2 * y + (p >> 1)) ^ (fq << 1)) << 4) + (p & 1) * 8;
#pragma unroll
        for (int t = 0; t < 4; t++)
        {
          lptr bt = b + (t ^ w) * 16384;
          B[y][half * 4 + t] = cat4(trr(bt), trr(bt + 256), trr(bt + 2048), trr(bt + 2304));
          PINA(B[y][half * 4 + t]);
        }
      }
    };
    dma16((const char*)mem0, (u32)(w * 2048 + l * 16), lds0 + 135168u + (u32)w * 2048u);
    dma16((const char*)mem0, (u32)(w * 2048 + 1024 + l * 16), lds0 + 136192u + (u32)w * 2048u);
    issueQK(Wq, 0, 0);
    issueQK(Wq, 1, 1);
    SCHED();
#pragma unroll
    for (int k = 0; k < 8; k++) st16(ws, ones, rblk0 + (u32)l * 16u + (k & 3) * 1024u, OFF_PART + (k >> 2) * MB + cp);
#pragma unroll
    for (int k = 0; k < 4; k++) {
      const int s = 1 + k * 16 + (l >> 2);
      st16(ws, ones, s < 64 ? slot_off(s) + rpiece + (u32)(l & 3) * 16u : OOB, OFF_MEM + cp);
    }
    SCHED();
    tag = gettag(wsb);
    WAITVM(28); BAR(); readQK(Bq, 0, 0);
    {
      const int d0 = 64 * rt + 16 * ((c >> 1) & 1) + 4 * ru + gb, r0 = 2 * gs + (c & 1);
      LAS unsigned short* m0 = (LAS unsigned short*)(smem + 135168) + r0 * 512 + d0;
      const u32 mv = (u32)m0[0] | ((u32)m0[32] << 16);
      BAR();
      st4(ws, mv, rpub4, OFF_MEM + cp + slot_off(0));
      SCHED();
    }
    issueQK(Wk, 0, 0);
    WAITVM(29); BAR(); readQK(Bq, 1, 1); BAR(); issueQK(Wk, 1, 1);
    WAITVM(16); BAR();
    st4(ws, tag, w == 0 ? lane0 : OOB, C_RDY + (u32)ci * 4u);
    SCHED();
    readQK(Bk, 0, 0); BAR(); issueV(0);
    WAITVM(16); BAR(); readQK(Bk, 1, 1); BAR(); issueO(1);
    WAITVM(16); BAR();
    {
      const int f2 = ((q >> 1) << 1) | (gb & 1);
      lptr b = smem + (16 * (q >> 1) + 2 * gb) * 256 + (q & 1) * 128 + (((2 * w + (p >> 1)) ^ (f2 << 1)) << 4) + (p & 1) * 8;
#pragma unroll
      for (int t = 0; t < 8; t++)
      {
        lptr bt = b + (t ^ w) * 8192;
        Bv[t] = cat4(trr(bt), trr(bt + 256), trr(bt + 2048), trr(bt + 2304));
        PINV(Bv[t]);
      }
    }
    SCHED();
    selw = ld4(ws, 0, C_SEL); rdy = ld4(ws, (u32)l * 4u, C_RDY); flg = ld4(ws, 0, C_KV + (u32)(h * 64) * 4u);
    SCHED();
    WAITVM0(); BAR();
    {
      const int fo = ((q >> 1) << 2) | ((gb & 1) << 1) | (q & 1);
      const int er = 16 * (gb >> 1) + 8 * (q >> 1) + 4 * (gb & 1) + (q & 1);
#pragma unroll
      for (int T = 0; T < 8; T++) {
        lptr b = smem + 65536 + er * 1024 + (((16 * w + 2 * T + (p >> 1)) ^ (fo << 1)) << 4) + (p & 1) * 8;
        Bo[T] = cat4(trr(b), trr(b + 2048), trr(b + 32768), trr(b + 32768 + 2048));
        PINV(Bo[T]);
      }
    }
    BAR();
  }
  if (anyl(rdy != tag)) waitflag(ws, (u32)l * 4u, C_RDY, ~0ull, ~0u, tag, dead, 1);
  if (anyl((selw & ~3u) != tag)) { waitflag(ws, 0, C_SEL, 1ull, ~3u, tag, dead, 1); selw = ld4(ws, 0, C_SEL); }
  selw = __builtin_amdgcn_readfirstlane(selw);
  flg = __builtin_amdgcn_readfirstlane(flg);
  const u32 sel0 = (selw & 1u) * 8192u, sel1 = ((selw >> 1) & 1u) * 8192u;
  const u32 rgat = rblk0 + sel0 + (u32)(4 * gb + (c & 3)) * 64u + (u32)(c >> 2) * 16u;
  constexpr u32 GK = 1024u;
  const u32 pst = (u32)(w * 16 + (l >> 3)) * 16384u + (u32)((l >> 2) & 1) * 4096u + sel0 + (u32)ci * 64u + (u32)(l & 3) * 16u;
  const u32 kvlane = (u32)l * 16u;
  lptr Lq = smem, Lk = smem + 6144, Lx = smem + 12288, Lv = smem + 20480, La = smem + 24576, Lo = smem + 49152;
#pragma unroll
  for (int i = 0; i < 3; i++) *(LAS u32x4*)(smem + i * 4096 + w * 1024 + l * 16) = (u32x4){0u, 0u, 0u, 0u};
  auto fetch_kv = [&](int sn) {
    if ((flg & ~3u) != tag) waitflag(ws, 0, C_KV + (u32)(h * 64 + sn) * 4u, 1ull, ~3u, tag, dead, 1);
    const u32 so = (u32)(sn * 32 + h) * 8192u;
    u32x4 t[6];
    const u32 fl = ld4(ws, 0, C_KV + (u32)(h * 64 + sn + 1) * 4u);
    SCHED();
#pragma unroll
    for (int i = 0; i < 4; i++) t[i] = ld16(ws, kvlane, OFF_KX + so + (u32)(w - 2) * 4096u + i * 1024u);
#pragma unroll
    for (int i = 0; i < 2; i++) t[4 + i] = ld16(ws, kvlane, OFF_VX + so + (u32)(4 * a + 2 * (w - 2) + i) * 1024u);
    SCHED();
    flg = __builtin_amdgcn_readfirstlane(fl);
#pragma unroll
    for (int i = 0; i < 4; i++) *(LAS u32x4*)(Lx + (w - 2) * 4096 + i * 1024 + l * 16) = t[i];
#pragma unroll
    for (int i = 0; i < 2; i++) *(LAS u32x4*)(Lv + (w - 2) * 2048 + i * 1024 + l * 16) = t[4 + i];
  };
  if (w >= 2) fetch_kv(0);
  WAITVM0();
  BAR();
  const u32 kmoff = (l >> 5) == a ? kvlane + (u32)w * 1024u : OOB;
  const u32 flgoff = w == 2 ? lane0 : OOB;
  const int idx = (l & 2) ? 0xEEEE : 0x4444;
  const int wq = (4 * w + (c >> 2)) * 384 + (c & 3) * 32 + gb * 8;
  const int rq = (4 * w + gb) * 384 + q * 32 + p * 8;
  const bool up = (c & 2) != 0;
  u32 pmask = up ? 0xFFFF0000u : 0x0000FFFFu;
  u32 tagv = tag;
  PINV(pmask); PINV(tagv);
  lptr Ls = smem + 53248;
  lptr Lar[4] = {La, La + (w ^ 1) * 1024 + l * 16, La + (w ^ 2) * 1024 + l * 16, La + (w ^ 3) * 1024 + l * 16};
  lptr Lkw = Lk + wq, Lsw = Ls + 4096 + w * 256 + l * 4;
  PINV(Lar[1]); PINV(Lar[2]); PINV(Lar[3]); PINV(Lkw); PINV(Lsw);
  const int orow = 2 * (c >> 2) + (c & 1);
  u32x4 selB;
  {
    const int j = c & 7;
    const u32 one = (j & 1) ? 0x3F800000u : 0x00003F80u;
    selB[0] = (j >> 1) == 0 ? one : 0u; selB[1] = (j >> 1) == 1 ? one : 0u;
    selB[2] = (j >> 1) == 2 ? one : 0u; selB[3] = (j >> 1) == 3 ? one : 0u;
  }
  u32x4 one8 = {0x3F803F80u, 0x3F803F80u, 0x3F803F80u, 0x3F803F80u};
  PINV(one8); PINV(selB);
#define SM2(d0, d1, B, t)                                                                                            \
  asm volatile("v_smfmac_f32_16x16x64_bf16 %0, %2, %3, %5\n\tv_smfmac_f32_16x16x64_bf16 %1, %2, %4, %5"              \
               : "+v"(d0), "+v"(d1) : "v"(A[t]), "a"(B[0][t]), "a"(B[1][t]), "v"(idx));                              \
  SCHED()
#define VSTEP(t)                                                                                                     \
  asm volatile("v_smfmac_f32_16x16x64_bf16 %0, %1, %2, %3" : "+v"(v0) : "v"(A[t]), "v"(Bv[t]), "v"(idx));            \
  SCHED()
#define MF0(D, F, G) asm volatile("v_mfma_f32_16x16x32_bf16 %0, %1, %2, 0" : "=&v"(D) : "v"(F), "v"(G)); SCHED()
#define MF1(D, F, G) asm volatile("v_mfma_f32_16x16x32_bf16 %0, %1, %2, %0" : "+v"(D) : "v"(F), "v"(G)); SCHED()
#define FOLD(D) pk2(__builtin_shufflevector(D, D, 0, 1) + __builtin_shufflevector(D, D, 2, 3))
#define UNDEF(x) asm volatile("" : "=v"(x))
#define NAP(n) do { if (n) __builtin_amdgcn_s_sleep(n); } while (0)
#define KVD 0
#define D1 10
#define D2 8
#define D3 10

  for (int s = 0; s < 64; s++) {
    const u32 mslot = OFF_MEM + sel1 + slot_off(s) + tile_off(w);
    u32x4 A[8];
    u32x4 g0 = ld16(ws, kvlane, mslot); SCHED();
    u32x4 g1 = ld16(ws, kvlane, mslot + 16384u); SCHED();
    f32x4 q0 = zero4(), q1 = zero4(), k0 = zero4(), k1 = zero4(), v0 = zero4();
    u32 parS = OFF_PART + (u32)(s & 1) * MB, parT = parS + 8 * 16384u, rstS = parS ^ MB, rstT = parT ^ MB;
    u32 kmS = OFF_KM + (u32)(s * 32 + h) * 4096u, vmS = kmS + (OFF_VM - OFF_KM), pubS = OFF_MEM + sel1 + slot_off(s + 1);
    u32 flS = C_KM - 8u + (u32)(h * 128 + a) * 4u + (u32)s * 8u, flV = s > 0 ? flgoff : OOB;
    asm volatile("" : "+s"(parS), "+s"(parT), "+s"(rstS), "+s"(rstT), "+s"(kmS), "+s"(vmS), "+s"(pubS), "+s"(flS), "+v"(flV));
    SCHED();
    *(LAS u32x4*)(La + w * 1024 + l * 16) = g0;
    *(LAS u32x4*)(La + w * 1024 + 4096 + l * 16) = g1;
    SCHED();
    if (__builtin_expect(anyl(umax8(g0, g1) == 0xFFFFFFFFu), 0)) {
      int n = 0;
      asm volatile("" : "+v"(g0), "+v"(g1));
#pragma unroll 1
      while (anyl(umax8(g0, g1) == 0xFFFFFFFFu) && !dead) {
        MEMBAR();
        g0 = ld16(ws, kvlane, mslot);
        g1 = ld16(ws, kvlane, mslot + 16384u);
        WAITVM0();
        if (++n > SPIN_MAX) dead = true;
      }
      *(LAS u32x4*)(La + w * 1024 + l * 16) = g0;
      *(LAS u32x4*)(La + w * 1024 + 4096 + l * 16) = g1;
    }
    BAR();
    A[0] = g0; A[4] = g1;
#define RDA(t) A[t] = *(LAS u32x4*)(Lar[t & 3] + (t & 4) * 1024); SCHED()
    RDA(1); RDA(2);
    SM2(q0, q1, Bq, 0);
    RDA(3);
    SM2(q0, q1, Bq, 4);
    RDA(5);
    SM2(k0, k1, Bk, 0);
    RDA(6);
    SM2(k0, k1, Bk, 4);
    RDA(7);
    SM2(q0, q1, Bq, 1);
    __builtin_amdgcn_raw_buffer_store_b128(ones, ws, pst, rstS, AUX); SCHED();
    SM2(q0, q1, Bq, 2);
    __builtin_amdgcn_raw_buffer_store_b128(ones, ws, pst, rstT, AUX); SCHED();
    SM2(q0, q1, Bq, 3);
    st4(ws, tagv, flV, flS); SCHED();
    SM2(q0, q1, Bq, 5);
    const u32x4 X0 = *(LAS u32x4*)(Lx + w * 1024 + l * 16), X1 = *(LAS u32x4*)(Lx + 4096 + w * 1024 + l * 16);
    SCHED();
    SM2(q0, q1, Bq, 6); SM2(q0, q1, Bq, 7);
    const u32x2 vxa = *(LAS u32x2*)(Lv + w * 1024 + l * 16);
    asm volatile("" ::: "memory");
    const u32x2 vxb = *(LAS u32x2*)(Lv + w * 1024 + l * 16 + 8);
    SCHED();
    SM2(k0, k1, Bk, 1);
    asm volatile("" : "+v"(q0), "+v"(q1));
    {
      const u32 a0 = FOLD(q0), a1 = FOLD(q1);
      *(LAS u32*)(Lq + wq) = a0; *(LAS u32*)(Lq + wq + 4) = a0; SCHED();
      SM2(k0, k1, Bk, 2);
      *(LAS u32*)(Lq + wq + 128) = a1; *(LAS u32*)(Lq + wq + 132) = a1; SCHED();
    }
    SM2(k0, k1, Bk, 3);
    const bf16x8 Fq = cat2(trr(Lq + rq), trr(Lq + rq + 128));
    SCHED();
    f32x4 D[8];
    SM2(k0, k1, Bk, 5);
    u32x4 VA = {0u, 0u, vxa[0], vxa[1]}, VB = {0u, 0u, vxb[0], vxb[1]};
    PINV(VA); PINV(VB);
    SCHED();
    SM2(k0, k1, Bk, 6);
    D[0] = zero4(); D[1] = zero4();
    SCHED();
    SM2(k0, k1, Bk, 7);
    f32x4 sM, s0, s1;
    MF0(s0, X0, Fq);
    MF0(s1, X1, Fq);
    VSTEP(0);
    asm volatile("" : "+v"(k0), "+v"(k1));
    { const u32 a0 = FOLD(k0), a1 = FOLD(k1); *(LAS u32*)(Lkw) = a0; *(LAS u32*)(Lkw + 128) = a1; SCHED(); }
    const bf16x8 Fk = cat2(trr(Lk + rq), trr(Lk + rq + 128));
    SCHED();
    VSTEP(1);
    asm volatile("" : "+v"(s0), "+v"(s1));
    {
      const f32x4 x = {up ? s1[0] : s0[0], up ? s1[1] : s0[1], up ? s1[2] : s0[2], up ? s1[3] : s0[3]};
      SCHED();
      VSTEP(2);
      *(LAS f32x4*)(Ls + w * 1024 + l * 16) = x;
      SCHED();
    }
    VSTEP(3);
    D[2] = zero4(); SCHED();
    VSTEP(4);
    D[3] = zero4(); SCHED();
    VSTEP(5);
    MF0(sM, Fk, Fq);
    VSTEP(6); VSTEP(7);
    st16(ws, __builtin_bit_cast(u32x4, Fk), kmoff, kmS);
    asm volatile("" : "+v"(sM));
    *(LAS float*)(Lsw) = up ? sM[1] : sM[0];
    SCHED();
    BAR();
    f32x4 xs[4];
    float ms[4];
#pragma unroll
    for (int j = 0; j < 4; j++) ms[j] = *(LAS float*)(Ls + 4096 + j * 256 + l * 4);
    SCHED();
#pragma unroll
    for (int j = 0; j < 4; j++) { xs[j] = *(LAS f32x4*)(Ls + j * 1024 + l * 16); SCHED(); }
    asm volatile("" : "+v"(v0));
    const u32 vm = FOLD(v0);
    st4(ws, vm, (u32)l * 4u + (u32)(4 * a + w) * 256u, vmS);
    if (s == 63) break;
    VA[0] = vm; VB[0] = vm;
    PINV(VA); PINV(VB);
    SCHED();
    float sm = ms[0] + ms[1], sn = ms[2] + ms[3];
    PINV(sm); PINV(sn);
    sm += sn;
    PINV(sm);
    SCHED();
    const f32x4 sx = (xs[0] + xs[1]) + (xs[2] + xs[3]);
    float m, mt;
    asm volatile("v_max3_f32 %0, %2, %3, %6\n\tv_max3_f32 %0, %0, %4, %5\n\ts_nop 1\n\t"
                 "v_max_f32_dpp %1, %0, %0 quad_perm:[2,3,0,1] row_mask:0xf bank_mask:0xf\n\t"
                 "v_max_f32_dpp %0, %0, %0 quad_perm:[2,3,0,1] row_mask:0xf bank_mask:0xf\n\ts_nop 1\n\t"
                 "v_permlane32_swap_b32 %0, %1\n\tv_max_f32 %0, %0, %1\n\tv_mov_b32 %1, %0\n\ts_nop 1\n\t"
                 "v_permlane16_swap_b32 %0, %1\n\tv_max_f32 %0, %0, %1"
                 : "=&v"(m), "=&v"(mt) : "v"(sx[0]), "v"(sx[1]), "v"(sx[2]), "v"(sx[3]), "v"(sm));
    m *= -SCL;
    const f32x2 m2 = {m, m}, scl2 = {SCL, SCL};
#define EXP(t) pk(__builtin_amdgcn_exp2f(t[0]), __builtin_amdgcn_exp2f(t[1]))
    float tM;
    asm volatile("v_fma_f32 %0, %1, %2, %3" : "=v"(tM) : "v"(sm), "s"(SCL), "v"(m));
    const f32x2 t0 = __builtin_elementwise_fma(__builtin_shufflevector(sx, sx, 0, 1), scl2, m2),
                t1 = __builtin_elementwise_fma(__builtin_shufflevector(sx, sx, 2, 3), scl2, m2);
    SCHED();
    const float eM = __builtin_amdgcn_exp2f(tM);
    const u32x4 P = {pk(eM, eM) & pmask, 0u, EXP(t0), EXP(t1)};
    f32x4 sum, oa, ob;
    asm volatile("s_nop 1\n\tv_mfma_f32_16x16x32_bf16 %0, %3, %4, 0\n\tv_mfma_f32_16x16x32_bf16 %2, %6, %4, 0\n\t"
                 "v_mfma_f32_16x16x32_bf16 %1, %5, %4, 0"
                 : "=&v"(sum), "=&v"(oa), "=&v"(ob) : "v"(one8), "v"(P), "v"(VA), "v"(VB));
    {
      float rs;
      f32x4 o;
      asm volatile("v_add_f32_dpp %0, %1, %1 quad_perm:[2,3,0,1] row_mask:0xf bank_mask:0xf\n\ts_nop 0\n\tv_rcp_f32 %0, %0" : "=&v"(rs) : "v"(sum[0]));
      D[4] = zero4(); D[5] = zero4(); D[6] = zero4();
      asm volatile("v_add_f32_dpp %0, %4, %8 quad_perm:[2,3,0,1] row_mask:0xf bank_mask:0xf\n\t"
                   "v_add_f32_dpp %1, %5, %9 quad_perm:[2,3,0,1] row_mask:0xf bank_mask:0xf\n\t"
                   "v_add_f32_dpp %2, %6, %10 quad_perm:[2,3,0,1] row_mask:0xf bank_mask:0xf\n\t"
                   "v_add_f32_dpp %3, %7, %11 quad_perm:[2,3,0,1] row_mask:0xf bank_mask:0xf"
                   : "=&v"(o[0]), "=&v"(o[1]), "=&v"(o[2]), "=&v"(o[3])
                   : "v"(ob[0]), "v"(ob[1]), "v"(ob[2]), "v"(ob[3]), "v"(oa[0]), "v"(oa[1]), "v"(oa[2]), "v"(oa[3]));
      const f32x2 r2 = {rs, rs};
      u32x2 oo = {pk2(__builtin_shufflevector(o, o, 0, 1) * r2), pk2(__builtin_shufflevector(o, o, 2, 3) * r2)};
      *(LAS u32x2*)(Lo + (orow + ((c & 2) << 2)) * 144 + w * 32 + gb * 8) = oo;
    }
    WAITVM(2);
    BAR();
    const u32x4 Ao = *(LAS u32x4*)(Lo + orow * 144 + gb * 32 + ((c >> 1) & 1) * 16);
    SCHED();
    D[7] = zero4();
    SCHED();
#define OST(T) asm volatile("v_smfmac_f32_16x16x64_bf16 %0, %1, %2, %3" : "+v"(D[T]) : "v"(Ao), "v"(Bo[T]), "v"(idx)); SCHED()
#define OAD(T) asm volatile("v_pk_add_f32 %0, %1, %2" : "=v"(f[T]) : "v"(__builtin_shufflevector(D[T], D[T], 0, 1)), "v"(__builtin_shufflevector(D[T], D[T], 2, 3))); SCHED()
#define OCV(P_, i, T) asm volatile("v_cvt_pk_bf16_f32 %0, %1, %2" : "=v"(P_[i]) : "v"(f[T][0]), "v"(f[T][1])); SCHED()
#define NOP(n) asm volatile("s_nop " #n); SCHED()
    {
      u32 pv[4], pw[4];
      f32x2 f[8];
      OST(0); OST(1); OST(2);
      OAD(0); OST(3);
      OAD(1); OCV(pv, 0, 0); OST(4);
      OAD(2); OCV(pv, 1, 1); OST(5);
      OAD(3); OCV(pv, 2, 2); OST(6);
      OAD(4); OCV(pv, 3, 3); OST(7);
      __builtin_amdgcn_raw_buffer_store_b128((u32x4){pv[0], pv[1], pv[2], pv[3]}, ws, pst, parS, AUX); SCHED();
      OAD(5); OCV(pw, 0, 4); NOP(0); OCV(pw, 1, 5); NOP(0);
      OAD(6); NOP(0); OCV(pw, 2, 6); NOP(0);
      OAD(7); NOP(0); OCV(pw, 3, 7);
      asm volatile("" ::"v"(pv[0]), "v"(pv[1]), "v"(pv[2]), "v"(pv[3]));
      st16(ws, (u32x4){pw[0], pw[1], pw[2], pw[3]}, pst, parT);
    }
    if (w < 2) {
      u32x4 g[4], gl[4];
      NAP(D1);
      SCHED();
#pragma unroll
      for (int k = 0; k < 4; k++) { g[k] = ld16(ws, rgat, parS + k * GK); SCHED(); }
      NAP(D2);
      SCHED();
#pragma unroll
      for (int k = 0; k < 4; k++) { gl[k] = ld16(ws, rgat, parS + k * GK); SCHED(); }
      float tot;
#define REDUCE(G)                                                                                                    \
  {                                                                                                                  \
    f32x4 acc;                                                                                                       \
    MF0(acc, G[0], selB); MF1(acc, G[1], selB); MF1(acc, G[2], selB); MF1(acc, G[3], selB);                           \
    asm volatile("s_nop 7" : "+v"(acc));                                                                             \
    const f32x2 t_ = __builtin_shufflevector(acc, acc, 0, 1) + __builtin_shufflevector(acc, acc, 2, 3); tot = t_[0] + t_[1];                                                               \
  }
      REDUCE(g);
      if (__builtin_expect(anyl(tot != tot), 0)) {
        REDUCE(gl);
        int n = 0;
#pragma unroll 1
        while (anyl(tot != tot) && !dead) {
          MEMBAR();
#pragma unroll
          for (int k = 0; k < 4; k++) { g[k] = ld16(ws, rgat, parS + k * GK); SCHED(); }
          REDUCE(g);
          if (++n > SPIN_MAX) dead = true;
        }
      }
      const float hi = __builtin_bit_cast(float, __builtin_amdgcn_update_dpp(0, __builtin_bit_cast(int, tot), 0x104, 0xf, 0xf, true));
      st4(ws, pk(tot, hi), rpub4, pubS);
      NAP(D3);
      asm volatile("" ::"v"(gl[0]), "v"(gl[1]), "v"(gl[2]), "v"(gl[3]));
    } else {
      if (s < 62) {
        NAP(KVD);
        fetch_kv(s + 1);
      } else {
        WAITVM0();
      }
    }
    BAR();
  }
  WAITVM0();
  BAR();
  if (w == 2 && l == 0) st4(ws, tag, 0, C_KM + (u32)((h * 64 + 63) * 2 + a) * 4u);
  if (ci == 0 && w == 1 && l == 0) st4(ws, tag, 0, C_EP);
}

DI void stageW(const char* W, u32 off0, u32 rs, u32 ldsbuf, int w, int l) {
  const u32 bx = (u32)((l & 15) ^ ((l >> 4) << 1));
  const u32 ro = off0 + (u32)(64 * w + (l >> 4)) * rs;
#pragma unroll
  for (int it = 0; it < 16; it++)
    dma16(W, ro + (u32)(it * 4) * rs + ((bx ^ (u32)(((it >> 1) & 1) << 3)) << 4), ldsbuf + (w * 16 + it) * 1024);
}
template <bool AG>
DI void readW(bf16x8 (&B)[2][16], int half, lptr buf, int w, int l) {
  const int gb = l >> 4, q = (l >> 2) & 3, p = l & 3;
  const int f = ((gb & 1) << 2) | q;
#pragma unroll
  for (int y = 0; y < 2; y++) {
    lptr b = buf + (8 * gb + q) * 256 + (((4 * w + 2 * y + (p >> 1)) ^ (f << 1)) << 4) + (p & 1) * 8;
#pragma unroll
    for (int k = 0; k < 8; k++) {
      B[y][half * 8 + k] = cat2(trr(b + k * 8192), trr(b + k * 8192 + 1024));
    }
  }
}
DI void stageX(const char* x, int s, u32 ldsbuf, int w, int l) {
  const u32 ro = (u32)(32 * s + 16 * (w >> 1) + (l & 15)) * 1024u + (u32)((w & 1) * 8) * 64u + (u32)(l >> 4) * 16u;
#pragma unroll
  for (int it = 0; it < 8; it++) dma16(x, ro + it * 64u, ldsbuf + (w * 8 + it) * 1024);
}

template <bool K, bool V, bool Q>
DI void qkv_seg(lptr xb, rsrc_t ws, u32 so, int w, int l, const bf16x8 (&Bq)[2][16], const bf16x8 (&Bk)[2][16], const bf16x8 (&Bv)[2][16]) {
  const f32x4 z4 = {0.f, 0.f, 0.f, 0.f};
  f32x4 aq[2][2], ak[2][2], av[2][2];
#pragma unroll
  for (int i = 0; i < 4; i++) { aq[i >> 1][i & 1] = z4; ak[i >> 1][i & 1] = z4; av[i >> 1][i & 1] = z4; }
  bf16x8 n0 = *(LAS bf16x8*)(xb), n1 = *(LAS bf16x8*)(xb + 16384);
#pragma unroll
  for (int ks = 0; ks < 16; ks++) {
    bf16x8 x0 = n0, x1 = n1;
    if (ks < 15) { n0 = *(LAS bf16x8*)(xb + (ks + 1) * 1024); n1 = *(LAS bf16x8*)(xb + 16384 + (ks + 1) * 1024); }
    asm volatile("" : "+v"(x0), "+v"(x1));
#pragma unroll
    for (int y = 0; y < 2; y++) {
      if (K) { ak[0][y] = mfma32(Bk[y][ks], x0, ak[0][y]); ak[1][y] = mfma32(Bk[y][ks], x1, ak[1][y]); }
      if (V) { av[0][y] = mfma32(x0, Bv[y][ks], av[0][y]); av[1][y] = mfma32(x1, Bv[y][ks], av[1][y]); }
      if (Q) { aq[0][y] = mfma32(Bq[y][ks], x0, aq[0][y]); aq[1][y] = mfma32(Bq[y][ks], x1, aq[1][y]); }
    }
    if (K) asm volatile("" : "+a"(ak[0][0]), "+a"(ak[0][1]), "+a"(ak[1][0]), "+a"(ak[1][1]));
    if (V) asm volatile("" : "+a"(av[0][0]), "+a"(av[0][1]), "+a"(av[1][0]), "+a"(av[1][1]));
    if (Q) asm volatile("" : "+a"(aq[0][0]), "+a"(aq[0][1]), "+a"(aq[1][0]), "+a"(aq[1][1]));
  }
#pragma unroll
  for (int t = 0; t < 2; t++) {
    if (K) {
      u32x4 kk = {pk(ak[t][0][0], ak[t][0][1]), pk(ak[t][0][2], ak[t][0][3]), pk(ak[t][1][0], ak[t][1][1]), pk(ak[t][1][2], ak[t][1][3])};
      st16(ws, kk, (u32)l * 16u + (u32)w * 1024u, OFF_KX + so + t * 4096u);
    }
    if (V) {
      u32x4 vv = {pk(av[0][t][0], av[0][t][1]), pk(av[0][t][2], av[0][t][3]), pk(av[1][t][0], av[1][t][1]), pk(av[1][t][2], av[1][t][3])};
      st16(ws, vv, (u32)l * 16u + (u32)(2 * w + t) * 1024u, OFF_VX + so);
    }
    if (Q) {
      u32x4 qq = {pk(aq[t][0][0], aq[t][0][1]), pk(aq[t][0][2], aq[t][0][3]), pk(aq[t][1][0], aq[t][1][1]), pk(aq[t][1][2], aq[t][1][3])};
      st16(ws, qq, (u32)l * 16u + (u32)w * 1024u, OFF_QX + so + t * 4096u);
    }
  }
}

#define TQ 30
DI u32 qkv_role(lptr smem, rsrc_t ws, const char* wsb, int h, int j, int w, int l, const char* x, const char* Wq, const char* Wk, const char* Wv) {
  const u32 lds0 = (u32)(size_t)smem;
  bf16x8 Bq[2][16], Bk[2][16], Bv[2][16];
  const u32 ho = (u32)h * 256u, hh = 256u * 8192u;
  const u32 fl = (w == 0 && l == 0) ? 0u : OOB;
  stageW(Wk, ho, 8192u, lds0, w, l);
  stageW(Wk, ho + hh, 8192u, lds0 + 65536, w, l);
  SCHED();
  const u32 tag = gettag(wsb);
  WAITVM(16); BAR(); readW<true>(Bk, 0, smem, w, l); BAR(); stageX(x, j, lds0 + 131072, w, l); stageW(Wv, ho, 8192u, lds0, w, l);
  WAITVM(24); BAR();
  readW<true>(Bk, 1, smem + 65536, w, l); BAR(); stageW(Wv, ho + hh, 8192u, lds0 + 65536, w, l);
  WAITVM(32); BAR();
  qkv_seg<true, false, false>(smem + 131072 + l * 16, ws, (u32)(j * 32 + h) * 8192u, w, l, Bq, Bk, Bv);
  WAITVM(18); BAR(); readW<false>(Bv, 0, smem, w, l);
  WAITVM(2); BAR(); readW<false>(Bv, 1, smem + 65536, w, l);
  qkv_seg<false, true, false>(smem + 131072 + l * 16, ws, (u32)(j * 32 + h) * 8192u, w, l, Bq, Bk, Bv);
  BAR();
  stageW(Wq, ho, 8192u, lds0, w, l);
  WAITVM(16); BAR();
  st4(ws, tag + 1u, fl, C_KV + (u32)(h * 64 + j) * 4u);
  stageW(Wq, ho + hh, 8192u, lds0 + 65536, w, l);
  WAITVM(16); BAR(); readW<true>(Bq, 0, smem, w, l); BAR(); stageX(x, j + 6, lds0, w, l);
  WAITVM(8); BAR(); readW<true>(Bq, 1, smem + 65536, w, l);
  qkv_seg<false, false, true>(smem + 131072 + l * 16, ws, (u32)(j * 32 + h) * 8192u, w, l, Bq, Bk, Bv);
  WAITVM(0); BAR();
  st4(ws, tag + 2u, fl, C_KV + (u32)(h * 64 + j) * 4u);
  int buf = 0, rs = j + 12, e = j;
  for (int s = j + 6; s < 64;) {
    const int own = rs < (j < 4 ? 64 : TQ) ? rs : 64;
    const int ext = (j < 4 && e < 20 - TQ / 3) ? TQ + 4 + 6 * (e >> 1) + (e & 1) : 64;
    const int sn = own <= ext ? own : ext;
    if (own <= ext) rs += 6; else e += 4;
    if (sn < 64) stageX(x, sn, lds0 + (buf ^ 1) * 32768, w, l);
    qkv_seg<true, true, true>(smem + buf * 32768 + l * 16, ws, (u32)(s * 32 + h) * 8192u, w, l, Bq, Bk, Bv);
    WAITVM(0); BAR();
    st4(ws, tag + 2u, fl, C_KV + (u32)(h * 64 + s) * 4u);
    buf ^= 1;
    s = sn;
  }
  return tag;
}

DI void ffn1_role(lptr smem, rsrc_t ws, u32 tag, int cg, int jj, int w, int l, const char* x, const char* w1, const bf16* b1, const bf16* gw) {
  const u32 lds0 = (u32)(size_t)smem;
  bf16x8 B[2][16], Gf[16];
  stageW(w1, (u32)cg * 256u, 3584u, lds0, w, l);
  stageW(w1, (u32)cg * 256u + 256u * 3584u, 3584u, lds0 + 65536, w, l);
  float bias[8];
#pragma unroll
  for (int i = 0; i < 8; i++) bias[i] = (float)b1[cg * 128 + 32 * w + 16 * (i >> 2) + 4 * (l >> 4) + (i & 3)];
#pragma unroll
  for (int ks = 0; ks < 16; ks++) Gf[ks] = *(const bf16x8*)(gw + 32 * ks + 8 * (l >> 4));
  WAITVM(0); BAR(); readW<true>(B, 0, smem, w, l); readW<true>(B, 1, smem + 65536, w, l); BAR();
  stageX(x, jj, lds0, w, l);
  WAITVM(0); BAR();
  const f32x4 z4 = {0.f, 0.f, 0.f, 0.f};
  const bool dog = (cg == 0 && w == 0);
  int buf = 0;
  for (int s = jj; s < 64; s += 9) {
    if (s + 9 < 64) stageX(x, s + 9, lds0 + (buf ^ 1) * 32768, w, l);
    lptr xb = smem + buf * 32768 + l * 16;
    f32x4 a[2][2] = {{z4, z4}, {z4, z4}};
#pragma unroll
    for (int ks = 0; ks < 16; ks++) {
      const bf16x8 x0 = *(LAS bf16x8*)(xb + ks * 1024), x1 = *(LAS bf16x8*)(xb + 16384 + ks * 1024);
#pragma unroll
      for (int y = 0; y < 2; y++) { a[0][y] = mfma32(B[y][ks], x0, a[0][y]); a[1][y] = mfma32(B[y][ks], x1, a[1][y]); }
    }
#pragma unroll
    for (int t = 0; t < 2; t++) {
      float r[8];
#pragma unroll
      for (int i = 0; i < 8; i++) {
        const float hv = rbf(rbf(a[t][i >> 2][i & 3]) + bias[i]);
        const float z = hv > 0.f ? hv : 0.f;
        r[i] = z * z;
      }
      u32x4 o = {pk(r[0], r[1]), pk(r[2], r[3]), pk(r[4], r[5]), pk(r[6], r[7])};
      st16(ws, o, (u32)l * 16u + (u32)(cg * 4 + w) * 1024u, OFF_HF + (u32)(s * 2 + t) * 57344u);
    }
    if (dog) {
      f32x4 g0 = z4, g1 = z4;
#pragma unroll
      for (int ks = 0; ks < 16; ks++) {
        g0 = mfma32(Gf[ks], *(LAS bf16x8*)(xb + ks * 1024), g0);
        g1 = mfma32(Gf[ks], *(LAS bf16x8*)(xb + 16384 + ks * 1024), g1);
      }
      const float zz = rbf(l < 16 ? g0[0] : g1[0]);
      const float gg = rbf(1.f / (1.f + __expf(-zz)));
      if (l < 32) st4(ws, __builtin_bit_cast(u32, gg), (u32)l * 4u, OFF_G + (u32)s * 128u);
    }
    WAITVM(0); BAR();
    if (w == 0 && l == 0) st4(ws, tag, 0, C_HC + (u32)s * 128u + (u32)cg * 4u);
    buf ^= 1;
  }
}

DI void attn_role(rsrc_t ws, u32 tag, int u, int l) {
  const int h = u & 31, gb = l >> 4, c = l & 15;
  bool dead = false;
  const f32x4 z4 = {0.f, 0.f, 0.f, 0.f};
  const u32x4 one8 = {0x3F803F80u, 0x3F803F80u, 0x3F803F80u, 0x3F803F80u};
  const u32x2 one4 = {0x3F803F80u, 0x3F803F80u};
  for (int s = u >> 5; s < 64; s += 4) {
    const u32 so = (u32)(s * 32 + h) * 8192u, sm = (u32)(s * 32 + h) * 4096u, lo = (u32)l * 16u;
    waitflag(ws, 0, C_KV + (u32)(h * 64 + s) * 4u, 1ull, ~0u, tag + 2u, dead, 2);
    u32x4 Q[2][4], K[2][4], V[8], KM[4];
    u32x2 VM[8];
#pragma unroll
    for (int i = 0; i < 8; i++) {
      Q[i >> 2][i & 3] = ld16(ws, lo + (i & 3) * 1024u, OFF_QX + so + (i >> 2) * 4096u);
      K[i >> 2][i & 3] = ld16(ws, lo + (i & 3) * 1024u, OFF_KX + so + (i >> 2) * 4096u);
      V[i] = ld16(ws, lo + (i & 3) * 1024u, OFF_VX + so + (i >> 2) * 4096u);
    }
    SCHED();
    f32x4 s00 = z4, s01 = z4, s11 = z4;
#pragma unroll
    for (int ks = 0; ks < 4; ks++) {
      s00 = mfma32(as8(K[0][ks]), as8(Q[0][ks]), s00);
      s01 = mfma32(as8(K[0][ks]), as8(Q[1][ks]), s01);
      s11 = mfma32(as8(K[1][ks]), as8(Q[1][ks]), s11);
    }
#pragma unroll
    for (int i = 0; i < 4; i++)
      if (4 * gb + i > c) { s00[i] = NEG; s11[i] = NEG; }
    waitflag(ws, (u32)(l & 1) * 4u, C_KM + (u32)((h * 64 + s) * 2) * 4u, 3ull, ~0u, tag, dead, s < 60 ? 2 : 0);
#pragma unroll
    for (int i = 0; i < 4; i++) KM[i] = ld16(ws, lo + i * 1024u, OFF_KM + sm);
#pragma unroll
    for (int i = 0; i < 8; i++) VM[i] = (u32x2){ld4(ws, (u32)l * 4u + i * 256u, OFF_VM + sm), 0u};
    SCHED();
    f32x4 m0 = z4, m1 = z4;
#pragma unroll
    for (int ks = 0; ks < 4; ks++) { m0 = mfma32(as8(KM[ks]), as8(Q[0][ks]), m0); m1 = mfma32(as8(KM[ks]), as8(Q[1][ks]), m1); }
#pragma unroll
    for (int t = 0; t < 2; t++) {
      const f32x4 sM = t ? m1 : m0, sa = t ? s01 : s00;
      float m = fmaxf(fmaxf(sM[0], sM[1]), fmaxf(fmaxf(sa[0], sa[1]), fmaxf(sa[2], sa[3])));
      if (t) m = fmaxf(m, fmaxf(fmaxf(s11[0], s11[1]), fmaxf(s11[2], s11[3])));
      m = maxall(m) * SCL;
      float pM[4], p0[4], p1[4];
#pragma unroll
      for (int i = 0; i < 4; i++) {
        pM[i] = i < 2 ? __builtin_amdgcn_exp2f(__builtin_fmaf(sM[i], SCL, -m)) : 0.f;
        p0[i] = __builtin_amdgcn_exp2f(__builtin_fmaf(sa[i], SCL, -m));
        p1[i] = t ? __builtin_amdgcn_exp2f(__builtin_fmaf(s11[i], SCL, -m)) : 0.f;
      }
      const u32x2 PM = {pk(pM[0], pM[1]), 0u};
      const u32x4 PX = {pk(p0[0], p0[1]), pk(p0[2], p0[3]), pk(p1[0], p1[1]), pk(p1[2], p1[3])};
      const float rs = __builtin_amdgcn_rcpf(mfma16(one4, PM, z4)[0] + mfma32(as8(one8), as8(PX), z4)[0]);
#pragma unroll
      for (int ks = 0; ks < 4; ks++) {
        f32x4 oa = mfma32(as8(V[2 * ks]), as8(PX), z4) + mfma16(VM[2 * ks], PM, z4);
        f32x4 ob = mfma32(as8(V[2 * ks + 1]), as8(PX), z4) + mfma16(VM[2 * ks + 1], PM, z4);
        u32x4 o = {pk(oa[0] * rs, oa[1] * rs), pk(oa[2] * rs, oa[3] * rs), pk(ob[0] * rs, ob[1] * rs), pk(ob[2] * rs, ob[3] * rs)};
        st16(ws, o, lo + ks * 1024u, OFF_OF + (u32)(s * 2 + t) * 131072u + (u32)h * 4096u);
      }
    }
    WAITVM(0);
    if (l == 0) st4(ws, tag, 0, C_OC + (u32)s * 128u + (u32)h * 4u);
  }
}

DI void out_role(lptr smem, rsrc_t ws, u32 tag, int grp, int xt, int sl, int w, int l, const char* Wo, const char* w2, const bf16* b2, bf16* out) {
  const u32 lds0 = (u32)(size_t)smem + (u32)w * 32768u;
  lptr wb = smem + w * 32768;
  const int gb = l >> 4, q = (l >> 2) & 3, p = l & 3, c = l & 15;
  bf16x8 A[2][32], A2[2][14];
  bool dead = false;
  {
    const u32 ro = (u32)(l >> 2) * 1024u + (u32)sl * 64u + (u32)(l & 3) * 16u;
    auto issue = [&](const char* W, int row0, int n, int buf) {
#pragma unroll
      for (int it = 0; it < 16; it++)
        if (it < n) dma16(W, ro + (u32)(row0 + it * 16) * 1024u, lds0 + buf * 16384 + it * 1024);
    };
    lptr b = wb + (4 * gb + q) * 64 + p * 16;
    issue(w2, w * 448, 16, 0);
    issue(w2, w * 448 + 256, 12, 1);
    WAITVM(12);
#pragma unroll
    for (int k = 0; k < 8; k++)
#pragma unroll
      for (int y = 0; y < 2; y++) { A2[y][k] = cat2(trr(b + k * 2048 + y * 8), trr(b + k * 2048 + 1024 + y * 8)); }
    asm volatile("s_waitcnt lgkmcnt(0)" ::: "memory");
    issue(Wo, w * 1024, 16, 0);
    WAITVM(16);
#pragma unroll
    for (int k = 0; k < 6; k++)
#pragma unroll
      for (int y = 0; y < 2; y++) { A2[y][8 + k] = cat2(trr(b + 16384 + k * 2048 + y * 8), trr(b + 16384 + k * 2048 + 1024 + y * 8)); }
#pragma unroll
    for (int r = 0; r < 4; r++) {
      asm volatile("s_waitcnt lgkmcnt(0)" ::: "memory");
      if (r < 3) { issue(Wo, w * 1024 + (r + 1) * 256, 16, (r + 1) & 1); WAITVM(16); } else { WAITVM(0); }
#pragma unroll
      for (int k = 0; k < 8; k++)
#pragma unroll
        for (int y = 0; y < 2; y++)
        { A[y][r * 8 + k] = cat2(trr(b + (r & 1) * 16384 + k * 2048 + y * 8), trr(b + (r & 1) * 16384 + k * 2048 + 1024 + y * 8)); }
    }
  }
  float bias[8];
#pragma unroll
  for (int i = 0; i < 8; i++) bias[i] = (float)b2[sl * 32 + 8 * gb + i];
#define LDO(v, s) ld16(ws, v, s)
  BAR();
  const f32x4 z4 = {0.f, 0.f, 0.f, 0.f};
  {
    int n = 0;
#pragma unroll 1
    while (!dead) {
      MEMBAR();
      u32 bad = 0;
#pragma unroll
      for (int k = 0; k < 14; k++) bad |= ld4(ws, (u32)l * 128u + k * 4u, C_HC) ^ tag;
      if (!anyl(bad != 0)) break;
      asm volatile("" : "+s"(n));
      if (++n > SPIN_MAX) dead = true;
      __builtin_amdgcn_s_sleep(16);
    }
    MEMBAR();
  }
  for (int si = grp; si < 65; si += 3) {
    const int s = si < 63 ? si : 63;
    const bool act = si < 63 || (c >> 3) == si - 63;
    const u32 lo = act ? (u32)l * 16u : OOB;
    f32x4 a0 = z4, a1 = z4, h0 = z4, h1 = z4;
    {
      const u32 so = OFF_HF + (u32)(s * 2 + xt) * 57344u + (u32)w * 14336u;
      u32x4 F[14];
#pragma unroll
      for (int k = 0; k < 14; k++) F[k] = LDO(lo, so + k * 1024u);
      SCHED();
#pragma unroll
      for (int k = 0; k < 14; k++) { h0 = mfma32(A2[0][k], as8(F[k]), h0); h1 = mfma32(A2[1][k], as8(F[k]), h1); }
    }
    const float g = __builtin_bit_cast(float, ld4(ws, (u32)c * 4u, OFF_G + (u32)(s * 2 + xt) * 64u));
    waitflag(ws, (u32)(l & 31) * 4u, C_OC + (u32)s * 128u, 0xFFFFFFFFull, ~0u, tag, dead, s < 61 ? 1 : 0);
    {
      const u32 so = OFF_OF + (u32)(s * 2 + xt) * 131072u + (u32)w * 32768u;
      u32x4 F[16];
#pragma unroll
      for (int k = 0; k < 16; k++) F[k] = LDO(lo, so + k * 1024u);
      SCHED();
#pragma unroll
      for (int k = 0; k < 32; k++) {
        a0 = mfma32(A[0][k], as8(F[k & 15]), a0); a1 = mfma32(A[1][k], as8(F[k & 15]), a1);
        if (k < 16) {
          asm volatile("" : "+a"(a0), "+a"(a1));
          F[k] = LDO(lo, so + 16384u + k * 1024u);
          SCHED();
        }
      }
    }
    lptr rb = smem + w * 4096 + l * 16;
    *(LAS f32x4*)(rb) = a0; *(LAS f32x4*)(rb + 1024) = a1; *(LAS f32x4*)(rb + 2048) = h0; *(LAS f32x4*)(rb + 3072) = h1;
    BAR();
    if (w == 0) {
#pragma unroll
      for (int k = 1; k < 4; k++) {
        a0 += *(LAS f32x4*)(rb + k * 4096); a1 += *(LAS f32x4*)(rb + k * 4096 + 1024);
        h0 += *(LAS f32x4*)(rb + k * 4096 + 2048); h1 += *(LAS f32x4*)(rb + k * 4096 + 3072);
      }
      const float g1 = rbf(1.f - g);
      float r[8];
#pragma unroll
      for (int i = 0; i < 8; i++) {
        const float av = rbf(i < 4 ? a0[i & 3] : a1[i & 3]);
        const float bv = rbf(rbf(i < 4 ? h0[i & 3] : h1[i & 3]) + bias[i]);
        r[i] = rbf(g * av) + rbf(g1 * bv);
      }
      u32x4 o = {pk(r[0], r[1]), pk(r[2], r[3]), pk(r[4], r[5]), pk(r[6], r[7])};
      if (act) *(u32x4*)(out + (size_t)(32 * s + 16 * xt + c) * 512 + sl * 32 + 8 * gb) = o;
    }
    BAR();
  }
}

DI void probe_role(lptr smem, rsrc_t ws, u32 tag, int w, int l) {
  const u32 base = ((w & 1) ? OFF_MEM : OFF_PART) + (u32)(w >> 1) * 131072u;
  u32 best[2] = {0xFFFFFFFFu, 0xFFFFFFFFu};
#pragma unroll 1
  for (int i = 0; i < 8; i++) {
#pragma unroll
    for (int sd = 0; sd < 2; sd++) {
      u64 t0 = __builtin_amdgcn_s_memtime();
      asm volatile("s_waitcnt lgkmcnt(0)" ::: "memory");
      st4(ws, 0xFFFFFFFFu, l == 0 ? 0u : OOB, base + (u32)i * 16384u + (u32)sd * 8192u);
      WAITVM(0);
      u64 t1 = __builtin_amdgcn_s_memtime();
      best[sd] = min(best[sd], (u32)(t1 - t0));
    }
  }
  if (l == 0) { *(LAS u32*)(smem + w * 8) = best[0]; *(LAS u32*)(smem + w * 8 + 4) = best[1]; }
  BAR();
  if (w == 0 && l == 0) {
    LAS u32* t = (LAS u32*)smem;
    const u32 a = min(t[0], t[4]), b = min(t[1], t[5]), c = min(t[2], t[6]), d = min(t[3], t[7]);
    st4(ws, tag | (b < a ? 1u : 0u) | (d < c ? 2u : 0u), 0, C_SEL);
  }
  BAR();
}

__global__ __launch_bounds__(256, 1) void fused_kernel(const char* x, const bf16* mem0, const char* Wq, const char* Wk, const char* Wv,
                                                      const char* Wo, const char* w1, const bf16* b1, const char* w2, const bf16* b2,
                                                      const bf16* gw, bf16* out, char* wsb) {
  extern __shared__ __attribute__((aligned(16))) char smem_[];
  lptr smem = (lptr)smem_;
  const int b = blockIdx.x, xq = b & 7, r = b >> 3;
  const int w = __builtin_amdgcn_readfirstlane(threadIdx.x >> 6), l = threadIdx.x & 63;
  const rsrc_t ws = __builtin_amdgcn_make_buffer_rsrc(wsb, 0, (int)WS_BYTES, 0x00020000);
  if (xq < 4 && r < 16) {
    chain_role(smem, ws, 8 * xq + (r >> 1), r & 1, w, l, Wq, Wk, Wv, Wo, mem0, wsb);
    return;
  }
  const int head = xq < 4 ? 8 * xq + ((r - 16) >> 1) : 8 * (xq - 4) + (r >> 2);
  const int j = xq < 4 ? 4 + (r & 1) : (r & 3);
  if (b == 31 * 8) probe_role(smem, ws, gettag(wsb), w, l);
  const u32 tag = qkv_role(smem, ws, wsb, head, j, w, l, x, Wq, Wk, Wv);
  const int wi = (xq - 4) * 32 + r;
  if (xq >= 4 && wi < 126) ffn1_role(smem, ws, tag, wi / 9, wi % 9, w, l, x, w1, b1, gw);
  if (xq >= 4) {
    if (r >= 24) {
      attn_role(ws, tag, ((xq - 4) * 8 + (r - 24)) * 4 + w, l);
    } else {
      const int g2 = (xq - 4) >> 1;
      out_role(smem, ws, tag, r < 16 ? g2 : 2, xq & 1, r < 16 ? r : r - 16 + 8 * g2, w, l, Wo, w2, b2, out);
    }
  }
}

extern "C" void kernel_launch(void* const* d_in, const int* in_sizes, int n_in, void* d_out, int out_size, void* d_ws,
                              size_t ws_size, hipStream_t stream) {
  char* base = (char*)(((size_t)d_ws + 2 * MB - 1) & ~(size_t)(2 * MB - 1));
  (void)hipFuncSetAttribute((const void*)fused_kernel, hipFuncAttributeMaxDynamicSharedMemorySize, LDS_BYTES);
  fused_kernel<<<256, 256, LDS_BYTES, stream>>>((const char*)d_in[0], (const bf16*)d_in[1], (const char*)d_in[2], (const char*)d_in[3],
                                                (const char*)d_in[4], (const char*)d_in[5], (const char*)d_in[8], (const bf16*)d_in[9],
                                                (const char*)d_in[10], (const bf16*)d_in[11], (const bf16*)d_in[12], (bf16*)d_out, base);
}
```
